```python
import torch

def _softmax(x, axis=-1):
    return torch.softmax(x, dim=axis)
def _gelu(x):
    return torch.nn.functional.gelu(x, approximate="tanh")
def _silu(x):
    return torch.nn.functional.silu(x)
def _sigmoid(x):
    return torch.sigmoid(x)
def _layernorm(x, axis=-1, eps=1e-5):
    m = x.mean(dim=axis, keepdim=True)
    v = x.var(dim=axis, keepdim=True, unbiased=False)
    return (x - m) / torch.sqrt(v + eps)
def _scatter_add(tgt, idx, src, axis=0):
    return tgt.clone().index_add_(axis, idx, src)
def _segment_sum(src, seg, n):
    out = torch.zeros((n,) + src.shape[1:], dtype=src.dtype)
    return out.index_add_(0, seg, src)
def _pad(x, pw):
    flat = tuple(v for pair in reversed(pw) for v in pair)
    return torch.nn.functional.pad(x, flat)

kernel_name = 'recurrent_scan__softmax_full_mha__glu_gated_geglu__prompt'

def setup_inputs():

    _w = (lambda a: (lambda t: t.to(torch.bfloat16) if t.is_floating_point() else t)(torch.as_tensor(a)))
    def _A_setup():
        B, S, D = 1, 2048, 512
        return {
            "x": _w((lambda *s: torch.randn(*s, dtype=torch.bfloat16) * 0.05)(B, S, D)),
            "wqkv": _w((lambda *s: torch.randn(*s, dtype=torch.bfloat16) * 0.2)(D, 3 * D)),
            "wo": _w((lambda *s: torch.randn(*s, dtype=torch.bfloat16) * 0.2)(D, D)),
        }
    _A_in = _A_setup()

    def _B_setup():
        B, S, Dm, Df = 1, 2048, 512, 1792
        return {
            "x": _w((lambda *s: torch.randn(*s, dtype=torch.bfloat16) * 0.05)(B, S, Dm)),
            "w_gate": _w((lambda *s: torch.randn(*s, dtype=torch.bfloat16) * 0.1)(Dm, Df) * (Dm ** -0.5)),
            "w_up": _w((lambda *s: torch.randn(*s, dtype=torch.bfloat16) * 0.1)(Dm, Df) * (Dm ** -0.5)),
            "w_down": _w((lambda *s: torch.randn(*s, dtype=torch.bfloat16) * 0.1)(Df, Dm) * (Df ** -0.5)),
        }
    _B_in = _B_setup()

    return {
        "A_x": _A_in["x"],
        "A_wqkv": _A_in["wqkv"],
        "A_wo": _A_in["wo"],
        "B_x": _B_in["x"],
        "B_w_gate": _B_in["w_gate"],
        "B_w_up": _B_in["w_up"],
        "B_w_down": _B_in["w_down"],
    }

def reference(A_x, A_wqkv, A_wo, B_x, B_w_gate, B_w_up, B_w_down):
    def _A_ref(x, wqkv, wo):
        B, S, D = x.shape
        H = 32; Dh = D // H
        qkv = (lambda x, *d: x.permute(*d))((x @ wqkv).reshape(B, S, 3, H, Dh), 2, 0, 3, 1, 4)
        q, k, v = qkv[0], qkv[1], qkv[2]
        scale = Dh ** -0.5
        scores = torch.einsum("bhqd,bhkd->bhqk", q, k) * scale
        p = _softmax(scores, axis=-1)
        o = torch.einsum("bhqk,bhkd->bhqd", p, v)
        return (lambda x, *d: x.permute(*d))(o, 0, 2, 1, 3).reshape(B, S, D) @ wo

    def _B_ref(x, w_gate, w_up, w_down):
        g = torch.einsum("bsd,df->bsf", x, w_gate)
        u = torch.einsum("bsd,df->bsf", x, w_up)
        h = _gelu(g) * u
        return torch.einsum("bsf,fd->bsd", h, w_down)

    x = A_x
    xn = _layernorm(x, axis=-1)
    s = _A_ref(xn, A_wqkv, A_wo)
    g = _silu(_B_ref(xn, B_w_gate, B_w_up, B_w_down))
    return x + s * g
```

```cpp
#include <hip/hip_runtime.h>
#include <hip/hip_bf16.h>
#include <stdint.h>

typedef __attribute__((ext_vector_type(8))) short bf16x8;
typedef __attribute__((ext_vector_type(4))) short s16x4;
typedef __attribute__((ext_vector_type(16))) float f32x16;
typedef __attribute__((ext_vector_type(4))) float f32x4;
typedef __attribute__((ext_vector_type(2))) float f32x2;
typedef __attribute__((ext_vector_type(4))) unsigned u32x4;
typedef __attribute__((ext_vector_type(2))) unsigned u32x2;
typedef __attribute__((ext_vector_type(2))) __bf16 bf16x2_t;
typedef short v4i16_t __attribute__((ext_vector_type(4)));
typedef unsigned short u16;
#define LDSP(T) __attribute__((address_space(3))) T
typedef LDSP(const char)* lds_cptr;
typedef LDSP(char)* lds_ptr;

#define DI __device__ __forceinline__
#define MFMA(a, b, c) __builtin_amdgcn_mfma_f32_32x32x16_bf16((a), (b), (c), 0, 0, 0)

constexpr int S = 2048, D = 512, NH = 32, DH = 16, FF = 1792;
constexpr int MTILES = S / 32;
constexpr int KS_D = D / 16;
constexpr int KS_F = FF / 16;
constexpr size_t WS_XN = 0;
constexpr size_t WS_Q = WS_XN + 2 * 1024 * 1024;
constexpr size_t WS_K = WS_Q + 2 * 1024 * 1024;
constexpr size_t WS_V = WS_K + 2 * 1024 * 1024;
constexpr size_t WS_O = WS_V + 2 * 1024 * 1024;
constexpr size_t WS_G = WS_O + 2 * 1024 * 1024;
constexpr size_t WS_H = WS_G + 2 * 1024 * 1024;

DI unsigned cvtpk(float lo, float hi) {
  f32x2 v = {lo, hi};
  bf16x2_t b = __builtin_convertvector(v, bf16x2_t);
  return __builtin_bit_cast(unsigned, b);
}
DI float bf_lo(unsigned u) { return __uint_as_float(u << 16); }
DI float bf_hi(unsigned u) { return __uint_as_float(u & 0xffff0000u); }
DI unsigned rfl(unsigned x) { return __builtin_amdgcn_readfirstlane(x); }
DI __amdgpu_buffer_rsrc_t mk_rsrc(const void* p, unsigned bytes) {
  return __builtin_amdgcn_make_buffer_rsrc((void*)p, (short)0, (int)bytes, 0x00020000);
}
DI void dma16(__amdgpu_buffer_rsrc_t rsrc, unsigned voff, unsigned soff, unsigned lds_addr) {
  asm volatile("s_nop 4\n\ts_mov_b32 m0, %0\n\ts_nop 0\n\tbuffer_load_dwordx4 %1, %2, %3 offen lds"
               :: "s"(lds_addr), "v"(voff), "s"(rsrc), "s"(soff) : "memory");
}
#define WAIT_BAR(N) asm volatile("s_waitcnt vmcnt(" #N ") lgkmcnt(0)\n\ts_barrier" ::: "memory")
DI bf16x8 lds_rd128(lds_cptr p) { return *(LDSP(const bf16x8)*)p; }
DI s16x4 lds_tr(lds_cptr p) {
  return __builtin_bit_cast(s16x4, __builtin_amdgcn_ds_read_tr16_b64_v4i16((LDSP(v4i16_t)*)p));
}
DI bf16x8 cat8(s16x4 a, s16x4 b) { return __builtin_shufflevector(a, b, 0, 1, 2, 3, 4, 5, 6, 7); }

__global__ __launch_bounds__(256) void k_ln(const u16* __restrict__ x, char* __restrict__ ws) {
  const int lane = threadIdx.x & 63, w = threadIdx.x >> 6;
  const int row = (blockIdx.x * 4 + w) * 8 + (lane >> 3), c = lane & 7;
  const u32x4* src = (const u32x4*)(x + (size_t)row * D);
  u32x4 v[8];
#pragma unroll
  for (int i = 0; i < 8; ++i) v[i] = src[i * 8 + c];
  float f[64];
  float s = 0.f;
#pragma unroll
  for (int i = 0; i < 8; ++i)
#pragma unroll
    for (int j = 0; j < 4; ++j) {
      f[i * 8 + 2 * j] = bf_lo(v[i][j]);
      f[i * 8 + 2 * j + 1] = bf_hi(v[i][j]);
      s += f[i * 8 + 2 * j] + f[i * 8 + 2 * j + 1];
    }
  s += __shfl_xor(s, 1); s += __shfl_xor(s, 2); s += __shfl_xor(s, 4);
  const float mean = s * (1.f / D);
  float q = 0.f;
#pragma unroll
  for (int i = 0; i < 64; ++i) { f[i] -= mean; q += f[i] * f[i]; }
  q += __shfl_xor(q, 1); q += __shfl_xor(q, 2); q += __shfl_xor(q, 4);
  const float rstd = 1.f / sqrtf(q * (1.f / D) + 1e-5f);
  char* xn = ws + WS_XN;
  const int mt = row >> 5, r = row & 31;
#pragma unroll
  for (int i = 0; i < 8; ++i) {
    u32x4 o;
#pragma unroll
    for (int j = 0; j < 4; ++j) o[j] = cvtpk(f[i * 8 + 2 * j] * rstd, f[i * 8 + 2 * j + 1] * rstd);
    const int s16 = 4 * i + (c >> 1), h = c & 1;
    *(u32x4*)(xn + ((size_t)(mt * KS_D + s16) * 64 + h * 32 + r) * 16) = o;
  }
}

template <int MT, int NT, int KSPLIT, int KSTEPS, int LDW, bool PERM, int EPI, int NSTG>
__global__ __launch_bounds__(512, 2) void k_gemm(const char* __restrict__ tok, const u16* __restrict__ W,
                                                 char* __restrict__ ws, const u16* __restrict__ x,
                                                 u16* __restrict__ out) {
  constexpr int STG_BYTES = (MT + NT) * 4096;
  constexpr int NINS = (MT + NT) * 4;
  constexpr int IPW = (NINS + 7) / 8;
  constexpr int NST = KSTEPS / 4;
  constexpr int RED_BYTES = (KSPLIT - 1) * MT * NT * 4096;
  constexpr int LDS_BYTES = (NSTG * STG_BYTES > RED_BYTES) ? NSTG * STG_BYTES : RED_BYTES;
  __shared__ __attribute__((aligned(16))) char lds_[LDS_BYTES];
  const int tid = threadIdx.x, lane = tid & 63;
  const int w = rfl(tid >> 6);
  const int mt = w % MT, kh = w / MT;
  constexpr int NBLK_N = (EPI == 0 ? 1536 : 512) / (NT * 32);
  const int nb = blockIdx.x % NBLK_N, mb = blockIdx.x / NBLK_N;
  const unsigned lds0 = (unsigned)(uintptr_t)lds_;
  const __amdgpu_buffer_rsrc_t rt = mk_rsrc(tok, MTILES * KSTEPS * 1024);
  const __amdgpu_buffer_rsrc_t rw = mk_rsrc(W, KSTEPS * 16 * LDW * 2);
  const unsigned vt = lane * 16;
  const unsigned vw = (lane >> 2) * (LDW * 2) + (lane & 3) * 16;

  auto issue = [&](int st) {
    const unsigned slot = lds0 + (st % NSTG) * STG_BYTES;
#pragma unroll
    for (int j = 0; j < IPW; ++j) {
      int i = w + 8 * j;
      if (i > NINS - 1) i = NINS - 1;
      if (i < MT * 4) {
        const int m = i >> 2, ks = i & 3;
        dma16(rt, vt, (unsigned)(((mb * MT + m) * KSTEPS + st * 4 + ks) * 1024), slot + i * 1024);
      } else {
        const int ii = i - MT * 4, n = ii >> 2, ks = ii & 3;
        dma16(rw, vw, (unsigned)((st * 64 + ks * 16) * (LDW * 2) + (nb * NT + n) * 64), slot + i * 1024);
      }
    }
  };
  f32x16 acc[NT];
#pragma unroll
  for (int n = 0; n < NT; ++n)
#pragma unroll
    for (int r = 0; r < 16; ++r) acc[n][r] = 0.f;
  const int h = lane >> 5, i16 = lane & 15, q4 = i16 >> 2, p4 = i16 & 3, blk = (lane >> 4) & 1;
  const unsigned wbase = ((PERM ? 4 * h : 8 * h) + q4) * 64 + blk * 32 + p4 * 8;
  constexpr unsigned TOFF = PERM ? 512 : 256;
  lds_cptr L = (lds_cptr)lds_;

#pragma unroll
  for (int s = 0; s < NSTG - 1; ++s)
    if (s < NST) issue(s);
  for (int st = 0; st < NST; ++st) {
    if (st + NSTG - 2 <= NST - 1) {
      if constexpr (NSTG == 3) { if constexpr (IPW == 4) WAIT_BAR(4); else if constexpr (IPW == 2) WAIT_BAR(2); else WAIT_BAR(0); }
      else WAIT_BAR(0);
    } else {
      WAIT_BAR(0);
    }
    if (st + NSTG - 1 < NST) issue(st + NSTG - 1);
    lds_cptr sl = L + (st % NSTG) * STG_BYTES;
#pragma unroll
    for (int ks0 = 0; ks0 < 4; ++ks0) {
      if ((ks0 % KSPLIT) != kh) continue;
      const bf16x8 tf = lds_rd128(sl + (mt * 4 + ks0) * 1024 + lane * 16);
#pragma unroll
      for (int n = 0; n < NT; ++n) {
        lds_cptr wp = sl + (MT * 4 + n * 4 + ks0) * 1024 + wbase;
        const bf16x8 wf = cat8(lds_tr(wp), lds_tr(wp + TOFF));
        bool isV = false;
        if (EPI == 0) isV = (nb * NT + n) >= 32;
        if (isV) acc[n] = MFMA(tf, wf, acc[n]);
        else acc[n] = MFMA(wf, tf, acc[n]);
      }
    }
  }
  __syncthreads();
  if (kh > 0) {
#pragma unroll
    for (int n = 0; n < NT; ++n)
#pragma unroll
      for (int g = 0; g < 4; ++g) {
        f32x4 v = {acc[n][4 * g], acc[n][4 * g + 1], acc[n][4 * g + 2], acc[n][4 * g + 3]};
        *(f32x4*)(lds_ + ((((kh - 1) * MT + mt) * NT + n) * 4 + g) * 1024 + lane * 16) = v;
      }
  }
  __syncthreads();
  if (kh > 0) return;
#pragma unroll
  for (int k2 = 0; k2 < KSPLIT - 1; ++k2)
#pragma unroll
    for (int n = 0; n < NT; ++n)
#pragma unroll
      for (int g = 0; g < 4; ++g) {
        f32x4 v = *(const f32x4*)(lds_ + (((k2 * MT + mt) * NT + n) * 4 + g) * 1024 + lane * 16);
#pragma unroll
        for (int e = 0; e < 4; ++e) acc[n][4 * g + e] += v[e];
      }
  const int mtg = mb * MT + mt;
#pragma unroll
  for (int n = 0; n < NT; ++n) {
    const int ct = nb * NT + n;
    if (EPI == 0) {
      const int which = ct >> 4, hp = (ct & 15) * 2;
      if (which < 2) {
        const float sc = (which == 0) ? 0.25f * 1.4426950408889634f : 1.f;
        char* dst = ws + (which == 0 ? WS_Q : WS_K);
#pragma unroll
        for (int hh = 0; hh < 2; ++hh) {
          u32x4 o;
#pragma unroll
          for (int j = 0; j < 4; ++j) o[j] = cvtpk(acc[n][8 * hh + 2 * j] * sc, acc[n][8 * hh + 2 * j + 1] * sc);
          *(u32x4*)(dst + ((size_t)((hp + hh) * 64 + mtg) * 64 + lane) * 16) = o;
        }
      } else {
        char* dst = ws + WS_V;
        const int hd = hp + blk, d = lane & 15;
#pragma unroll
        for (int s2 = 0; s2 < 2; ++s2) {
          u32x4 o;
#pragma unroll
          for (int j = 0; j < 4; ++j) o[j] = cvtpk(acc[n][8 * s2 + 2 * j], acc[n][8 * s2 + 2 * j + 1]);
          *(u32x4*)(dst + ((((size_t)(hd * 64 + mtg) * 2 + s2) * 2 + h) * 16 + d) * 16) = o;
        }
      }
    } else if (EPI == 1) {
      u32x4 o[2];
#pragma unroll
      for (int j = 0; j < 8; ++j) {
        float a = acc[n][2 * j], b = acc[n][2 * j + 1];
        a = a / (1.f + __expf(-a));
        b = b / (1.f + __expf(-b));
        o[j >> 2][j & 3] = cvtpk(a, b);
      }
      u32x4* dst = (u32x4*)(ws + WS_G + ((size_t)(mtg * 16 + ct) * 64 + lane) * 32);
      dst[0] = o[0]; dst[1] = o[1];
    } else {
      const u32x4* gp = (const u32x4*)(ws + WS_G + ((size_t)(mtg * 16 + ct) * 64 + lane) * 32);
      const u32x4 g0 = gp[0], g1 = gp[1];
      const int token = mtg * 32 + (lane & 31);
#pragma unroll
      for (int g = 0; g < 4; ++g) {
        const int col = ct * 32 + 8 * g + 4 * h;
        const u32x2 xv = *(const u32x2*)(x + (size_t)token * D + col);
        const unsigned ga = (g < 2 ? g0 : g1)[(g & 1) * 2], gb = (g < 2 ? g0 : g1)[(g & 1) * 2 + 1];
        u32x2 o;
        o[0] = cvtpk(bf_lo(xv[0]) + acc[n][4 * g] * bf_lo(ga), bf_hi(xv[0]) + acc[n][4 * g + 1] * bf_hi(ga));
        o[1] = cvtpk(bf_lo(xv[1]) + acc[n][4 * g + 2] * bf_lo(gb), bf_hi(xv[1]) + acc[n][4 * g + 3] * bf_hi(gb));
        *(u32x2*)(out + (size_t)token * D + col) = o;
      }
    }
  }
}

__global__ __launch_bounds__(512, 4) void k_gateup(const u16* __restrict__ Wg, const u16* __restrict__ Wu,
                                                   char* __restrict__ ws) {
  constexpr int STG = 24 * 1024;
  __shared__ __attribute__((aligned(16))) char lds_[3 * STG];
  const int tid = threadIdx.x, lane = tid & 63;
  const int w = rfl(tid >> 6);
  const int wm = w & 3, wn = w >> 2;
  const int nb = blockIdx.x & 15, mb = blockIdx.x >> 4;
  const unsigned lds0 = (unsigned)(uintptr_t)lds_;
  const __amdgpu_buffer_rsrc_t rt = mk_rsrc(ws + WS_XN, MTILES * KS_D * 1024);
  const __amdgpu_buffer_rsrc_t rg = mk_rsrc(Wg, D * FF * 2);
  const __amdgpu_buffer_rsrc_t ru = mk_rsrc(Wu, D * FF * 2);
  const unsigned vt = lane * 16;
  const int drow = lane >> 4, pos = (lane >> 1) & 7, half = lane & 1;
  int ug = pos ^ (2 * (drow & 3)), uu = pos ^ (2 * (drow & 3) + 1);
  if (ug > 6) ug = 6;
  if (uu > 6) uu = 6;
  const unsigned vg = drow * (FF * 2) + ug * 32 + half * 16;
  const unsigned vu = drow * (FF * 2) + uu * 32 + half * 16;
  auto issue = [&](int st) {
    const unsigned slot = lds0 + (st % 3) * STG;
    dma16(rt, vt, (unsigned)(((mb * 4 + (w >> 1)) * KS_D + st * 2 + (w & 1)) * 1024), slot + w * 1024);
    const unsigned so = (unsigned)((st * 32 + w * 4) * (FF * 2) + nb * 224);
    dma16(rg, vg, so, slot + 8192 + w * 1024);
    dma16(ru, vu, so, slot + 16384 + w * 1024);
  };
  f32x16 acc[4];
#pragma unroll
  for (int n = 0; n < 4; ++n)
#pragma unroll
    for (int r = 0; r < 16; ++r) acc[n][r] = 0.f;
  const int h = lane >> 5, i16 = lane & 15, q4 = i16 >> 2, p4 = i16 & 3, blk = (lane >> 4) & 1;
  lds_cptr L = (lds_cptr)lds_;
  unsigned wa[4];
#pragma unroll
  for (int n = 0; n < 4; ++n) {
    const int nn = wn * 4 + n;
    wa[n] = 8192 * (1 + blk) + (8 * h + q4) * 256 + ((nn ^ (2 * q4 + blk)) & 7) * 32 + p4 * 8;
  }
  issue(0); issue(1);
  for (int st = 0; st < 16; ++st) {
    if (st < 15) WAIT_BAR(3); else WAIT_BAR(0);
    if (st + 2 < 16) issue(st + 2);
    lds_cptr sl = L + (st % 3) * STG;
#pragma unroll
    for (int ks = 0; ks < 2; ++ks) {
      const bf16x8 tf = lds_rd128(sl + (wm * 2 + ks) * 1024 + lane * 16);
#pragma unroll
      for (int n = 0; n < 4; ++n) {
        if (n == 3 && wn == 1) continue;
        lds_cptr wp = sl + wa[n] + ks * 4096;
        const bf16x8 wf = cat8(lds_tr(wp), lds_tr(wp + 1024));
        acc[n] = MFMA(wf, tf, acc[n]);
      }
    }
  }
  const int mtg = mb * 4 + wm;
#pragma unroll
  for (int n = 0; n < 4; ++n) {
    if (n == 3 && wn == 1) continue;
    float hv[8];
#pragma unroll
    for (int j = 0; j < 8; ++j) {
      const float g = acc[n][j], u = acc[n][8 + j];
      const float z = g * (-2.3022081985f + -0.1029432397f * g * g);
      const float e = __builtin_amdgcn_exp2f(z);
      hv[j] = g * __builtin_amdgcn_rcpf(1.f + e) * u;
    }
    u32x4 o;
#pragma unroll
    for (int j = 0; j < 4; ++j) o[j] = cvtpk(hv[2 * j], hv[2 * j + 1]);
    const int ksg = nb * 7 + wn * 4 + n;
    *(u32x4*)(ws + WS_H + ((size_t)(mtg * KS_F + ksg) * 64 + lane) * 16) = o;
  }
}

constexpr float ATT_BIAS = 40.f;
constexpr float ATT_LIM = 1.2676506e30f;
__global__ __launch_bounds__(512, 2) void k_attn(char* __restrict__ ws) {
  constexpr int SLOT = 16384, ONES = 4 * SLOT;
  __shared__ __attribute__((aligned(16))) char lds_[ONES + 8192];
  const int tid = threadIdx.x, lane = tid & 63;
  const int w = rfl(tid >> 6);
  const int head = (blockIdx.x & 7) * 4 + ((blockIdx.x >> 3) & 3), qb = blockIdx.x >> 5;
  const unsigned lds0 = (unsigned)(uintptr_t)lds_;
  const __amdgpu_buffer_rsrc_t rk = mk_rsrc(ws + WS_K + (size_t)head * 65536, 65536);
  const __amdgpu_buffer_rsrc_t rv = mk_rsrc(ws + WS_V + (size_t)head * 65536, 65536);
  const unsigned vl = lane * 16;
  auto issue = [&](int c) {
    const unsigned slot = lds0 + (c & 3) * SLOT;
    dma16(rk, vl, (unsigned)(c * 8192 + w * 1024), slot + w * 1024);
    dma16(rv, vl, (unsigned)(c * 8192 + w * 1024), slot + 8192 + w * 1024);
  };
  issue(0); issue(1); issue(2);
  { u32x4 one = {0x3f803f80u, 0x3f803f80u, 0x3f803f80u, 0x3f803f80u};
    *(u32x4*)(lds_ + ONES + tid * 16) = one; }
  const bf16x8 qf = *(const bf16x8*)(ws + WS_Q + ((size_t)(head * 64 + qb * 8 + w) * 64 + lane) * 16);
  const int r32 = lane & 31, h = lane >> 5;
  const bool isdata = r32 < 16;
  const unsigned vdata = 8192 + h * 256 + r32 * 16;
  const unsigned vconst = ONES + ((r32 >= 20 && r32 < 28) ? 64u : 0u);
  lds_cptr L = (lds_cptr)lds_;
  f32x16 O, C;
#pragma unroll
  for (int r = 0; r < 16; ++r) { O[r] = 0.f; C[r] = 0.f; }
  float mref;
  WAIT_BAR(4);
  {
    const bf16x8 k0 = lds_rd128(L + lane * 16), k1 = lds_rd128(L + 1024 + lane * 16);
    const f32x16 s0 = MFMA(k0, qf, C), s1 = MFMA(k1, qf, C);
    float mx = fmaxf(s0[0], s1[0]);
#pragma unroll
    for (int r = 1; r < 16; ++r) mx = fmaxf(mx, fmaxf(s0[r], s1[r]));
    mx = fmaxf(mx, __shfl_xor(mx, 32));
    mref = mx + ATT_BIAS;
#pragma unroll
    for (int r = 0; r < 16; ++r) C[r] = -mref;
  }
  for (int c = 0; c < 8; ++c) {
    if (c > 0) { if (c < 6) WAIT_BAR(4); else if (c == 6) WAIT_BAR(2); else WAIT_BAR(0); }
    if (c + 3 < 8) issue(c + 3);
    lds_cptr kp = L + (c & 3) * SLOT + lane * 16;
    lds_cptr vp = L + (isdata ? (c & 3) * SLOT + vdata : vconst);
    float ck[9];
#pragma unroll
    for (int r = 0; r < 9; ++r) ck[r] = O[r];
    bool retry = false;
    while (true) {
#pragma unroll
      for (int t = 0; t < 8; ++t) {
        const bf16x8 kf = lds_rd128(kp + t * 1024);
        const f32x16 s = MFMA(kf, qf, C);
        u32x4 p0, p1;
#pragma unroll
        for (int j = 0; j < 4; ++j) {
          p0[j] = cvtpk(__builtin_amdgcn_exp2f(s[2 * j]), __builtin_amdgcn_exp2f(s[2 * j + 1]));
          p1[j] = cvtpk(__builtin_amdgcn_exp2f(s[8 + 2 * j]), __builtin_amdgcn_exp2f(s[8 + 2 * j + 1]));
        }
        const bf16x8 v0 = lds_rd128(vp + t * 1024), v1 = lds_rd128(vp + t * 1024 + 512);
        O = MFMA(v0, __builtin_bit_cast(bf16x8, p0), O);
        O = MFMA(v1, __builtin_bit_cast(bf16x8, p1), O);
      }
      float z = O[8];
#pragma unroll
      for (int r = 0; r < 8; ++r) z = __builtin_fmaf(O[r], 0.f, z);
      const bool bad = !(z < ATT_LIM);
      if (__builtin_expect(!__any(bad), 1) || retry) break;
      retry = true;
      f32x16 Z;
#pragma unroll
      for (int r = 0; r < 16; ++r) Z[r] = 0.f;
      float mx = -INFINITY;
      for (int t = 0; t < 8; ++t) {
        const bf16x8 kf = lds_rd128(kp + t * 1024);
        const f32x16 s = MFMA(kf, qf, Z);
#pragma unroll
        for (int r = 0; r < 16; ++r) mx = fmaxf(mx, s[r]);
      }
      mx = fmaxf(mx, __shfl_xor(mx, 32));
      const float mnew = fmaxf(mref, mx + ATT_BIAS);
      const float sc = __builtin_amdgcn_exp2f(mref - mnew);
      mref = mnew;
#pragma unroll
      for (int r = 0; r < 16; ++r) { C[r] = -mref; O[r] = 0.f; }
#pragma unroll
      for (int r = 0; r < 9; ++r) O[r] = ck[r] * sc;
    }
  }
  const float rl = __builtin_amdgcn_rcpf(O[8]);
  u32x4 o;
#pragma unroll
  for (int j = 0; j < 4; ++j) o[j] = cvtpk(O[2 * j] * rl, O[2 * j + 1] * rl);
  *(u32x4*)(ws + WS_O + ((size_t)((qb * 8 + w) * 32 + head) * 64 + lane) * 16) = o;
}

extern "C" void kernel_launch(void* const* d_in, const int* in_sizes, int n_in, void* d_out, int out_size,
                              void* d_ws, size_t ws_size, hipStream_t stream) {
  const u16* x = (const u16*)d_in[0];
  const u16* wqkv = (const u16*)d_in[1];
  const u16* wo = (const u16*)d_in[2];
  const u16* wg = (const u16*)d_in[4];
  const u16* wu = (const u16*)d_in[5];
  const u16* wd = (const u16*)d_in[6];
  char* ws = (char*)d_ws;
  u16* out = (u16*)d_out;
  k_ln<<<64, 256, 0, stream>>>(x, ws);
  k_gemm<4, 3, 2, KS_D, 1536, false, 0, 3><<<256, 512, 0, stream>>>(ws + WS_XN, wqkv, ws, x, out);
  k_attn<<<256, 512, 0, stream>>>(ws);
  k_gateup<<<256, 512, 0, stream>>>(wg, wu, ws);
  k_gemm<2, 2, 4, KS_F, 512, true, 1, 3><<<256, 512, 0, stream>>>(ws + WS_H, wd, ws, x, out);
  k_gemm<2, 2, 4, KS_D, 512, true, 2, 3><<<256, 512, 0, stream>>>(ws + WS_O, wo, ws, x, out);
}
```

```cpp
#include <hip/hip_runtime.h>
#include <hip/hip_bf16.h>
#include <stdint.h>

typedef __attribute__((ext_vector_type(8))) short bf16x8;
typedef __attribute__((ext_vector_type(4))) short s16x4;
typedef __attribute__((ext_vector_type(16))) float f32x16;
typedef __attribute__((ext_vector_type(4))) float f32x4;
typedef __attribute__((ext_vector_type(2))) float f32x2;
typedef __attribute__((ext_vector_type(4))) unsigned u32x4;
typedef __attribute__((ext_vector_type(2))) unsigned u32x2;
typedef __attribute__((ext_vector_type(2))) __bf16 bf16x2_t;
typedef short v4i16_t __attribute__((ext_vector_type(4)));
typedef unsigned short u16;
#define LDSP(T) __attribute__((address_space(3))) T
typedef LDSP(const char)* lds_cptr;
typedef __attribute__((address_space(1))) unsigned gu32;

#define DI __device__ __forceinline__
#define SBAR() __builtin_amdgcn_sched_barrier(0)
#define MLP_PRIO 3
#define TOUCH 1
#define SLP1 0
#define SLP2 0
#define PACE_G1() do { if (SLP1 > 0) { SBAR(); __builtin_amdgcn_s_sleep(SLP1); SBAR(); } } while (0)
#define PACE_G2() do { if (SLP2 > 0) { SBAR(); __builtin_amdgcn_s_sleep(SLP2); SBAR(); } } while (0)
#define MFMA(a, b, c) __builtin_amdgcn_mfma_f32_32x32x16_bf16((a), (b), (c), 0, 0, 0)

constexpr int S = 2048, D = 512, FF = 1792;
constexpr int MTILES = S / 32;
constexpr int KS_D = D / 16;
constexpr int KS_F = FF / 16;
constexpr size_t MB = 1024 * 1024;
constexpr size_t WS_XN = 0;
constexpr size_t WS_Q = 2 * MB;
constexpr size_t WS_K = 4 * MB;
constexpr size_t WS_V = 6 * MB;
constexpr size_t WS_O = 8 * MB;
constexpr size_t WS_G = 10 * MB;
constexpr size_t WS_H = 12 * MB;
constexpr size_t WS_CNT = 20 * MB;

DI unsigned cvtpk(float lo, float hi) {
  f32x2 v = {lo, hi};
  bf16x2_t b = __builtin_convertvector(v, bf16x2_t);
  return __builtin_bit_cast(unsigned, b);
}
DI float bf_lo(unsigned u) { return __uint_as_float(u << 16); }
DI float bf_hi(unsigned u) { return __uint_as_float(u & 0xffff0000u); }
DI unsigned rfl(unsigned x) { return __builtin_amdgcn_readfirstlane(x); }
DI __amdgpu_buffer_rsrc_t mk_rsrc(const void* p, unsigned bytes) {
  return __builtin_amdgcn_make_buffer_rsrc((void*)p, (short)0, (int)bytes, 0x00020000);
}
DI void dma16(__amdgpu_buffer_rsrc_t rsrc, unsigned voff, unsigned soff, unsigned lds_addr) {
  asm volatile("s_mov_b32 m0, %0\n\ts_nop 0\n\tbuffer_load_dwordx4 %1, %2, %3 offen lds"
               :: "s"(lds_addr), "v"(voff), "s"(rsrc), "s"(soff) : "memory");
}
DI void dma16x4(__amdgpu_buffer_rsrc_t rsrc, unsigned voff, unsigned s0, unsigned s1, unsigned s2, unsigned s3, unsigned lds_addr) {
  asm volatile("s_mov_b32 m0, %0\n\ts_nop 0\n\t"
               "buffer_load_dwordx4 %1, %2, %3 offen lds\n\t"
               "buffer_load_dwordx4 %1, %2, %4 offen offset:1024 lds\n\t"
               "buffer_load_dwordx4 %1, %2, %5 offen offset:2048 lds\n\t"
               "buffer_load_dwordx4 %1, %2, %6 offen offset:3072 lds"
               :: "s"(lds_addr), "v"(voff), "s"(rsrc), "s"(s0 + 4096u), "s"(s1 + 3072u), "s"(s2 + 2048u), "s"(s3 + 1024u) : "memory");
}
DI void dma16x4e(__amdgpu_buffer_rsrc_t rsrc, unsigned v0, unsigned v1, unsigned s0, unsigned s1, unsigned s2, unsigned s3, unsigned lds_addr) {
  asm volatile("s_mov_b32 m0, %0\n\ts_nop 0\n\t"
               "buffer_load_dwordx4 %1, %3, %4 offen lds\n\t"
               "buffer_load_dwordx4 %2, %3, %5 offen offset:1024 lds\n\t"
               "buffer_load_dwordx4 %1, %3, %6 offen offset:2048 lds\n\t"
               "buffer_load_dwordx4 %2, %3, %7 offen offset:3072 lds"
               :: "s"(lds_addr), "v"(v0), "v"(v1), "s"(rsrc), "s"(s0 + 4096u), "s"(s1 + 3072u), "s"(s2 + 2048u), "s"(s3 + 1024u) : "memory");
}
DI void dma16x2(__amdgpu_buffer_rsrc_t rsrc, unsigned voff, unsigned s0, unsigned s1, unsigned lds_addr) {
  asm volatile("s_mov_b32 m0, %0\n\ts_nop 0\n\t"
               "buffer_load_dwordx4 %1, %2, %3 offen lds\n\t"
               "buffer_load_dwordx4 %1, %2, %4 offen offset:1024 lds"
               :: "s"(lds_addr), "v"(voff), "s"(rsrc), "s"(s0 + 4096u), "s"(s1 + 3072u) : "memory");
}
DI __amdgpu_buffer_rsrc_t mk_rsrc_x(const void* p, unsigned bytes) {
  return __builtin_amdgcn_make_buffer_rsrc((void*)((const char*)p - 4096), (short)0, (int)(bytes + 4096u), 0x00020000);
}
DI void bar_plain() { asm volatile("s_barrier" ::: "memory"); }
DI void touch(__amdgpu_buffer_rsrc_t rsrc, unsigned voff, unsigned lds_addr) {
  asm volatile("s_mov_b32 m0, %0\n\ts_nop 0\n\tbuffer_load_dword %1, %2, 0 offen lds"
               :: "s"(lds_addr), "v"(voff), "s"(rsrc) : "memory");
}
DI void snop4() { asm volatile("s_nop 4" ::: "memory"); }
template <int N> DI void wait_bar() {
  asm volatile("s_waitcnt vmcnt(%0) lgkmcnt(0)\n\ts_barrier" :: "i"(N) : "memory");
}
template <int PER, int N> DI void wait_tail() {
  if constexpr (N > 0) { wait_bar<PER * (N - 1)>(); wait_tail<PER, N - 1>(); }
}
DI void bar_only() { asm volatile("s_waitcnt lgkmcnt(0)\n\ts_barrier" ::: "memory"); }
DI bf16x8 lds_rd128(lds_cptr p) { return *(LDSP(const bf16x8)*)p; }
DI s16x4 lds_tr(lds_cptr p) {
  return __builtin_bit_cast(s16x4, __builtin_amdgcn_ds_read_tr16_b64_v4i16((LDSP(v4i16_t)*)p));
}
DI bf16x8 cat8(s16x4 a, s16x4 b) { return __builtin_shufflevector(a, b, 0, 1, 2, 3, 4, 5, 6, 7); }

#define DG_BEGIN()
#define DG_END(kid)
#define DG_MARK(kid)

#define K1_LA 3
constexpr int K1_STG = 28 * 1024, K1_NS = 5;
template <int VMASK>
DI void k1_body(char* lds_, const u16* __restrict__ x, const u16* __restrict__ W, char* __restrict__ ws, int mb, int nb) {
  const int tid = threadIdx.x, lane = tid & 63;
  const int w = rfl(tid >> 6);
  const unsigned lds0 = (unsigned)(uintptr_t)lds_;
  const int rot = (blockIdx.x >> 3) & 7;
  if (w >= 8) {
    if (w < 10) {
      const __amdgpu_buffer_rsrc_t rx = mk_rsrc_x(x, S * D * 2);
      const unsigned vx0 = (lane >> 3) * 1024 + (((lane & 7) ^ ((lane >> 4) & 7)) * 16);
      const unsigned vx1 = (lane >> 3) * 1024 + (((lane & 7) ^ ((4 + (lane >> 4)) & 7)) * 16);
      const int i0 = (w - 8) * 8;
      unsigned slot = lds0 + i0 * 1024, kc = rot;
      const unsigned slot_end = slot + K1_NS * K1_STG, sb = (unsigned)(mb * 128 * 1024 + i0 * 8192);
      auto issue = [&]() {
        const unsigned so = sb + kc * 128;
        dma16x4e(rx, vx0, vx1, so, so + 8192, so + 2 * 8192, so + 3 * 8192, slot);
        dma16x4e(rx, vx0, vx1, so + 4 * 8192, so + 5 * 8192, so + 6 * 8192, so + 7 * 8192, slot + 4096);
        kc = (kc + 1) & 7; slot += K1_STG;
        if (slot == slot_end) slot -= K1_NS * K1_STG;
      };
      snop4();
#pragma unroll
      for (int s0 = 0; s0 < K1_LA; ++s0) issue();
#pragma unroll 1
      for (int st = 0; st < 8 - K1_LA; ++st) { wait_bar<8 * (K1_LA - 1)>(); issue(); }
      wait_tail<8, K1_LA>();
    } else {
      const __amdgpu_buffer_rsrc_t rw = mk_rsrc_x(W, D * 1536 * 2);
      const unsigned vw = (lane >> 2) * 3072 + (lane & 3) * 16;
      const int i0 = (w - 10) * 6;
      unsigned slot = lds0 + 16384 + i0 * 1024, kc = rot;
      const unsigned slot_end = slot + K1_NS * K1_STG;
      unsigned po[6];
#pragma unroll
      for (int j = 0; j < 6; ++j) { const int i = i0 + j; po[j] = (unsigned)(nb * 192 + (i & 3) * 16 * 3072 + (i >> 2) * 64); }
      auto issue = [&]() {
        const unsigned so = kc * (64 * 3072);
        dma16x4(rw, vw, so + po[0], so + po[1], so + po[2], so + po[3], slot);
        dma16x2(rw, vw, so + po[4], so + po[5], slot + 4096);
        kc = (kc + 1) & 7; slot += K1_STG;
        if (slot == slot_end) slot -= K1_NS * K1_STG;
      };
      snop4();
#pragma unroll
      for (int s0 = 0; s0 < K1_LA; ++s0) issue();
#pragma unroll 1
      for (int st = 0; st < 8 - K1_LA; ++st) { wait_bar<6 * (K1_LA - 1)>(); issue(); }
      wait_tail<6, K1_LA>();
    }
    bar_only(); bar_only();
    return;
  }
  const int mt = w & 3, kh = w >> 2;
  const int xrl = mt * 32 + nb * 2 + kh, xrow = mb * 128 + xrl;
  const u32x4 xrv = *(const u32x4*)(x + (size_t)xrow * D + lane * 8);
  f32x16 acc[3], csa;
#pragma unroll
  for (int r = 0; r < 16; ++r) { acc[0][r] = 0.f; acc[1][r] = 0.f; acc[2][r] = 0.f; csa[r] = 0.f; }
  float sum4[4] = {0.f, 0.f, 0.f, 0.f}, sq4[4] = {0.f, 0.f, 0.f, 0.f};
  const bf16x8 ones = {0x3f80, 0x3f80, 0x3f80, 0x3f80, 0x3f80, 0x3f80, 0x3f80, 0x3f80};
  const int h = lane >> 5, i16 = lane & 15, q4 = i16 >> 2, p4 = i16 & 3, blk = (lane >> 4) & 1;
  const unsigned wbase = 16384 + (8 * h + q4) * 64 + blk * 32 + p4 * 8;
  const int sw = (lane >> 1) & 7;
  unsigned xa[2];
#pragma unroll
  for (int j = 0; j < 2; ++j) xa[j] = (mt * 32 + (lane & 31)) * 128 + (((2 * (kh + 2 * j) + h) ^ sw) * 16);
  lds_cptr L = (lds_cptr)lds_;
  struct Fr { bf16x8 t, w0, w1, w2, wc; };
  const unsigned wcoff = (mt == 3 ? 0 : mt) * 4096;
  auto rd = [&](Fr& f, lds_cptr sl, int j) {
    const int ks = kh + 2 * j;
    lds_cptr wp = sl + wbase + ks * 1024;
    f.t = lds_rd128(sl + xa[j]);
    f.w0 = cat8(lds_tr(wp), lds_tr(wp + 256));
    f.w1 = cat8(lds_tr(wp + 4096), lds_tr(wp + 4096 + 256));
    f.w2 = cat8(lds_tr(wp + 8192), lds_tr(wp + 8192 + 256));
    f.wc = cat8(lds_tr(wp + wcoff), lds_tr(wp + wcoff + 256));
  };
  auto mm = [&](const Fr& f) {
    if (VMASK & 1) acc[0] = MFMA(f.t, f.w0, acc[0]); else acc[0] = MFMA(f.w0, f.t, acc[0]);
    if (VMASK & 2) acc[1] = MFMA(f.t, f.w1, acc[1]); else acc[1] = MFMA(f.w1, f.t, acc[1]);
    if (VMASK & 4) acc[2] = MFMA(f.t, f.w2, acc[2]); else acc[2] = MFMA(f.w2, f.t, acc[2]);
    csa = MFMA(f.wc, ones, csa);
    const u32x4 tu = __builtin_bit_cast(u32x4, f.t);
#pragma unroll
    for (int e = 0; e < 4; ++e) {
      const float lo = bf_lo(tu[e]), hi = bf_hi(tu[e]);
      sum4[e] += lo; sq4[e] = __builtin_fmaf(lo, lo, sq4[e]);
      sum4[e] += hi; sq4[e] = __builtin_fmaf(hi, hi, sq4[e]);
    }
  };
  Fr fa, fb;
  bar_plain();
  rd(fa, L, 0); SBAR();
  rd(fb, L, 1); SBAR();
  mm(fa); SBAR();
  unsigned cso = K1_STG;
#pragma unroll 1
  for (int st = 1; st < 8; ++st) {
    bar_plain();
    lds_cptr sl = L + cso;
    cso += K1_STG; if (cso == K1_NS * K1_STG) cso = 0;
    rd(fa, sl, 0); SBAR();
    mm(fb); SBAR();
    rd(fb, sl, 1); SBAR();
    mm(fa); SBAR();
  }
  mm(fb);
  float sum = (sum4[0] + sum4[1]) + (sum4[2] + sum4[3]), sq = (sq4[0] + sq4[1]) + (sq4[2] + sq4[3]);
  sum += __shfl_xor(sum, 32); sq += __shfl_xor(sq, 32);
  bar_only();
  float* ex = (float*)lds_;
  float* exs = (float*)(lds_ + 65536);
  float* exc = exs + 8 * 128;
  float* tab = exc + 192 + w * 64;
  {
    f32x4* dst = (f32x4*)(ex + w * 2048);
    if (kh == 1) {
#pragma unroll
      for (int n = 0; n < 2; ++n)
#pragma unroll
        for (int g = 0; g < 4; ++g)
          dst[(n * 4 + g) * 64 + lane] = f32x4{acc[n][4 * g], acc[n][4 * g + 1], acc[n][4 * g + 2], acc[n][4 * g + 3]};
    } else {
#pragma unroll
      for (int g = 0; g < 4; ++g)
        dst[g * 64 + lane] = f32x4{acc[2][4 * g], acc[2][4 * g + 1], acc[2][4 * g + 2], acc[2][4 * g + 3]};
    }
    float* ds = exs + w * 128;
    ds[lane] = sum; ds[64 + lane] = sq;
    if (mt < 3 && (lane & 31) == 0) {
#pragma unroll
      for (int g = 0; g < 4; ++g)
        *(f32x4*)(exc + kh * 96 + mt * 32 + 8 * g + 4 * h) = f32x4{csa[4 * g], csa[4 * g + 1], csa[4 * g + 2], csa[4 * g + 3]};
    }
  }
  bar_only();
  const int wo = w ^ 4;
  {
    const f32x4* src = (const f32x4*)(ex + wo * 2048);
    if (kh == 0) {
#pragma unroll
      for (int n = 0; n < 2; ++n)
#pragma unroll
        for (int g = 0; g < 4; ++g) {
          const f32x4 v = src[(n * 4 + g) * 64 + lane];
#pragma unroll
          for (int e = 0; e < 4; ++e) acc[n][4 * g + e] += v[e];
        }
    } else {
#pragma unroll
      for (int g = 0; g < 4; ++g) {
        const f32x4 v = src[g * 64 + lane];
#pragma unroll
        for (int e = 0; e < 4; ++e) acc[2][4 * g + e] += v[e];
      }
    }
    const float* ds = exs + wo * 128;
    sum += ds[lane]; sq += ds[64 + lane];
  }
  const float mean = sum * (1.f / D);
  const float var = fmaxf(sq * (1.f / D) - mean * mean, 0.f);
  const float ra = 1.f / sqrtf(var + 1e-5f), rb = ra * mean;
  if (h == 0) { tab[lane] = ra; tab[32 + lane] = rb; }
  const int mtg = mb * 4 + mt;
#pragma unroll
  for (int n = 0; n < 3; ++n) {
    if ((n < 2) != (kh == 0)) continue;
    const int ct = nb * 3 + n;
    const int which = ct >> 4, hp = (ct & 15) * 2;
    float y[16];
    if (!((VMASK >> n) & 1)) {
      const float sc = (which == 0) ? 0.25f * 1.4426950408889634f : 1.f;
      const float a2 = ra * sc, b2 = rb * sc;
      char* dst = ws + (which == 0 ? WS_Q : WS_K);
#pragma unroll
      for (int g = 0; g < 4; ++g) {
        const f32x4 c4 = *(const f32x4*)(exc + n * 32 + 8 * g + 4 * h), c5 = *(const f32x4*)(exc + 96 + n * 32 + 8 * g + 4 * h);
#pragma unroll
        for (int e = 0; e < 4; ++e) y[4 * g + e] = acc[n][4 * g + e] * a2 - b2 * (c4[e] + c5[e]);
      }
#pragma unroll
      for (int hh = 0; hh < 2; ++hh) {
        u32x4 o;
#pragma unroll
        for (int j = 0; j < 4; ++j) o[j] = cvtpk(y[8 * hh + 2 * j], y[8 * hh + 2 * j + 1]);
        *(u32x4*)(dst + ((size_t)((hp + hh) * 64 + mtg) * 64 + lane) * 16) = o;
      }
    } else {
      char* dst = ws + WS_V;
      const int hd = hp + blk, d = lane & 15;
      const float csl = exc[n * 32 + (lane & 31)] + exc[96 + n * 32 + (lane & 31)];
#pragma unroll
      for (int g = 0; g < 4; ++g) {
        const f32x4 a4 = *(const f32x4*)(tab + 8 * g + 4 * h);
        const f32x4 b4 = *(const f32x4*)(tab + 32 + 8 * g + 4 * h);
#pragma unroll
        for (int e = 0; e < 4; ++e) y[4 * g + e] = acc[n][4 * g + e] * a4[e] - b4[e] * csl;
      }
#pragma unroll
      for (int s2 = 0; s2 < 2; ++s2) {
        u32x4 o;
#pragma unroll
        for (int j = 0; j < 4; ++j) o[j] = cvtpk(y[8 * s2 + 2 * j], y[8 * s2 + 2 * j + 1]);
        *(u32x4*)(dst + ((((size_t)(hd * 64 + mtg) * 2 + s2) * 2 + h) * 16 + d) * 16) = o;
      }
    }
  }
  {
    const float a = __shfl(ra, nb * 2 + kh), b = __shfl(rb, nb * 2 + kh);
    u32x4 o;
#pragma unroll
    for (int j = 0; j < 4; ++j) o[j] = cvtpk(bf_lo(xrv[j]) * a - b, bf_hi(xrv[j]) * a - b);
    const int s16 = lane >> 1, hh = lane & 1;
    *(u32x4*)(ws + WS_XN + ((size_t)((xrow >> 5) * KS_D + s16) * 64 + hh * 32 + (xrow & 31)) * 16) = o;
  }
}
__global__ __launch_bounds__(768) void k1_qkv(const u16* __restrict__ x, const u16* __restrict__ W,
                                              char* __restrict__ ws) {
  __shared__ __attribute__((aligned(16))) char lds_[K1_NS * K1_STG];
  DG_BEGIN();
  if (blockIdx.x == 0 && threadIdx.x < 16) *(unsigned*)(ws + WS_CNT + threadIdx.x * 256) = 0u;
  const int xc = blockIdx.x & 7, bi = blockIdx.x >> 3;
  const int mb = (xc & 3) * 4 + (bi & 3), nb = (xc >> 2) * 8 + (bi >> 2);
  if (nb < 10) k1_body<0>(lds_, x, W, ws, mb, nb);
  else if (nb == 10) k1_body<4>(lds_, x, W, ws, mb, nb);
  else k1_body<7>(lds_, x, W, ws, mb, nb);
  DG_END(0);
}

DI unsigned e4_vw(int lane) {
  const int drow = lane >> 3, dpos = (lane >> 1) & 3;
  return drow * 1024 + ((dpos ^ (((drow >> 1) & 1) << 1)) * 32) + (lane & 1) * 16;
}
struct E4Fr { bf16x8 t0, w0, t1, w1; };
DI void e4_rd(E4Fr& f, lds_cptr sl, int lane, int mt, unsigned wbase, int half) {
  const int ks = half * 2;
  f.t0 = lds_rd128(sl + (mt * 4 + ks) * 1024 + lane * 16);
  lds_cptr wp = sl + wbase + ks * 2048;
  f.w0 = cat8(lds_tr(wp), lds_tr(wp + 1024));
  f.t1 = lds_rd128(sl + (mt * 4 + ks + 1) * 1024 + lane * 16);
  f.w1 = cat8(lds_tr(wp + 2048), lds_tr(wp + 2048 + 1024));
}
DI void e4_mm(const E4Fr& f, f32x16& acc) { acc = MFMA(f.w0, f.t0, acc); acc = MFMA(f.w1, f.t1, acc); }
DI void e4_mm_paced(const E4Fr& f, f32x16& acc) { acc = MFMA(f.w0, f.t0, acc); PACE_G2(); acc = MFMA(f.w1, f.t1, acc); PACE_G2(); }
DI unsigned e4_wbase(int lane, int nt) {
  const int h = lane >> 5, i16 = lane & 15, q4 = i16 >> 2, p4 = i16 & 3, blk = (lane >> 4) & 1;
  return 8192 + (4 * h + q4) * 128 + (((2 * nt + blk) ^ (((q4 >> 1) & 1) << 1)) * 32) + p4 * 8;
}

constexpr int K2_LDS = 73728 + 256, K2_SCR = 73728;
DI void mlp_role(char* lds_, const u16* __restrict__ Wg, const u16* __restrict__ Wu, const u16* __restrict__ Wd,
                 const u16* __restrict__ Wo, char* __restrict__ ws, int b) {
  const int tid = threadIdx.x, lane = tid & 63;
  const int w = rfl(tid >> 6);
  const unsigned lds0 = (unsigned)(uintptr_t)lds_;
  lds_cptr L = (lds_cptr)lds_;
  const int xc = b & 7, bi = b >> 3;
  __builtin_amdgcn_s_setprio(MLP_PRIO);
  {
    constexpr int STG = 12 * 1024, NS = 6, LA = 4, NST = 32;
    const int mb = (xc & 3) * 4 + (bi & 3), nb = (xc >> 2) * 8 + (bi >> 2);
    if (w == 7) {
      const __amdgpu_buffer_rsrc_t rt = mk_rsrc_x(ws + WS_XN, MTILES * KS_D * 1024);
      const __amdgpu_buffer_rsrc_t rg = mk_rsrc_x(Wg, D * FF * 2);
      const __amdgpu_buffer_rsrc_t ru = mk_rsrc_x(Wu, D * FF * 2);
      const unsigned vt = lane * 16;
      const int drow = lane >> 4, pos = (lane >> 1) & 7, half = lane & 1;
      int ug = pos ^ (2 * (drow & 3)), uu = pos ^ (2 * (drow & 3) + 1);
      if (ug > 6) ug = 6;
      if (uu > 6) uu = 6;
      const unsigned vg = drow * (FF * 2) + ug * 32 + half * 16;
      const unsigned vu = drow * (FF * 2) + uu * 32 + half * 16;
      unsigned slot = lds0, st0 = (unsigned)(mb * 4 * KS_D * 1024), so = (unsigned)(nb * 224);
      auto issue = [&]() {
        dma16x4(rt, vt, st0, st0 + KS_D * 1024, st0 + 2 * KS_D * 1024, st0 + 3 * KS_D * 1024, slot);
        dma16x4(rg, vg, so, so + 4 * FF * 2, so + 8 * FF * 2, so + 12 * FF * 2, slot + 4096);
        dma16x4(ru, vu, so, so + 4 * FF * 2, so + 8 * FF * 2, so + 12 * FF * 2, slot + 8192);
        st0 += 1024; so += 16 * FF * 2; slot += STG;
        if (slot == lds0 + NS * STG) slot = lds0;
      };
      snop4();
#pragma unroll
      for (int st = 0; st < LA; ++st) issue();
#pragma unroll 1
      for (int st = 0; st < NST - LA; ++st) { wait_bar<12 * (LA - 1)>(); issue(); }
      wait_tail<12, LA>();
    } else {
      f32x16 acc[4];
#pragma unroll
      for (int n = 0; n < 4; ++n)
#pragma unroll
        for (int r = 0; r < 16; ++r) acc[n][r] = 0.f;
      const int h = lane >> 5, i16 = lane & 15, q4 = i16 >> 2, p4 = i16 & 3, blk = (lane >> 4) & 1;
      const unsigned wa = 4096 * (1 + blk) + (8 * h + q4) * 256 + ((w ^ (2 * q4 + blk)) & 7) * 32 + p4 * 8;
      struct Fr { bf16x8 w, t0, t1, t2, t3; };
      unsigned so = 0;
      auto rd = [&](Fr& f) {
        lds_cptr sl = L + so;
        so += STG; if (so == NS * STG) so = 0;
        f.w = cat8(lds_tr(sl + wa), lds_tr(sl + wa + 1024));
        f.t0 = lds_rd128(sl + 0 * 1024 + lane * 16);
        f.t1 = lds_rd128(sl + 1 * 1024 + lane * 16);
        f.t2 = lds_rd128(sl + 2 * 1024 + lane * 16);
        f.t3 = lds_rd128(sl + 3 * 1024 + lane * 16);
      };
      auto mm = [&](const Fr& f) {
        acc[0] = MFMA(f.w, f.t0, acc[0]); PACE_G1(); acc[1] = MFMA(f.w, f.t1, acc[1]); PACE_G1();
        acc[2] = MFMA(f.w, f.t2, acc[2]); PACE_G1(); acc[3] = MFMA(f.w, f.t3, acc[3]); PACE_G1();
      };
      Fr fa, fb;
      bar_plain();
      rd(fa); SBAR();
      bar_plain();
      rd(fb); SBAR();
      mm(fa); SBAR();
#pragma unroll 1
      for (int st = 2; st < NST; st += 2) {
        bar_plain();
        rd(fa); SBAR();
        mm(fb); SBAR();
        bar_plain();
        rd(fb); SBAR();
        mm(fa); SBAR();
      }
      mm(fb);
      const __amdgpu_buffer_rsrc_t rh = mk_rsrc(ws + WS_H, MTILES * KS_F * 1024);
#pragma unroll
      for (int m = 0; m < 4; ++m) {
        float hv[8];
#pragma unroll
        for (int j = 0; j < 8; ++j) {
          const float g = acc[m][j], u = acc[m][8 + j];
          const float z = g * (-2.3022081985f + -0.1029432397f * g * g);
          const float e = __builtin_amdgcn_exp2f(z);
          hv[j] = g * __builtin_amdgcn_rcpf(1.f + e) * u;
        }
        u32x4 o;
#pragma unroll
        for (int j = 0; j < 4; ++j) o[j] = cvtpk(hv[2 * j], hv[2 * j + 1]);
        __builtin_amdgcn_raw_buffer_store_b128(o, rh, (unsigned)(lane * 16 + ((mb * 4 + m) * KS_F + nb * 7 + w) * 1024), 0, 16);
      }
    }
    wait_bar<0>();
    if (tid == 0) __hip_atomic_fetch_add((gu32*)(ws + WS_CNT + mb * 256), 1u, __ATOMIC_RELAXED, __HIP_MEMORY_SCOPE_AGENT);
  }
  DG_MARK(2);
  {
    constexpr int STG = 8192, NS = 9, LA = 7, NST = KS_F / 2;
    const int mb2 = ((xc & 3) * 4 + (xc >> 2) * 2) * 2 + (bi & 3), nb2 = bi >> 2;
    if (w > 5) return;
    const __amdgpu_buffer_rsrc_t rl = (w == 5) ? mk_rsrc_x(Wd, FF * D * 2) : mk_rsrc_x(ws + WS_H, MTILES * KS_F * 1024);
    const unsigned vl = (w == 5) ? e4_vw(lane) : (unsigned)(lane * 16);
    unsigned slot = lds0 + ((w == 5) ? 4096u : 0u);
    const unsigned slot_end = slot + NS * STG;
    unsigned so = (w == 5) ? (unsigned)(nb2 * 128) : (unsigned)(mb2 * 2 * KS_F * 1024);
    const unsigned d1 = (w == 5) ? 8192u : 1024u, d2 = (w == 5) ? 16384u : (unsigned)(KS_F * 1024), d3 = (w == 5) ? 24576u : (unsigned)(KS_F * 1024 + 1024);
    const unsigned dst = (w == 5) ? 32768u : 2048u;
    auto issue = [&]() {
      dma16x4(rl, vl, so, so + d1, so + d2, so + d3, slot);
      so += dst; slot += STG;
      if (slot == slot_end) slot -= NS * STG;
    };
    if (w == 5) {
      snop4();
#pragma unroll
      for (int st = 0; st < LA; ++st) issue();
    }
    if (tid == 0) {
      gu32* cnt = (gu32*)(ws + WS_CNT + (mb2 >> 1) * 256);
      unsigned spins = 0;
      while (__hip_atomic_load(cnt, __ATOMIC_RELAXED, __HIP_MEMORY_SCOPE_AGENT) < 16u) {
        __builtin_amdgcn_s_sleep(2);
        if (++spins > (1u << 22)) break;
      }
      __builtin_amdgcn_fence(__ATOMIC_ACQUIRE, "agent");
      asm volatile("s_waitcnt vmcnt(0)" ::: "memory");
    }
    bar_only();
    if (w >= 4) {
      if (w == 4) {
        snop4();
#pragma unroll
        for (int st = 0; st < LA; ++st) issue();
      }
#pragma unroll 1
      for (int st = 0; st < NST - LA; ++st) { wait_bar<4 * (LA - 1)>(); issue(); }
      wait_tail<4, LA>();
      return;
    }
    const int mt = w & 1, nt = w >> 1;
    f32x16 acc;
#pragma unroll
    for (int r = 0; r < 16; ++r) acc[r] = 0.f;
    const int h = lane >> 5, i16 = lane & 15, q4 = i16 >> 2, p4 = i16 & 3, blk = (lane >> 4) & 1;
    const unsigned wbase = 4096 + (4 * h + q4) * 128 + (((2 * nt + blk) ^ (((q4 >> 1) & 1) << 1)) * 32) + p4 * 8;
    unsigned cso = 0;
    auto rd = [&](E4Fr& f) {
      lds_cptr sl = L + cso;
      cso += STG; if (cso == NS * STG) cso = 0;
      f.t0 = lds_rd128(sl + (mt * 2) * 1024 + lane * 16);
      f.w0 = cat8(lds_tr(sl + wbase), lds_tr(sl + wbase + 1024));
      f.t1 = lds_rd128(sl + (mt * 2 + 1) * 1024 + lane * 16);
      f.w1 = cat8(lds_tr(sl + wbase + 2048), lds_tr(sl + wbase + 2048 + 1024));
    };
    E4Fr fa, fb;
    bar_plain();
    rd(fa); SBAR();
    bar_plain();
    rd(fb); SBAR();
    e4_mm_paced(fa, acc); SBAR();
#pragma unroll 1
    for (int st = 2; st < NST; st += 2) {
      bar_plain();
      rd(fa); SBAR();
      e4_mm_paced(fb, acc); SBAR();
      bar_plain();
      rd(fb); SBAR();
      e4_mm_paced(fa, acc); SBAR();
    }
    e4_mm_paced(fb, acc);
    u32x4 o[2];
#pragma unroll
    for (int j = 0; j < 8; ++j) {
      float a = acc[2 * j], bb = acc[2 * j + 1];
      a = a * __builtin_amdgcn_rcpf(1.f + __builtin_amdgcn_exp2f(-1.4426950408889634f * a));
      bb = bb * __builtin_amdgcn_rcpf(1.f + __builtin_amdgcn_exp2f(-1.4426950408889634f * bb));
      o[j >> 2][j & 3] = cvtpk(a, bb);
    }
    u32x4* gp = (u32x4*)(ws + WS_G + ((size_t)((mb2 * 2 + mt) * 16 + nb2 * 2 + nt) * 64 + lane) * 32);
    gp[0] = o[0]; gp[1] = o[1];
  }
}

constexpr float ATT_BIAS = 64.f;
constexpr float ATT_LIM = 1.329228e36f;
#define ATTN_ASM \
    "s_waitcnt lgkmcnt(0)\n\t" \
    "s_and_b32 %[t0], %[c], 3\n\t" \
    "s_lshl_b32 %[t0], %[t0], 14\n\t" \
    "s_add_u32 %[t1], %[c], 1\n\t" \
    "s_and_b32 %[t1], %[t1], 3\n\t" \
    "s_lshl_b32 %[t1], %[t1], 14\n\t" \
    "v_add_u32 v101, %[t0], v109\n\t" \
    "v_add_u32 v102, %[t1], v109\n\t" \
    "v_mad_u32_u24 v103, v107, %[t0], v106\n\t" \
    "v_mad_u32_u24 v104, v107, %[t1], v106\n\t" \
    "v_mov_b32 v68, 0\n\t" \
    "v_mov_b32 v69, 0\n\t" \
    "v_mov_b32 v70, 0\n\t" \
    "v_mov_b32 v71, 0\n\t" \
    "v_mov_b32 v84, 0\n\t" \
    "v_mov_b32 v85, 0\n\t" \
    "v_mov_b32 v86, 0\n\t" \
    "v_mov_b32 v87, 0\n\t" \
    "ds_read_b128 v[72:75], v101 offset:0\n\t" \
    "ds_read_b128 v[76:79], v101 offset:1024\n\t" \
    "ds_read_b128 v[80:83], v103 offset:0\n\t" \
    "s_waitcnt lgkmcnt(2)\n\t" \
    "v_mfma_f32_32x32x16_bf16 v[32:47], v[72:75], v[88:91], v[16:31]\n\t" \
    "s_waitcnt lgkmcnt(1)\n\t" \
    "s_nop 7\n\t" \
    "L_loop_%=:\n\t" \
    "s_waitcnt lgkmcnt(2)\n\t" \
    "v_mfma_f32_32x32x16_bf16 v[48:63], v[76:79], v[88:91], v[16:31]\n\t" \
    "ds_read_b128 v[72:75], v101 offset:2048\n\t" \
    "v_exp_f32 v32, v32\n\t" \
    "v_exp_f32 v33, v33\n\t" \
    "v_exp_f32 v34, v34\n\t" \
    "v_cvt_pk_bf16_f32 v64, v32, v33\n\t" \
    "v_exp_f32 v35, v35\n\t" \
    "v_exp_f32 v36, v36\n\t" \
    "v_cvt_pk_bf16_f32 v65, v34, v35\n\t" \
    "v_exp_f32 v37, v37\n\t" \
    "s_waitcnt lgkmcnt(2)\n\t" \
    "v_mfma_f32_32x32x16_bf16 v[0:15], v[84:87], v[68:71], v[0:15]\n\t" \
    "ds_read_b128 v[84:87], v103 offset:512\n\t" \
    "v_exp_f32 v38, v38\n\t" \
    "v_cvt_pk_bf16_f32 v66, v36, v37\n\t" \
    "v_exp_f32 v39, v39\n\t" \
    "v_exp_f32 v40, v40\n\t" \
    "v_cvt_pk_bf16_f32 v67, v38, v39\n\t" \
    "v_exp_f32 v41, v41\n\t" \
    "v_exp_f32 v42, v42\n\t" \
    "v_cvt_pk_bf16_f32 v68, v40, v41\n\t" \
    "v_exp_f32 v43, v43\n\t" \
    "v_exp_f32 v44, v44\n\t" \
    "v_cvt_pk_bf16_f32 v69, v42, v43\n\t" \
    "v_exp_f32 v45, v45\n\t" \
    "v_fma_f32 v105, v0, 0, v8\n\t" \
    "v_fma_f32 v105, v1, 0, v105\n\t" \
    "v_fma_f32 v105, v2, 0, v105\n\t" \
    "v_fma_f32 v105, v3, 0, v105\n\t" \
    "v_fma_f32 v105, v4, 0, v105\n\t" \
    "v_fma_f32 v105, v5, 0, v105\n\t" \
    "v_fma_f32 v105, v6, 0, v105\n\t" \
    "v_fma_f32 v105, v7, 0, v105\n\t" \
    "v_cmp_ngt_f32 vcc, %[lim], v105\n\t" \
    "s_and_b64 vcc, vcc, %[chk]\n\t" \
    "s_cbranch_vccnz L_fail_%=\n\t" \
    "v_mov_b32 v92, v0\n\t" \
    "v_mov_b32 v93, v1\n\t" \
    "v_mov_b32 v94, v2\n\t" \
    "v_mov_b32 v95, v3\n\t" \
    "v_mov_b32 v96, v4\n\t" \
    "v_mov_b32 v97, v5\n\t" \
    "v_mov_b32 v98, v6\n\t" \
    "v_mov_b32 v99, v7\n\t" \
    "v_mov_b32 v100, v8\n\t" \
    "s_add_u32 %[t0], %[c], 3\n\t" \
    "s_and_b32 %[t1], %[t0], 3\n\t" \
    "s_min_u32 %[t0], %[t0], 7\n\t" \
    "s_lshl_b32 %[t0], %[t0], 13\n\t" \
    "s_add_u32 %[t0], %[t0], %[w1024]\n\t" \
    "s_lshl_b32 %[t1], %[t1], 14\n\t" \
    "s_add_u32 %[t1], %[t1], %[ldsw]\n\t" \
    "s_mov_b32 m0, %[t1]\n\t" \
    "s_waitcnt vmcnt(2)\n\t" \
    "s_barrier\n\t" \
    "buffer_load_dwordx4 v108, %[rk], %[t0] offen lds\n\t" \
    "s_add_u32 m0, %[t1], 0x2000\n\t" \
    "s_nop 0\n\t" \
    "buffer_load_dwordx4 v108, %[rv], %[t0] offen lds\n\t" \
    "s_waitcnt lgkmcnt(2)\n\t" \
    "v_mfma_f32_32x32x16_bf16 v[0:15], v[80:83], v[64:67], v[0:15]\n\t" \
    "ds_read_b128 v[80:83], v103 offset:1024\n\t" \
    "v_exp_f32 v46, v46\n\t" \
    "v_exp_f32 v47, v47\n\t" \
    "v_cvt_pk_bf16_f32 v70, v44, v45\n\t" \
    "v_cvt_pk_bf16_f32 v71, v46, v47\n\t" \
    "s_waitcnt lgkmcnt(2)\n\t" \
    "v_mfma_f32_32x32x16_bf16 v[32:47], v[72:75], v[88:91], v[16:31]\n\t" \
    "ds_read_b128 v[76:79], v101 offset:3072\n\t" \
    "v_exp_f32 v48, v48\n\t" \
    "v_exp_f32 v49, v49\n\t" \
    "v_exp_f32 v50, v50\n\t" \
    "v_cvt_pk_bf16_f32 v64, v48, v49\n\t" \
    "v_exp_f32 v51, v51\n\t" \
    "v_exp_f32 v52, v52\n\t" \
    "v_cvt_pk_bf16_f32 v65, v50, v51\n\t" \
    "v_exp_f32 v53, v53\n\t" \
    "s_waitcnt lgkmcnt(2)\n\t" \
    "v_mfma_f32_32x32x16_bf16 v[0:15], v[84:87], v[68:71], v[0:15]\n\t" \
    "ds_read_b128 v[84:87], v103 offset:1536\n\t" \
    "v_exp_f32 v54, v54\n\t" \
    "v_cvt_pk_bf16_f32 v66, v52, v53\n\t" \
    "v_exp_f32 v55, v55\n\t" \
    "v_exp_f32 v56, v56\n\t" \
    "v_cvt_pk_bf16_f32 v67, v54, v55\n\t" \
    "v_exp_f32 v57, v57\n\t" \
    "v_exp_f32 v58, v58\n\t" \
    "s_waitcnt lgkmcnt(2)\n\t" \
    "v_mfma_f32_32x32x16_bf16 v[0:15], v[80:83], v[64:67], v[0:15]\n\t" \
    "ds_read_b128 v[80:83], v103 offset:2048\n\t" \
    "v_cvt_pk_bf16_f32 v68, v56, v57\n\t" \
    "v_exp_f32 v59, v59\n\t" \
    "v_exp_f32 v60, v60\n\t" \
    "v_cvt_pk_bf16_f32 v69, v58, v59\n\t" \
    "v_exp_f32 v61, v61\n\t" \
    "v_exp_f32 v62, v62\n\t" \
    "v_exp_f32 v63, v63\n\t" \
    "v_cvt_pk_bf16_f32 v70, v60, v61\n\t" \
    "v_cvt_pk_bf16_f32 v71, v62, v63\n\t" \
    "s_waitcnt lgkmcnt(2)\n\t" \
    "v_mfma_f32_32x32x16_bf16 v[48:63], v[76:79], v[88:91], v[16:31]\n\t" \
    "ds_read_b128 v[72:75], v101 offset:4096\n\t" \
    "v_exp_f32 v32, v32\n\t" \
    "v_exp_f32 v33, v33\n\t" \
    "v_exp_f32 v34, v34\n\t" \
    "v_cvt_pk_bf16_f32 v64, v32, v33\n\t" \
    "v_exp_f32 v35, v35\n\t" \
    "v_exp_f32 v36, v36\n\t" \
    "v_cvt_pk_bf16_f32 v65, v34, v35\n\t" \
    "v_exp_f32 v37, v37\n\t" \
    "s_waitcnt lgkmcnt(2)\n\t" \
    "v_mfma_f32_32x32x16_bf16 v[0:15], v[84:87], v[68:71], v[0:15]\n\t" \
    "ds_read_b128 v[84:87], v103 offset:2560\n\t" \
    "v_exp_f32 v38, v38\n\t" \
    "v_cvt_pk_bf16_f32 v66, v36, v37\n\t" \
    "v_exp_f32 v39, v39\n\t" \
    "v_exp_f32 v40, v40\n\t" \
    "v_cvt_pk_bf16_f32 v67, v38, v39\n\t" \
    "v_exp_f32 v41, v41\n\t" \
    "v_exp_f32 v42, v42\n\t" \
    "s_waitcnt lgkmcnt(2)\n\t" \
    "v_mfma_f32_32x32x16_bf16 v[0:15], v[80:83], v[64:67], v[0:15]\n\t" \
    "ds_read_b128 v[80:83], v103 offset:3072\n\t" \
    "v_cvt_pk_bf16_f32 v68, v40, v41\n\t" \
    "v_exp_f32 v43, v43\n\t" \
    "v_exp_f32 v44, v44\n\t" \
    "v_cvt_pk_bf16_f32 v69, v42, v43\n\t" \
    "v_exp_f32 v45, v45\n\t" \
    "v_exp_f32 v46, v46\n\t" \
    "v_exp_f32 v47, v47\n\t" \
    "v_cvt_pk_bf16_f32 v70, v44, v45\n\t" \
    "v_cvt_pk_bf16_f32 v71, v46, v47\n\t" \
    "s_waitcnt lgkmcnt(2)\n\t" \
    "v_mfma_f32_32x32x16_bf16 v[32:47], v[72:75], v[88:91], v[16:31]\n\t" \
    "ds_read_b128 v[76:79], v101 offset:5120\n\t" \
    "v_exp_f32 v48, v48\n\t" \
    "v_exp_f32 v49, v49\n\t" \
    "v_exp_f32 v50, v50\n\t" \
    "v_cvt_pk_bf16_f32 v64, v48, v49\n\t" \
    "v_exp_f32 v51, v51\n\t" \
    "v_exp_f32 v52, v52\n\t" \
    "v_cvt_pk_bf16_f32 v65, v50, v51\n\t" \
    "v_exp_f32 v53, v53\n\t" \
    "s_waitcnt lgkmcnt(2)\n\t" \
    "v_mfma_f32_32x32x16_bf16 v[0:15], v[84:87], v[68:71], v[0:15]\n\t" \
    "ds_read_b128 v[84:87], v103 offset:3584\n\t" \
    "v_exp_f32 v54, v54\n\t" \
    "v_cvt_pk_bf16_f32 v66, v52, v53\n\t" \
    "v_exp_f32 v55, v55\n\t" \
    "v_exp_f32 v56, v56\n\t" \
    "v_cvt_pk_bf16_f32 v67, v54, v55\n\t" \
    "v_exp_f32 v57, v57\n\t" \
    "v_exp_f32 v58, v58\n\t" \
    "s_waitcnt lgkmcnt(2)\n\t" \
    "v_mfma_f32_32x32x16_bf16 v[0:15], v[80:83], v[64:67], v[0:15]\n\t" \
    "ds_read_b128 v[80:83], v103 offset:4096\n\t" \
    "v_cvt_pk_bf16_f32 v68, v56, v57\n\t" \
    "v_exp_f32 v59, v59\n\t" \
    "v_exp_f32 v60, v60\n\t" \
    "v_cvt_pk_bf16_f32 v69, v58, v59\n\t" \
    "v_exp_f32 v61, v61\n\t" \
    "v_exp_f32 v62, v62\n\t" \
    "v_exp_f32 v63, v63\n\t" \
    "v_cvt_pk_bf16_f32 v70, v60, v61\n\t" \
    "v_cvt_pk_bf16_f32 v71, v62, v63\n\t" \
    "s_waitcnt lgkmcnt(2)\n\t" \
    "v_mfma_f32_32x32x16_bf16 v[48:63], v[76:79], v[88:91], v[16:31]\n\t" \
    "ds_read_b128 v[72:75], v101 offset:6144\n\t" \
    "v_exp_f32 v32, v32\n\t" \
    "v_exp_f32 v33, v33\n\t" \
    "v_exp_f32 v34, v34\n\t" \
    "v_cvt_pk_bf16_f32 v64, v32, v33\n\t" \
    "v_exp_f32 v35, v35\n\t" \
    "v_exp_f32 v36, v36\n\t" \
    "v_cvt_pk_bf16_f32 v65, v34, v35\n\t" \
    "v_exp_f32 v37, v37\n\t" \
    "s_waitcnt lgkmcnt(2)\n\t" \
    "v_mfma_f32_32x32x16_bf16 v[0:15], v[84:87], v[68:71], v[0:15]\n\t" \
    "ds_read_b128 v[84:87], v103 offset:4608\n\t" \
    "v_exp_f32 v38, v38\n\t" \
    "v_cvt_pk_bf16_f32 v66, v36, v37\n\t" \
    "v_exp_f32 v39, v39\n\t" \
    "v_exp_f32 v40, v40\n\t" \
    "v_cvt_pk_bf16_f32 v67, v38, v39\n\t" \
    "v_exp_f32 v41, v41\n\t" \
    "v_exp_f32 v42, v42\n\t" \
    "s_waitcnt lgkmcnt(2)\n\t" \
    "v_mfma_f32_32x32x16_bf16 v[0:15], v[80:83], v[64:67], v[0:15]\n\t" \
    "ds_read_b128 v[80:83], v103 offset:5120\n\t" \
    "v_cvt_pk_bf16_f32 v68, v40, v41\n\t" \
    "v_exp_f32 v43, v43\n\t" \
    "v_exp_f32 v44, v44\n\t" \
    "v_cvt_pk_bf16_f32 v69, v42, v43\n\t" \
    "v_exp_f32 v45, v45\n\t" \
    "v_exp_f32 v46, v46\n\t" \
    "v_exp_f32 v47, v47\n\t" \
    "v_cvt_pk_bf16_f32 v70, v44, v45\n\t" \
    "v_cvt_pk_bf16_f32 v71, v46, v47\n\t" \
    "s_waitcnt lgkmcnt(2)\n\t" \
    "v_mfma_f32_32x32x16_bf16 v[32:47], v[72:75], v[88:91], v[16:31]\n\t" \
    "ds_read_b128 v[76:79], v101 offset:7168\n\t" \
    "v_exp_f32 v48, v48\n\t" \
    "v_exp_f32 v49, v49\n\t" \
    "v_exp_f32 v50, v50\n\t" \
    "v_cvt_pk_bf16_f32 v64, v48, v49\n\t" \
    "v_exp_f32 v51, v51\n\t" \
    "v_exp_f32 v52, v52\n\t" \
    "v_cvt_pk_bf16_f32 v65, v50, v51\n\t" \
    "v_exp_f32 v53, v53\n\t" \
    "s_waitcnt lgkmcnt(2)\n\t" \
    "v_mfma_f32_32x32x16_bf16 v[0:15], v[84:87], v[68:71], v[0:15]\n\t" \
    "ds_read_b128 v[84:87], v103 offset:5632\n\t" \
    "v_exp_f32 v54, v54\n\t" \
    "v_cvt_pk_bf16_f32 v66, v52, v53\n\t" \
    "v_exp_f32 v55, v55\n\t" \
    "v_exp_f32 v56, v56\n\t" \
    "v_cvt_pk_bf16_f32 v67, v54, v55\n\t" \
    "v_exp_f32 v57, v57\n\t" \
    "v_exp_f32 v58, v58\n\t" \
    "s_waitcnt lgkmcnt(2)\n\t" \
    "v_mfma_f32_32x32x16_bf16 v[0:15], v[80:83], v[64:67], v[0:15]\n\t" \
    "ds_read_b128 v[80:83], v103 offset:6144\n\t" \
    "v_cvt_pk_bf16_f32 v68, v56, v57\n\t" \
    "v_exp_f32 v59, v59\n\t" \
    "v_exp_f32 v60, v60\n\t" \
    "v_cvt_pk_bf16_f32 v69, v58, v59\n\t" \
    "v_exp_f32 v61, v61\n\t" \
    "v_exp_f32 v62, v62\n\t" \
    "v_exp_f32 v63, v63\n\t" \
    "v_cvt_pk_bf16_f32 v70, v60, v61\n\t" \
    "v_cvt_pk_bf16_f32 v71, v62, v63\n\t" \
    "s_waitcnt lgkmcnt(2)\n\t" \
    "v_mfma_f32_32x32x16_bf16 v[48:63], v[76:79], v[88:91], v[16:31]\n\t" \
    "ds_read_b128 v[72:75], v102 offset:0\n\t" \
    "v_exp_f32 v32, v32\n\t" \
    "v_exp_f32 v33, v33\n\t" \
    "v_exp_f32 v34, v34\n\t" \
    "v_cvt_pk_bf16_f32 v64, v32, v33\n\t" \
    "v_exp_f32 v35, v35\n\t" \
    "v_exp_f32 v36, v36\n\t" \
    "v_cvt_pk_bf16_f32 v65, v34, v35\n\t" \
    "v_exp_f32 v37, v37\n\t" \
    "s_waitcnt lgkmcnt(2)\n\t" \
    "v_mfma_f32_32x32x16_bf16 v[0:15], v[84:87], v[68:71], v[0:15]\n\t" \
    "ds_read_b128 v[84:87], v103 offset:6656\n\t" \
    "v_exp_f32 v38, v38\n\t" \
    "v_cvt_pk_bf16_f32 v66, v36, v37\n\t" \
    "v_exp_f32 v39, v39\n\t" \
    "v_exp_f32 v40, v40\n\t" \
    "v_cvt_pk_bf16_f32 v67, v38, v39\n\t" \
    "v_exp_f32 v41, v41\n\t" \
    "v_exp_f32 v42, v42\n\t" \
    "s_waitcnt lgkmcnt(2)\n\t" \
    "v_mfma_f32_32x32x16_bf16 v[0:15], v[80:83], v[64:67], v[0:15]\n\t" \
    "ds_read_b128 v[80:83], v103 offset:7168\n\t" \
    "v_cvt_pk_bf16_f32 v68, v40, v41\n\t" \
    "v_exp_f32 v43, v43\n\t" \
    "v_exp_f32 v44, v44\n\t" \
    "v_cvt_pk_bf16_f32 v69, v42, v43\n\t" \
    "v_exp_f32 v45, v45\n\t" \
    "v_exp_f32 v46, v46\n\t" \
    "v_exp_f32 v47, v47\n\t" \
    "v_cvt_pk_bf16_f32 v70, v44, v45\n\t" \
    "v_cvt_pk_bf16_f32 v71, v46, v47\n\t" \
    "s_waitcnt lgkmcnt(2)\n\t" \
    "v_mfma_f32_32x32x16_bf16 v[32:47], v[72:75], v[88:91], v[16:31]\n\t" \
    "ds_read_b128 v[76:79], v102 offset:1024\n\t" \
    "v_exp_f32 v48, v48\n\t" \
    "v_exp_f32 v49, v49\n\t" \
    "v_exp_f32 v50, v50\n\t" \
    "v_cvt_pk_bf16_f32 v64, v48, v49\n\t" \
    "v_exp_f32 v51, v51\n\t" \
    "v_exp_f32 v52, v52\n\t" \
    "v_cvt_pk_bf16_f32 v65, v50, v51\n\t" \
    "v_exp_f32 v53, v53\n\t" \
    "s_waitcnt lgkmcnt(2)\n\t" \
    "v_mfma_f32_32x32x16_bf16 v[0:15], v[84:87], v[68:71], v[0:15]\n\t" \
    "ds_read_b128 v[84:87], v103 offset:7680\n\t" \
    "v_exp_f32 v54, v54\n\t" \
    "v_cvt_pk_bf16_f32 v66, v52, v53\n\t" \
    "v_exp_f32 v55, v55\n\t" \
    "v_exp_f32 v56, v56\n\t" \
    "v_cvt_pk_bf16_f32 v67, v54, v55\n\t" \
    "v_exp_f32 v57, v57\n\t" \
    "v_exp_f32 v58, v58\n\t" \
    "s_waitcnt lgkmcnt(2)\n\t" \
    "v_mfma_f32_32x32x16_bf16 v[0:15], v[80:83], v[64:67], v[0:15]\n\t" \
    "ds_read_b128 v[80:83], v104 offset:0\n\t" \
    "v_cvt_pk_bf16_f32 v68, v56, v57\n\t" \
    "v_exp_f32 v59, v59\n\t" \
    "v_exp_f32 v60, v60\n\t" \
    "v_cvt_pk_bf16_f32 v69, v58, v59\n\t" \
    "v_exp_f32 v61, v61\n\t" \
    "v_exp_f32 v62, v62\n\t" \
    "v_exp_f32 v63, v63\n\t" \
    "v_cvt_pk_bf16_f32 v70, v60, v61\n\t" \
    "v_cvt_pk_bf16_f32 v71, v62, v63\n\t" \
    "s_add_u32 %[c], %[c], 1\n\t" \
    "s_and_b32 %[t0], %[c], 3\n\t" \
    "s_lshl_b32 %[t0], %[t0], 14\n\t" \
    "s_add_u32 %[t1], %[c], 1\n\t" \
    "s_and_b32 %[t1], %[t1], 3\n\t" \
    "s_lshl_b32 %[t1], %[t1], 14\n\t" \
    "v_add_u32 v101, %[t0], v109\n\t" \
    "v_add_u32 v102, %[t1], v109\n\t" \
    "v_mad_u32_u24 v103, v107, %[t0], v106\n\t" \
    "v_mad_u32_u24 v104, v107, %[t1], v106\n\t" \
    "s_cmp_lt_u32 %[c], 8\n\t" \
    "s_cbranch_scc1 L_loop_%=\n\t" \
    "s_waitcnt lgkmcnt(0)\n\t" \
    "v_mfma_f32_32x32x16_bf16 v[0:15], v[84:87], v[68:71], v[0:15]\n\t" \
    "s_nop 15\n\t" \
    "v_fma_f32 v105, v0, 0, v8\n\t" \
    "v_fma_f32 v105, v1, 0, v105\n\t" \
    "v_fma_f32 v105, v2, 0, v105\n\t" \
    "v_fma_f32 v105, v3, 0, v105\n\t" \
    "v_fma_f32 v105, v4, 0, v105\n\t" \
    "v_fma_f32 v105, v5, 0, v105\n\t" \
    "v_fma_f32 v105, v6, 0, v105\n\t" \
    "v_fma_f32 v105, v7, 0, v105\n\t" \
    "v_cmp_ngt_f32 vcc, %[lim], v105\n\t" \
    "s_and_b64 vcc, vcc, %[chk]\n\t" \
    "s_cbranch_vccnz L_fail_%=\n\t" \
    "s_mov_b32 %[c], 0\n\t" \
    "s_branch L_done_%=\n\t" \
    "L_fail_%=:\n\t" \
    "s_waitcnt lgkmcnt(0)\n\t" \
    "s_nop 15\n\t" \
    "L_done_%=:\n\t" \
    ""
DI void attn_role(char* lds_, char* __restrict__ ws, int b) {
  constexpr int SLOT = 16384, CONSTS = 4 * SLOT;
  const int tid = threadIdx.x, lane = tid & 63;
  const int w = rfl(tid >> 6);
  const int head = (b & 7) * 4 + ((b >> 3) & 3), qb = b >> 5;
  const unsigned lds0 = (unsigned)(uintptr_t)lds_;
  const __amdgpu_buffer_rsrc_t rk = mk_rsrc(ws + WS_K + (size_t)head * 65536, 65536);
  const __amdgpu_buffer_rsrc_t rv = mk_rsrc(ws + WS_V + (size_t)head * 65536, 65536);
  const unsigned vl = lane * 16;
  auto issue = [&](int c) {
    const unsigned slot = lds0 + (c & 3) * SLOT;
    dma16(rk, vl, (unsigned)(c * 8192 + w * 1024), slot + w * 1024);
    dma16(rv, vl, (unsigned)(c * 8192 + w * 1024), slot + 8192 + w * 1024);
  };
  const bf16x8 qf = *(const bf16x8*)(ws + WS_Q + ((size_t)(head * 64 + qb * 8 + w) * 64 + lane) * 16);
  snop4();
  issue(0); issue(1); issue(2);
  {
    const unsigned v = ((tid & 31) == 0 || (tid & 31) == 4) ? 0x3f803f80u : 0u;
    *(u32x4*)(lds_ + CONSTS + tid * 16) = u32x4{v, v, v, v};
  }
  const int r32 = lane & 31;
  const int h = lane >> 5;
  const unsigned isdata = r32 < 16 ? 1u : 0u;
  const unsigned coff = (r32 == 16) ? 0u : (r32 == 20) ? 64u : (r32 > 20 && r32 < 28) ? 80u : 16u;
  const unsigned vbase = lds0 + (isdata ? (unsigned)(8192 + h * 256 + r32 * 16) : (unsigned)CONSTS + coff);
  const unsigned kb = lds0 + vl;
  lds_cptr L = (lds_cptr)lds_;
  f32x16 O, C;
#pragma unroll
  for (int r = 0; r < 16; ++r) { O[r] = 0.f; C[r] = 0.f; }
  float mref;
  wait_bar<4>();
  {
    float mx = -INFINITY;
#pragma unroll
    for (int t = 0; t < 4; ++t) {
      const f32x16 s0 = MFMA(lds_rd128(L + t * 1024 + lane * 16), qf, C);
#pragma unroll
      for (int r = 0; r < 16; r += 2) mx = fmaxf(mx, fmaxf(s0[r], s0[r + 1]));
    }
    mx = fmaxf(mx, __shfl_xor(mx, 32));
    mref = mx + ATT_BIAS;
#pragma unroll
    for (int r = 0; r < 16; ++r) C[r] = -mref;
  }
  const unsigned w1024 = w * 1024, ldsw = lds0 + w * 1024;
  unsigned long long chk = ~0ull;
  int lastfail = -1;
  unsigned c0 = 0;
  while (true) {
    unsigned c = c0, t0, t1;
    f32x4 cka, ckb; float ckc;
    asm volatile(ATTN_ASM
                 : "+{v[0:15]}"(O), [c] "+s"(c), "=&{v[92:95]}"(cka), "=&{v[96:99]}"(ckb), "=&{v100}"(ckc),
                   [t0] "=&s"(t0), [t1] "=&s"(t1)
                 : "{v[16:31]}"(C), "{v[88:91]}"(qf), "{v106}"(vbase), "{v107}"(isdata), "{v108}"(vl), "{v109}"(kb),
                   [rk] "s"(rk), [rv] "s"(rv), [lim] "s"(ATT_LIM), [w1024] "s"(w1024), [ldsw] "s"(ldsw), [chk] "s"(chk)
                 : "v32", "v33", "v34", "v35", "v36", "v37", "v38", "v39", "v40", "v41", "v42", "v43", "v44", "v45", "v46", "v47",
                   "v48", "v49", "v50", "v51", "v52", "v53", "v54", "v55", "v56", "v57", "v58", "v59", "v60", "v61", "v62", "v63",
                   "v64", "v65", "v66", "v67", "v68", "v69", "v70", "v71", "v72", "v73", "v74", "v75", "v76", "v77", "v78", "v79",
                   "v80", "v81", "v82", "v83", "v84", "v85", "v86", "v87", "v101", "v102", "v103", "v104", "v105",
                   "vcc", "scc", "memory");
    if (__builtin_expect(c == 0, 1)) break;
    const int cf = (int)c - 1;
    if (cf == lastfail) {
      chk = 0ull;
      if (c >= 8) break;
      c0 = c;
      continue;
    }
    lastfail = cf;
    lds_cptr kp = L + (cf & 3) * SLOT + lane * 16;
    lds_cptr vp = L + ((vbase - lds0) + (isdata ? (unsigned)((cf & 3) * SLOT) : 0u));
    f32x16 Z;
#pragma unroll
    for (int r = 0; r < 16; ++r) Z[r] = 0.f;
    float mx = -INFINITY;
#pragma unroll 1
    for (int t = 0; t < 8; ++t) {
      const f32x16 s = MFMA(lds_rd128(kp + t * 1024), qf, Z);
#pragma unroll
      for (int r = 0; r < 16; ++r) mx = fmaxf(mx, s[r]);
    }
    mx = fmaxf(mx, __shfl_xor(mx, 32));
    const float mnew = fmaxf(mref, mx + ATT_BIAS);
    const float sc = __builtin_amdgcn_exp2f(mref - mnew);
    mref = mnew;
#pragma unroll
    for (int r = 0; r < 16; ++r) { C[r] = -mref; O[r] = 0.f; }
#pragma unroll
    for (int r = 0; r < 4; ++r) { O[r] = cka[r] * sc; O[4 + r] = ckb[r] * sc; }
    O[8] = ckc * sc;
#pragma unroll 1
    for (int t = 0; t < 8; ++t) {
      const f32x16 s = MFMA(lds_rd128(kp + t * 1024), qf, C);
      u32x4 p0, p1;
#pragma unroll
      for (int j = 0; j < 4; ++j) {
        p0[j] = cvtpk(__builtin_amdgcn_exp2f(s[2 * j]), __builtin_amdgcn_exp2f(s[2 * j + 1]));
        p1[j] = cvtpk(__builtin_amdgcn_exp2f(s[8 + 2 * j]), __builtin_amdgcn_exp2f(s[8 + 2 * j + 1]));
      }
      const bf16x8 v0 = lds_rd128(vp + t * 1024), v1 = lds_rd128(vp + t * 1024 + 512);
      O = MFMA(v0, __builtin_bit_cast(bf16x8, p0), O);
      O = MFMA(v1, __builtin_bit_cast(bf16x8, p1), O);
    }
    c0 = (unsigned)cf + 1;
    if (c0 >= 8) break;
  }
  const float rl = __builtin_amdgcn_rcpf(O[8]);
  u32x4 o;
#pragma unroll
  for (int j = 0; j < 4; ++j) o[j] = cvtpk(O[2 * j] * rl, O[2 * j + 1] * rl);
  *(u32x4*)(ws + WS_O + ((size_t)((qb * 8 + w) * 32 + head) * 64 + lane) * 16) = o;
}

__global__ __launch_bounds__(512, 4) void k2_fused(const u16* __restrict__ Wg, const u16* __restrict__ Wu,
                                                   const u16* __restrict__ Wd, const u16* __restrict__ Wo,
                                                   char* __restrict__ ws) {
  __shared__ __attribute__((aligned(16))) char lds_[K2_LDS];
  DG_BEGIN();
  if (blockIdx.x < 256) { attn_role(lds_, ws, blockIdx.x); DG_END(1); }
  else { mlp_role(lds_, Wg, Wu, Wd, Wo, ws, blockIdx.x - 256); DG_END(3); }
}

#define WO_LA 4
__global__ __launch_bounds__(512, 2) void k3_wo(const u16* __restrict__ W, char* __restrict__ ws,
                                                const u16* __restrict__ x, u16* __restrict__ out) {
  constexpr int STG = 16384;
  __shared__ __attribute__((aligned(16))) char lds_[8 * STG];
  DG_BEGIN();
  const int xc = blockIdx.x & 7, bi = blockIdx.x >> 3;
  const int mb = xc * 4 + (bi & 3), nb = bi >> 2;
  const int tid = threadIdx.x, lane = tid & 63;
  const int w = rfl(tid >> 6);
  const unsigned lds0 = (unsigned)(uintptr_t)lds_;
  if (w >= 4) {
    const __amdgpu_buffer_rsrc_t rt = mk_rsrc(ws + WS_O, MTILES * KS_D * 1024);
    const __amdgpu_buffer_rsrc_t rw = mk_rsrc(W, D * D * 2);
    const unsigned vt = lane * 16, vw = e4_vw(lane);
    const int j0 = (w - 4) * 2;
    auto issue = [&](int st) {
#pragma unroll
      for (int j = 0; j < 2; ++j) {
        const int i = j0 + j;
        dma16(rt, vt, (unsigned)(((mb * 2 + (i >> 2)) * KS_D + st * 4 + (i & 3)) * 1024), lds0 + st * STG + i * 1024);
        dma16(rw, vw, (unsigned)((st * 64 + i * 8) * 1024 + nb * 128), lds0 + st * STG + 8192 + i * 1024);
      }
    };
    snop4();
    constexpr int LA = WO_LA;
#pragma unroll
    for (int st = 0; st < LA; ++st) issue(st);
#pragma unroll
    for (int st = 0; st < 8; ++st) {
      const int later = (st + LA <= 8 ? LA : 8 - st) - 1;
      switch (later) { case 7: wait_bar<28>(); break; case 6: wait_bar<24>(); break; case 5: wait_bar<20>(); break; case 4: wait_bar<16>(); break;
                       case 3: wait_bar<12>(); break; case 2: wait_bar<8>(); break; case 1: wait_bar<4>(); break; default: wait_bar<0>(); }
      if (st + LA < 8) issue(st + LA);
    }
    DG_END(4);
    return;
  }
  const int mt = w & 1, nt = w >> 1;
  const int mtg = mb * 2 + mt, ct = nb * 2 + nt, h = lane >> 5;
  const int token = mtg * 32 + (lane & 31);
  const u32x4* gp = (const u32x4*)(ws + WS_G + ((size_t)(mtg * 16 + ct) * 64 + lane) * 32);
  const u32x4 g0 = gp[0], g1 = gp[1];
  u32x2 xv[4];
#pragma unroll
  for (int g = 0; g < 4; ++g) xv[g] = *(const u32x2*)(x + (size_t)token * D + ct * 32 + 8 * g + 4 * h);
  f32x16 acc;
#pragma unroll
  for (int r = 0; r < 16; ++r) acc[r] = 0.f;
  const unsigned wbase = e4_wbase(lane, nt);
  lds_cptr L = (lds_cptr)lds_;
  E4Fr fa, fb;
  bar_plain();
  e4_rd(fa, L, lane, mt, wbase, 0); SBAR();
  e4_rd(fb, L, lane, mt, wbase, 1); SBAR();
  e4_mm(fa, acc); SBAR();
#pragma unroll
  for (int st = 1; st < 8; ++st) {
    bar_plain();
    lds_cptr sl = L + st * STG;
    e4_rd(fa, sl, lane, mt, wbase, 0); SBAR();
    e4_mm(fb, acc); SBAR();
    e4_rd(fb, sl, lane, mt, wbase, 1); SBAR();
    e4_mm(fa, acc); SBAR();
  }
  e4_mm(fb, acc);
#pragma unroll
  for (int g = 0; g < 4; ++g) {
    const unsigned ga = (g < 2 ? g0 : g1)[(g & 1) * 2], gb = (g < 2 ? g0 : g1)[(g & 1) * 2 + 1];
    u32x2 o;
    o[0] = cvtpk(bf_lo(xv[g][0]) + acc[4 * g] * bf_lo(ga), bf_hi(xv[g][0]) + acc[4 * g + 1] * bf_hi(ga));
    o[1] = cvtpk(bf_lo(xv[g][1]) + acc[4 * g + 2] * bf_lo(gb), bf_hi(xv[g][1]) + acc[4 * g + 3] * bf_hi(gb));
    *(u32x2*)(out + (size_t)token * D + ct * 32 + 8 * g + 4 * h) = o;
  }
  DG_END(4);
}

extern "C" void kernel_launch(void* const* d_in, const int* in_sizes, int n_in, void* d_out, int out_size,
                              void* d_ws, size_t ws_size, hipStream_t stream) {
  const u16* x = (const u16*)d_in[0];
  const u16* wqkv = (const u16*)d_in[1];
  const u16* wo = (const u16*)d_in[2];
  const u16* wg = (const u16*)d_in[4];
  const u16* wu = (const u16*)d_in[5];
  const u16* wd = (const u16*)d_in[6];
  char* ws = (char*)d_ws;
  u16* out = (u16*)d_out;
  k1_qkv<<<256, 768, 0, stream>>>(x, wqkv, ws);
  k2_fused<<<512, 512, 0, stream>>>(wg, wu, wd, wo, ws);
  k3_wo<<<256, 512, 0, stream>>>(wo, ws, x, out);
}
```

```cpp
#include <hip/hip_runtime.h>
#include <hip/hip_bf16.h>
#include <stdint.h>
#include <type_traits>

typedef __attribute__((ext_vector_type(8))) short bf16x8;
typedef __attribute__((ext_vector_type(4))) short s16x4;
typedef __attribute__((ext_vector_type(16))) float f32x16;
typedef __attribute__((ext_vector_type(4))) float f32x4;
typedef __attribute__((ext_vector_type(2))) float f32x2;
typedef __attribute__((ext_vector_type(4))) unsigned u32x4;
typedef __attribute__((ext_vector_type(2))) unsigned u32x2;
typedef __attribute__((ext_vector_type(2))) __bf16 bf16x2_t;
typedef short v4i16_t __attribute__((ext_vector_type(4)));
typedef unsigned short u16;
#define LDSP(T) __attribute__((address_space(3))) T
typedef LDSP(const char)* lds_cptr;
typedef __attribute__((address_space(1))) unsigned gu32;

#define DI __device__ __forceinline__
#define G2_SLP 1
#define G2_PRIO 3
#define PACE() do { if (G2_SLP > 0) { SBAR(); __builtin_amdgcn_s_sleep(G2_SLP); SBAR(); } } while (0)
#define G2_PER 1
#define G2_FIRST 1
#define K2_CODE_PF 1
#define K1_NBATCH 736
#define K1_TOUCH 0
#define SBAR() __builtin_amdgcn_sched_barrier(0)
#define TOUCH 1
#define SLP1 0
#define SLP2 0
#define PACE_G1() do { if (SLP1 > 0) { SBAR(); __builtin_amdgcn_s_sleep(SLP1); SBAR(); } } while (0)
#define PACE_G2() do { if (SLP2 > 0) { SBAR(); __builtin_amdgcn_s_sleep(SLP2); SBAR(); } } while (0)
#define MFMA(a, b, c) __builtin_amdgcn_mfma_f32_32x32x16_bf16((a), (b), (c), 0, 0, 0)

constexpr int S = 2048, D = 512, FF = 1792;
constexpr int MTILES = S / 32;
constexpr int KS_D = D / 16;
constexpr int KS_F = FF / 16;
constexpr unsigned MB = 1024 * 1024;
constexpr size_t WS_XN = 0;
constexpr size_t WS_Q = 2 * MB;
constexpr size_t WS_K = 4 * MB;
constexpr size_t WS_V = 6 * MB;
constexpr size_t WS_O = 8 * MB;
constexpr size_t WS_G = 10 * MB;
constexpr size_t WS_H = 12 * MB;
constexpr size_t WS_CNT = 20 * MB;
constexpr int CN_QKV = 0;
constexpr int CN_XN = 16;
constexpr int CN_PZ = 32;
constexpr int CN_H = 160;
constexpr int CN_TGU = 224;
constexpr int CN_CS = 224;
constexpr size_t WS_CS = 0;
constexpr int CN_TD = 238;
constexpr int CN_TO = 239;
constexpr int CN_FIN = 240;
constexpr int CN_NUM = 241;
constexpr int CN_FLAG = 248;
constexpr unsigned long long INIT_MAGIC = 0x9e3779b97f4a7c15ull;
constexpr size_t WS_TGU = 21 * MB;
constexpr size_t WS_TD = 25 * MB;
constexpr size_t WS_TO = 27 * MB;

DI unsigned cvtpk(float lo, float hi) {
  f32x2 v = {lo, hi};
  bf16x2_t b = __builtin_convertvector(v, bf16x2_t);
  return __builtin_bit_cast(unsigned, b);
}
DI float bf_lo(unsigned u) { return __uint_as_float(u << 16); }
DI float bf_hi(unsigned u) { return __uint_as_float(u & 0xffff0000u); }
template <int CTRL, int RM> DI float dpp_add(float v) {
  return v + __builtin_bit_cast(float, __builtin_amdgcn_update_dpp(0, __builtin_bit_cast(int, v), CTRL, RM, 0xf, false));
}
DI float wave_sum(float v) {
  v = dpp_add<0x111, 0xf>(v); v = dpp_add<0x112, 0xf>(v); v = dpp_add<0x114, 0xf>(v); v = dpp_add<0x118, 0xf>(v);
  v = dpp_add<0x142, 0xa>(v); v = dpp_add<0x143, 0xc>(v);
  return __builtin_bit_cast(float, __builtin_amdgcn_readlane(__builtin_bit_cast(int, v), 63));
}
DI unsigned rfl(unsigned x) { return __builtin_amdgcn_readfirstlane(x); }
DI int lane_fresh() {
  unsigned l;
  asm volatile("v_mbcnt_lo_u32_b32 %0, -1, 0\n\tv_mbcnt_hi_u32_b32 %0, -1, %0" : "=v"(l));
  return (int)l;
}
DI __amdgpu_buffer_rsrc_t mk_rsrc(const void* p, unsigned bytes) {
  return __builtin_amdgcn_make_buffer_rsrc((void*)p, (short)0, (int)bytes, 0x00020000);
}
DI void dma16(__amdgpu_buffer_rsrc_t rsrc, unsigned voff, unsigned soff, unsigned lds_addr) {
  asm volatile("s_mov_b32 m0, %0\n\ts_nop 0\n\tbuffer_load_dwordx4 %1, %2, %3 offen lds"
               :: "s"(lds_addr), "v"(voff), "s"(rsrc), "s"(soff) : "memory");
}
DI void dma16x4(__amdgpu_buffer_rsrc_t rsrc, unsigned voff, unsigned s0, unsigned s1, unsigned s2, unsigned s3, unsigned lds_addr) {
  asm volatile("s_mov_b32 m0, %0\n\ts_nop 0\n\t"
               "buffer_load_dwordx4 %1, %2, %3 offen lds\n\t"
               "buffer_load_dwordx4 %1, %2, %4 offen offset:1024 lds\n\t"
               "buffer_load_dwordx4 %1, %2, %5 offen offset:2048 lds\n\t"
               "buffer_load_dwordx4 %1, %2, %6 offen offset:3072 lds"
               :: "s"(lds_addr), "v"(voff), "s"(rsrc), "s"(s0 + 4096u), "s"(s1 + 3072u), "s"(s2 + 2048u), "s"(s3 + 1024u) : "memory");
}
DI void dma16x4e(__amdgpu_buffer_rsrc_t rsrc, unsigned v0, unsigned v1, unsigned s0, unsigned s1, unsigned s2, unsigned s3, unsigned lds_addr) {
  asm volatile("s_mov_b32 m0, %0\n\ts_nop 0\n\t"
               "buffer_load_dwordx4 %1, %3, %4 offen lds\n\t"
               "buffer_load_dwordx4 %2, %3, %5 offen offset:1024 lds\n\t"
               "buffer_load_dwordx4 %1, %3, %6 offen offset:2048 lds\n\t"
               "buffer_load_dwordx4 %2, %3, %7 offen offset:3072 lds"
               :: "s"(lds_addr), "v"(v0), "v"(v1), "s"(rsrc), "s"(s0 + 4096u), "s"(s1 + 3072u), "s"(s2 + 2048u), "s"(s3 + 1024u) : "memory");
}
DI void dma16x2(__amdgpu_buffer_rsrc_t rsrc, unsigned voff, unsigned s0, unsigned s1, unsigned lds_addr) {
  asm volatile("s_mov_b32 m0, %0\n\ts_nop 0\n\t"
               "buffer_load_dwordx4 %1, %2, %3 offen lds\n\t"
               "buffer_load_dwordx4 %1, %2, %4 offen offset:1024 lds"
               :: "s"(lds_addr), "v"(voff), "s"(rsrc), "s"(s0 + 4096u), "s"(s1 + 3072u) : "memory");
}
DI __amdgpu_buffer_rsrc_t mk_rsrc_x(const void* p, unsigned bytes) {
  return __builtin_amdgcn_make_buffer_rsrc((void*)((const char*)p - 4096), (short)0, (int)(bytes + 4096u), 0x00020000);
}
DI void bar_plain() { asm volatile("s_barrier" ::: "memory"); }
DI void touch(__amdgpu_buffer_rsrc_t rsrc, unsigned voff, unsigned lds_addr) {
  asm volatile("s_mov_b32 m0, %0\n\ts_nop 0\n\tbuffer_load_dword %1, %2, 0 offen lds"
               :: "s"(lds_addr), "v"(voff), "s"(rsrc) : "memory");
}
#define ST_AUX 16
DI void st16(__amdgpu_buffer_rsrc_t r, unsigned off, u32x4 v) { __builtin_amdgcn_raw_buffer_store_b128(v, r, off, 0, ST_AUX); }
#define QKV_AUX 16
DI void st16q(__amdgpu_buffer_rsrc_t r, unsigned off, u32x4 v) { __builtin_amdgcn_raw_buffer_store_b128(v, r, off, 0, QKV_AUX); }
DI void st8(__amdgpu_buffer_rsrc_t r, unsigned off, u32x2 v) { __builtin_amdgcn_raw_buffer_store_b64(v, r, off, 0, ST_AUX); }
DI void snop4() { asm volatile("s_nop 4" ::: "memory"); }
template <int N> DI void wait_bar() {
  asm volatile("s_waitcnt vmcnt(%0) lgkmcnt(0)\n\ts_barrier" :: "i"(N) : "memory");
}
template <int PER, int N> DI void wait_tail() {
  if constexpr (N > 0) { wait_bar<PER * (N - 1)>(); wait_tail<PER, N - 1>(); }
}
DI void bar_only() { asm volatile("s_waitcnt lgkmcnt(0)\n\ts_barrier" ::: "memory"); }
DI bf16x8 lds_rd128(lds_cptr p) { return *(LDSP(const bf16x8)*)p; }
DI s16x4 lds_tr(lds_cptr p) {
  return __builtin_bit_cast(s16x4, __builtin_amdgcn_ds_read_tr16_b64_v4i16((LDSP(v4i16_t)*)p));
}
DI bf16x8 cat8(s16x4 a, s16x4 b) { return __builtin_shufflevector(a, b, 0, 1, 2, 3, 4, 5, 6, 7); }

#define DG_BEGIN()
#define DG_END(kid)
#define DG_MARK(kid)

#define DBG(wv, i)
#define PCS 0
#define CS_B 16
#define K1EPI 1
#define AREF 1
#define W128 1
#define K1_NSL 4
constexpr int K1_STG = 28 * 1024, K1_NS = K1_NSL, K1_PT = K1_NS * K1_STG;
struct WPtrs { const u16 *wg, *wu, *wd, *wo; };
DI void retile_issue(const WPtrs& P, int id, int lane, unsigned ldsb) {
  if (id < 448) {
    const int ks = id / 14, ng = id % 14;
    const __amdgpu_buffer_rsrc_t rg = mk_rsrc_x(P.wg, D * FF * 2), ru = mk_rsrc_x(P.wu, D * FF * 2);
    const int drow = lane >> 4, pos = (lane >> 1) & 7, half = lane & 1;
    const unsigned vg = drow * (FF * 2) + (pos ^ (2 * drow)) * 32 + half * 16;
    const unsigned vu = drow * (FF * 2) + (pos ^ (2 * drow + 1)) * 32 + half * 16;
    const unsigned so = (unsigned)(ks * 16 * (FF * 2) + ng * 256);
    snop4();
    dma16x4(rg, vg, so, so + 4 * FF * 2, so + 8 * FF * 2, so + 12 * FF * 2, ldsb);
    dma16x4(ru, vu, so, so + 4 * FF * 2, so + 8 * FF * 2, so + 12 * FF * 2, ldsb + 4096);
  } else {
    const bool isd = id < 672;
    const int j = isd ? id - 448 : id - 672, ks = j >> 1, hf = j & 1;
    const __amdgpu_buffer_rsrc_t r = isd ? mk_rsrc_x(P.wd, FF * D * 2) : mk_rsrc_x(P.wo, D * D * 2);
    const int rl = lane >> 5, pos = (lane >> 1) & 15, half = lane & 1;
    const unsigned v0 = rl * 1024 + (pos ^ (2 * rl)) * 32 + half * 16;
    const unsigned v1 = rl * 1024 + (pos ^ (2 * (2 + rl))) * 32 + half * 16;
    const unsigned so = (unsigned)(ks * 16 * 1024 + hf * 512);
    snop4();
    dma16x4e(r, v0, v1, so, so + 2048, so + 4096, so + 6144, ldsb);
    dma16x4e(r, v0, v1, so + 8192, so + 10240, so + 12288, so + 14336, ldsb + 4096);
  }
}
DI void retile_store(char* ws, int id, int lane, lds_cptr lb) {
  const __amdgpu_buffer_rsrc_t rws = mk_rsrc(ws, 32u * MB);
  const int h = lane >> 5, i16 = lane & 15, q4 = i16 >> 2, p4 = i16 & 3, blk = (lane >> 4) & 1;
  if (id < 448) {
    const int ks = id / 14, ng = id % 14;
    lds_cptr base = lb + 4096 * blk + (8 * h + q4) * 256 + p4 * 8;
    const unsigned dst = (unsigned)(WS_TGU + ((ng * 8) * 32 + ks) * 1024 + lane * 16);
#pragma unroll
    for (int n = 0; n < 8; ++n) {
      lds_cptr p = base + ((n ^ (2 * q4 + blk)) & 7) * 32;
      st16(rws, dst + n * 32 * 1024, __builtin_bit_cast(u32x4, cat8(lds_tr(p), lds_tr(p + 1024))));
    }
  } else {
    const bool isd = id < 672;
    const int j = isd ? id - 448 : id - 672, ks = j >> 1, hf = j & 1;
    const int nks = isd ? KS_F : KS_D;
    lds_cptr base = lb + (4 * h + q4) * 512 + blk * 32 + p4 * 8;
    const unsigned dst = (unsigned)((isd ? WS_TD : WS_TO) + ((hf * 8) * nks + ks) * 1024 + lane * 16);
#pragma unroll
    for (int n = 0; n < 8; ++n) {
      lds_cptr p = base + (n ^ q4) * 64;
      st16(rws, dst + n * nks * 1024, __builtin_bit_cast(u32x4, cat8(lds_tr(p), lds_tr(p + 4096))));
    }
  }
}
#define TS(i)
#define K1ST(i)
#define K1CY(i)
#define G1MAP 1
#define X1_SLP 0
#define WC_SLP 1
#define NBQ_X 0
#define HP_PRIO 3
#define CODE_PF 1
#define CODE_PF_W 7
#define WPRE 1
#define XPRE 1
#define K1_ROTK 0
#define G1_ROTK 1
#define ROT_OF(kind) ((kind) == 0 ? ((blockIdx.x >> 3) & 7) : (kind) == 1 ? ((2 * ((blockIdx.x >> 3) & 3) + (blockIdx.x >> 5)) & 7) : 0)
#define HB(N) do { if (N == 63) bar_only(); else wait_bar<(N == 63 ? 0 : N)>(); } while (0)
constexpr int GPER = 12;
#define K1_LA 2
struct CSV { u32x4 v[16]; };
DI void colsum_issue(CSV& c, const WPtrs& P, int job, int lane) {
  const int rg = job / 28, cbm = job - rg * 28, mat = cbm >= 14 ? 1 : 0, cb = cbm - 14 * mat;
  const unsigned long long ag = (unsigned long long)P.wg, au = (unsigned long long)P.wu;
  const unsigned long long aw = mat ? au : ag;
  const __amdgpu_buffer_rsrc_t r = mk_rsrc((const void*)(((unsigned long long)rfl((unsigned)(aw >> 32)) << 32) | rfl((unsigned)aw)), D * FF * 2);
  const unsigned voff = (unsigned)((rg * 64 + (lane >> 4)) * (FF * 2) + cb * 256 + (lane & 15) * 16);
#pragma unroll
  for (int i = 0; i < 16; ++i)
    asm volatile("buffer_load_dwordx4 %0, %1, %2, %3 offen" : "=v"(c.v[i]) : "v"(voff), "s"(r), "s"((unsigned)(i * 4 * (FF * 2))) : "memory");
}
DI void colsum_store(CSV& c, char* ws, int job, int lane) {
  const int rg = job / 28, cbm = job - rg * 28;
  float s[8];
#pragma unroll
  for (int e = 0; e < 8; ++e) s[e] = 0.f;
#pragma unroll
  for (int i = 0; i < 16; ++i) {
    asm volatile("" : "+v"(c.v[i]));
#pragma unroll
    for (int e = 0; e < 4; ++e) { s[2 * e] += __uint_as_float(c.v[i][e] << 16); s[2 * e + 1] += __uint_as_float(c.v[i][e] & 0xffff0000u); }
  }
#pragma unroll
  for (int e = 0; e < 8; ++e) { s[e] += __shfl_xor(s[e], 16); s[e] += __shfl_xor(s[e], 32); }
  const __amdgpu_buffer_rsrc_t rws = mk_rsrc(ws, 32u * MB);
  const unsigned dst = lane < 16 ? (unsigned)(WS_CS + ((cbm * 128 + lane * 8) * 8 + rg) * 4) : 0x7fff0000u;
#pragma unroll
  for (int e = 0; e < 8; ++e) __builtin_amdgcn_raw_buffer_store_b32(__float_as_uint(s[e]), rws, dst + e * 32, 0, ST_AUX);
}
template <int PER, class F, class G4> DI void k1_loader_loop(F&& issue, G4&& post4, bool has_g1, const WPtrs& P, int id, int lane, unsigned ptb, char* dws, CSV& cs, int wgi) {
  const int dg_w = 8 + (id >> 8); (void)dg_w;
  snop4();
  const bool initw = blockIdx.x == 0 && id == 256;
  if (initw) {
    const __amdgpu_buffer_rsrc_t rz = mk_rsrc(dws + WS_CNT, CN_NUM * 256);
#pragma unroll
    for (int i = 0; i < 4; ++i) __builtin_amdgcn_raw_buffer_store_b32(0u, rz, (unsigned)((i * 64 + lane) * 256), 0, 16);
    snop4();
  }
#define INIT_FLAG() do { if (initw && lane == 0) __hip_atomic_store((__attribute__((address_space(1))) unsigned long long*)(dws + WS_CNT + CN_FLAG * 256), INIT_MAGIC, __ATOMIC_RELAXED, __HIP_MEMORY_SCOPE_AGENT); } while (0)
  K1ST(0);
  const bool csj = PCS && has_g1 && PER == 8 && (id >> 8) == 0;
#define ISS() issue()
#define K1_UP 3
  (void)P; (void)id; (void)ptb;
  { char* ws = dws; (void)ws; DBG(dg_w, 10); }
  issue(); issue(); issue();
  K1ST(1);
  { char* ws = dws; (void)ws; DBG(dg_w, 11); }
  wait_bar<PER * 2>(); INIT_FLAG(); issue();
  { char* ws = dws; (void)ws; DBG(dg_w, 12); }
  wait_bar<PER * 2>();
  wait_bar<PER>(); ISS();
  wait_bar<PER>(); ISS();
  if (csj) {
    colsum_issue(cs, P, (int)blockIdx.x, lane);
    wait_bar<PER + 16>(); ISS();
    wait_bar<PER + 16>(); ISS();
  } else {
    wait_bar<PER>(); ISS();
    wait_bar<PER>(); ISS();
  }
  if (has_g1) {
    post4();
    wait_bar<PER + GPER>();
    wait_bar<GPER>();
  } else {
    wait_bar<PER>();
    wait_bar<0>();
  }
}
#define K1_ASM_0 \
    "s_barrier\n\t" \
    "ds_read_b128 v[48:51], v112 offset:0\n\t" \
    "ds_read_b64_tr_b16 v[64:65], v116 offset:0\n\t" \
    "ds_read_b64_tr_b16 v[66:67], v116 offset:512\n\t" \
    "ds_read_b64_tr_b16 v[80:81], v120 offset:0\n\t" \
    "ds_read_b64_tr_b16 v[82:83], v120 offset:256\n\t" \
    "ds_read_b128 v[52:55], v113 offset:0\n\t" \
    "ds_read_b64_tr_b16 v[68:69], v117 offset:0\n\t" \
    "ds_read_b64_tr_b16 v[70:71], v117 offset:512\n\t" \
    "ds_read_b64_tr_b16 v[84:85], v121 offset:0\n\t" \
    "ds_read_b64_tr_b16 v[86:87], v121 offset:256\n\t" \
    "ds_read_b128 v[56:59], v114 offset:0\n\t" \
    "ds_read_b64_tr_b16 v[72:73], v118 offset:0\n\t" \
    "ds_read_b64_tr_b16 v[74:75], v118 offset:512\n\t" \
    "ds_read_b128 v[60:63], v115 offset:0\n\t" \
    "ds_read_b64_tr_b16 v[76:77], v119 offset:0\n\t" \
    "ds_read_b64_tr_b16 v[78:79], v119 offset:512\n\t" \
    "s_barrier\n\t" \
    "s_waitcnt lgkmcnt(13)\n\t" \
    "v_mfma_f32_32x32x16_bf16 v[0:15], v[64:67], v[48:51], v[0:15]\n\t" \
    "v_lshlrev_b32 v104, 16, v48\n\t" \
    "v_and_b32 v105, 0xffff0000, v48\n\t" \
    "v_add_f32 v96, v96, v104\n\t" \
    "v_fmac_f32 v100, v104, v104\n\t" \
    "v_add_f32 v96, v96, v105\n\t" \
    "v_fmac_f32 v100, v105, v105\n\t" \
    "v_mfma_f32_32x32x16_bf16 v[32:47], v[64:67], v[88:91], v[32:47]\n\t" \
    "ds_read_b64_tr_b16 v[64:65], v116 offset:28672\n\t" \
    "ds_read_b64_tr_b16 v[66:67], v116 offset:29184\n\t" \
    "v_lshlrev_b32 v106, 16, v49\n\t" \
    "v_and_b32 v107, 0xffff0000, v49\n\t" \
    "v_add_f32 v97, v97, v106\n\t" \
    "v_fmac_f32 v101, v106, v106\n\t" \
    "v_add_f32 v97, v97, v107\n\t" \
    "v_fmac_f32 v101, v107, v107\n\t" \
    "s_waitcnt lgkmcnt(13)\n\t" \
    "v_mfma_f32_32x32x16_bf16 v[16:31], v[80:83], v[48:51], v[16:31]\n\t" \
    "v_lshlrev_b32 v108, 16, v50\n\t" \
    "v_and_b32 v109, 0xffff0000, v50\n\t" \
    "v_add_f32 v98, v98, v108\n\t" \
    "v_fmac_f32 v102, v108, v108\n\t" \
    "v_add_f32 v98, v98, v109\n\t" \
    "v_fmac_f32 v102, v109, v109\n\t" \
    "s_cmp_lg_u32 s40, 0\n\t" \
    "s_cbranch_scc1 L_nocs0_%=\n\t" \
    "v_mfma_f32_32x32x16_bf16 v[32:47], v[80:83], v[92:95], v[32:47]\n\t" \
    "L_nocs0_%=:\n\t" \
    "ds_read_b64_tr_b16 v[80:81], v120 offset:28672\n\t" \
    "ds_read_b64_tr_b16 v[82:83], v120 offset:28928\n\t" \
    "v_lshlrev_b32 v110, 16, v51\n\t" \
    "v_and_b32 v111, 0xffff0000, v51\n\t" \
    "v_add_f32 v99, v99, v110\n\t" \
    "v_fmac_f32 v103, v110, v110\n\t" \
    "v_add_f32 v99, v99, v111\n\t" \
    "v_fmac_f32 v103, v111, v111\n\t" \
    "ds_read_b128 v[48:51], v112 offset:28672\n\t" \
    "s_waitcnt lgkmcnt(13)\n\t" \
    "v_mfma_f32_32x32x16_bf16 v[0:15], v[68:71], v[52:55], v[0:15]\n\t" \
    "ds_read_b64_tr_b16 v[68:69], v117 offset:28672\n\t" \
    "ds_read_b64_tr_b16 v[70:71], v117 offset:29184\n\t" \
    "v_lshlrev_b32 v104, 16, v52\n\t" \
    "v_and_b32 v105, 0xffff0000, v52\n\t" \
    "v_add_f32 v96, v96, v104\n\t" \
    "v_fmac_f32 v100, v104, v104\n\t" \
    "v_add_f32 v96, v96, v105\n\t" \
    "v_fmac_f32 v100, v105, v105\n\t" \
    "s_waitcnt lgkmcnt(13)\n\t" \
    "v_mfma_f32_32x32x16_bf16 v[16:31], v[84:87], v[52:55], v[16:31]\n\t" \
    "ds_read_b64_tr_b16 v[84:85], v121 offset:28672\n\t" \
    "ds_read_b64_tr_b16 v[86:87], v121 offset:28928\n\t" \
    "v_lshlrev_b32 v106, 16, v53\n\t" \
    "v_and_b32 v107, 0xffff0000, v53\n\t" \
    "v_add_f32 v97, v97, v106\n\t" \
    "v_fmac_f32 v101, v106, v106\n\t" \
    "v_add_f32 v97, v97, v107\n\t" \
    "v_fmac_f32 v101, v107, v107\n\t" \
    "s_waitcnt lgkmcnt(12)\n\t" \
    "v_mfma_f32_32x32x16_bf16 v[0:15], v[72:75], v[56:59], v[0:15]\n\t" \
    "ds_read_b64_tr_b16 v[72:73], v118 offset:28672\n\t" \
    "ds_read_b64_tr_b16 v[74:75], v118 offset:29184\n\t" \
    "ds_read_b128 v[56:59], v114 offset:28672\n\t" \
    "v_lshlrev_b32 v108, 16, v54\n\t" \
    "v_and_b32 v109, 0xffff0000, v54\n\t" \
    "v_add_f32 v98, v98, v108\n\t" \
    "v_fmac_f32 v102, v108, v108\n\t" \
    "v_add_f32 v98, v98, v109\n\t" \
    "v_fmac_f32 v102, v109, v109\n\t" \
    "s_waitcnt lgkmcnt(12)\n\t" \
    "v_mfma_f32_32x32x16_bf16 v[0:15], v[76:79], v[60:63], v[0:15]\n\t" \
    "ds_read_b64_tr_b16 v[76:77], v119 offset:28672\n\t" \
    "ds_read_b64_tr_b16 v[78:79], v119 offset:29184\n\t" \
    "ds_read_b128 v[60:63], v115 offset:28672\n\t" \
    "v_lshlrev_b32 v110, 16, v55\n\t" \
    "v_and_b32 v111, 0xffff0000, v55\n\t" \
    "v_add_f32 v99, v99, v110\n\t" \
    "v_fmac_f32 v103, v110, v110\n\t" \
    "v_add_f32 v99, v99, v111\n\t" \
    "v_fmac_f32 v103, v111, v111\n\t" \
    "ds_read_b128 v[52:55], v113 offset:28672\n\t" \
    "s_barrier\n\t" \
    "v_add_u32 v112, 0xe000, v112\n\t" \
    "v_add_u32 v113, 0xe000, v113\n\t" \
    "v_add_u32 v114, 0xe000, v114\n\t" \
    "v_add_u32 v115, 0xe000, v115\n\t" \
    "v_add_u32 v116, 0xe000, v116\n\t" \
    "v_add_u32 v117, 0xe000, v117\n\t" \
    "v_add_u32 v118, 0xe000, v118\n\t" \
    "v_add_u32 v119, 0xe000, v119\n\t" \
    "v_add_u32 v120, 0xe000, v120\n\t" \
    "v_add_u32 v121, 0xe000, v121\n\t" \
    "s_waitcnt lgkmcnt(11)\n\t" \
    "v_mfma_f32_32x32x16_bf16 v[0:15], v[64:67], v[48:51], v[0:15]\n\t" \
    "v_lshlrev_b32 v104, 16, v48\n\t" \
    "v_and_b32 v105, 0xffff0000, v48\n\t" \
    "v_add_f32 v96, v96, v104\n\t" \
    "v_fmac_f32 v100, v104, v104\n\t" \
    "v_add_f32 v96, v96, v105\n\t" \
    "v_fmac_f32 v100, v105, v105\n\t" \
    "v_mfma_f32_32x32x16_bf16 v[32:47], v[64:67], v[88:91], v[32:47]\n\t" \
    "ds_read_b64_tr_b16 v[64:65], v116 offset:0\n\t" \
    "ds_read_b64_tr_b16 v[66:67], v116 offset:512\n\t" \
    "v_lshlrev_b32 v106, 16, v49\n\t" \
    "v_and_b32 v107, 0xffff0000, v49\n\t" \
    "v_add_f32 v97, v97, v106\n\t" \
    "v_fmac_f32 v101, v106, v106\n\t" \
    "v_add_f32 v97, v97, v107\n\t" \
    "v_fmac_f32 v101, v107, v107\n\t" \
    "v_mfma_f32_32x32x16_bf16 v[16:31], v[80:83], v[48:51], v[16:31]\n\t" \
    "v_lshlrev_b32 v108, 16, v50\n\t" \
    "v_and_b32 v109, 0xffff0000, v50\n\t" \
    "v_add_f32 v98, v98, v108\n\t" \
    "v_fmac_f32 v102, v108, v108\n\t" \
    "v_add_f32 v98, v98, v109\n\t" \
    "v_fmac_f32 v102, v109, v109\n\t" \
    "s_cmp_lg_u32 s40, 0\n\t" \
    "s_cbranch_scc1 L_nocs1_%=\n\t" \
    "v_mfma_f32_32x32x16_bf16 v[32:47], v[80:83], v[92:95], v[32:47]\n\t" \
    "L_nocs1_%=:\n\t" \
    "ds_read_b64_tr_b16 v[80:81], v120 offset:0\n\t" \
    "ds_read_b64_tr_b16 v[82:83], v120 offset:256\n\t" \
    "v_lshlrev_b32 v110, 16, v51\n\t" \
    "v_and_b32 v111, 0xffff0000, v51\n\t" \
    "v_add_f32 v99, v99, v110\n\t" \
    "v_fmac_f32 v103, v110, v110\n\t" \
    "v_add_f32 v99, v99, v111\n\t" \
    "v_fmac_f32 v103, v111, v111\n\t" \
    "ds_read_b128 v[48:51], v112 offset:0\n\t" \
    "s_waitcnt lgkmcnt(5)\n\t" \
    "v_mfma_f32_32x32x16_bf16 v[0:15], v[68:71], v[52:55], v[0:15]\n\t" \
    "ds_read_b64_tr_b16 v[68:69], v117 offset:0\n\t" \
    "ds_read_b64_tr_b16 v[70:71], v117 offset:512\n\t" \
    "v_lshlrev_b32 v104, 16, v52\n\t" \
    "v_and_b32 v105, 0xffff0000, v52\n\t" \
    "v_add_f32 v96, v96, v104\n\t" \
    "v_fmac_f32 v100, v104, v104\n\t" \
    "v_add_f32 v96, v96, v105\n\t" \
    "v_fmac_f32 v100, v105, v105\n\t" \
    "v_mfma_f32_32x32x16_bf16 v[16:31], v[84:87], v[52:55], v[16:31]\n\t" \
    "ds_read_b64_tr_b16 v[84:85], v121 offset:0\n\t" \
    "ds_read_b64_tr_b16 v[86:87], v121 offset:256\n\t" \
    "v_lshlrev_b32 v106, 16, v53\n\t" \
    "v_and_b32 v107, 0xffff0000, v53\n\t" \
    "v_add_f32 v97, v97, v106\n\t" \
    "v_fmac_f32 v101, v106, v106\n\t" \
    "v_add_f32 v97, v97, v107\n\t" \
    "v_fmac_f32 v101, v107, v107\n\t" \
    "v_mfma_f32_32x32x16_bf16 v[0:15], v[72:75], v[56:59], v[0:15]\n\t" \
    "ds_read_b64_tr_b16 v[72:73], v118 offset:0\n\t" \
    "ds_read_b64_tr_b16 v[74:75], v118 offset:512\n\t" \
    "ds_read_b128 v[56:59], v114 offset:0\n\t" \
    "v_lshlrev_b32 v108, 16, v54\n\t" \
    "v_and_b32 v109, 0xffff0000, v54\n\t" \
    "v_add_f32 v98, v98, v108\n\t" \
    "v_fmac_f32 v102, v108, v108\n\t" \
    "v_add_f32 v98, v98, v109\n\t" \
    "v_fmac_f32 v102, v109, v109\n\t" \
    "v_mfma_f32_32x32x16_bf16 v[0:15], v[76:79], v[60:63], v[0:15]\n\t" \
    "ds_read_b64_tr_b16 v[76:77], v119 offset:0\n\t" \
    "ds_read_b64_tr_b16 v[78:79], v119 offset:512\n\t" \
    "ds_read_b128 v[60:63], v115 offset:0\n\t" \
    "v_lshlrev_b32 v110, 16, v55\n\t" \
    "v_and_b32 v111, 0xffff0000, v55\n\t" \
    "v_add_f32 v99, v99, v110\n\t" \
    "v_fmac_f32 v103, v110, v110\n\t" \
    "v_add_f32 v99, v99, v111\n\t" \
    "v_fmac_f32 v103, v111, v111\n\t" \
    "ds_read_b128 v[52:55], v113 offset:0\n\t" \
    "s_barrier\n\t" \
    "s_cmp_eq_u32 s42, 0\n\t" \
    "s_cbranch_scc1 L_nf1_%=\n\t" \
    "buffer_load_dwordx2 v[132:133], off, s[60:63], s43 sc1\n\t" \
    "L_nf1_%=:\n\t" \
    "s_waitcnt lgkmcnt(11)\n\t" \
    "v_mfma_f32_32x32x16_bf16 v[0:15], v[64:67], v[48:51], v[0:15]\n\t" \
    "v_lshlrev_b32 v104, 16, v48\n\t" \
    "v_and_b32 v105, 0xffff0000, v48\n\t" \
    "v_add_f32 v96, v96, v104\n\t" \
    "v_fmac_f32 v100, v104, v104\n\t" \
    "v_add_f32 v96, v96, v105\n\t" \
    "v_fmac_f32 v100, v105, v105\n\t" \
    "v_mfma_f32_32x32x16_bf16 v[32:47], v[64:67], v[88:91], v[32:47]\n\t" \
    "ds_read_b64_tr_b16 v[64:65], v116 offset:28672\n\t" \
    "ds_read_b64_tr_b16 v[66:67], v116 offset:29184\n\t" \
    "v_lshlrev_b32 v106, 16, v49\n\t" \
    "v_and_b32 v107, 0xffff0000, v49\n\t" \
    "v_add_f32 v97, v97, v106\n\t" \
    "v_fmac_f32 v101, v106, v106\n\t" \
    "v_add_f32 v97, v97, v107\n\t" \
    "v_fmac_f32 v101, v107, v107\n\t" \
    "v_mfma_f32_32x32x16_bf16 v[16:31], v[80:83], v[48:51], v[16:31]\n\t" \
    "v_lshlrev_b32 v108, 16, v50\n\t" \
    "v_and_b32 v109, 0xffff0000, v50\n\t" \
    "v_add_f32 v98, v98, v108\n\t" \
    "v_fmac_f32 v102, v108, v108\n\t" \
    "v_add_f32 v98, v98, v109\n\t" \
    "v_fmac_f32 v102, v109, v109\n\t" \
    "s_cmp_lg_u32 s40, 0\n\t" \
    "s_cbranch_scc1 L_nocs2_%=\n\t" \
    "v_mfma_f32_32x32x16_bf16 v[32:47], v[80:83], v[92:95], v[32:47]\n\t" \
    "L_nocs2_%=:\n\t" \
    "ds_read_b64_tr_b16 v[80:81], v120 offset:28672\n\t" \
    "ds_read_b64_tr_b16 v[82:83], v120 offset:28928\n\t" \
    "v_lshlrev_b32 v110, 16, v51\n\t" \
    "v_and_b32 v111, 0xffff0000, v51\n\t" \
    "v_add_f32 v99, v99, v110\n\t" \
    "v_fmac_f32 v103, v110, v110\n\t" \
    "v_add_f32 v99, v99, v111\n\t" \
    "v_fmac_f32 v103, v111, v111\n\t" \
    "ds_read_b128 v[48:51], v112 offset:28672\n\t" \
    "s_waitcnt lgkmcnt(5)\n\t" \
    "v_mfma_f32_32x32x16_bf16 v[0:15], v[68:71], v[52:55], v[0:15]\n\t" \
    "ds_read_b64_tr_b16 v[68:69], v117 offset:28672\n\t" \
    "ds_read_b64_tr_b16 v[70:71], v117 offset:29184\n\t" \
    "v_lshlrev_b32 v104, 16, v52\n\t" \
    "v_and_b32 v105, 0xffff0000, v52\n\t" \
    "v_add_f32 v96, v96, v104\n\t" \
    "v_fmac_f32 v100, v104, v104\n\t" \
    "v_add_f32 v96, v96, v105\n\t" \
    "v_fmac_f32 v100, v105, v105\n\t" \
    "v_mfma_f32_32x32x16_bf16 v[16:31], v[84:87], v[52:55], v[16:31]\n\t" \
    "ds_read_b64_tr_b16 v[84:85], v121 offset:28672\n\t" \
    "ds_read_b64_tr_b16 v[86:87], v121 offset:28928\n\t" \
    "v_lshlrev_b32 v106, 16, v53\n\t" \
    "v_and_b32 v107, 0xffff0000, v53\n\t" \
    "v_add_f32 v97, v97, v106\n\t" \
    "v_fmac_f32 v101, v106, v106\n\t" \
    "v_add_f32 v97, v97, v107\n\t" \
    "v_fmac_f32 v101, v107, v107\n\t" \
    "v_mfma_f32_32x32x16_bf16 v[0:15], v[72:75], v[56:59], v[0:15]\n\t" \
    "ds_read_b64_tr_b16 v[72:73], v118 offset:28672\n\t" \
    "ds_read_b64_tr_b16 v[74:75], v118 offset:29184\n\t" \
    "ds_read_b128 v[56:59], v114 offset:28672\n\t" \
    "v_lshlrev_b32 v108, 16, v54\n\t" \
    "v_and_b32 v109, 0xffff0000, v54\n\t" \
    "v_add_f32 v98, v98, v108\n\t" \
    "v_fmac_f32 v102, v108, v108\n\t" \
    "v_add_f32 v98, v98, v109\n\t" \
    "v_fmac_f32 v102, v109, v109\n\t" \
    "v_mfma_f32_32x32x16_bf16 v[0:15], v[76:79], v[60:63], v[0:15]\n\t" \
    "ds_read_b64_tr_b16 v[76:77], v119 offset:28672\n\t" \
    "ds_read_b64_tr_b16 v[78:79], v119 offset:29184\n\t" \
    "ds_read_b128 v[60:63], v115 offset:28672\n\t" \
    "v_lshlrev_b32 v110, 16, v55\n\t" \
    "v_and_b32 v111, 0xffff0000, v55\n\t" \
    "v_add_f32 v99, v99, v110\n\t" \
    "v_fmac_f32 v103, v110, v110\n\t" \
    "v_add_f32 v99, v99, v111\n\t" \
    "v_fmac_f32 v103, v111, v111\n\t" \
    "ds_read_b128 v[52:55], v113 offset:28672\n\t" \
    "s_barrier\n\t" \
    "v_add_u32 v112, 0xffff2000, v112\n\t" \
    "v_add_u32 v113, 0xffff2000, v113\n\t" \
    "v_add_u32 v114, 0xffff2000, v114\n\t" \
    "v_add_u32 v115, 0xffff2000, v115\n\t" \
    "v_add_u32 v116, 0xffff2000, v116\n\t" \
    "v_add_u32 v117, 0xffff2000, v117\n\t" \
    "v_add_u32 v118, 0xffff2000, v118\n\t" \
    "v_add_u32 v119, 0xffff2000, v119\n\t" \
    "v_add_u32 v120, 0xffff2000, v120\n\t" \
    "v_add_u32 v121, 0xffff2000, v121\n\t" \
    "s_cmp_eq_u32 s42, 0\n\t" \
    "s_cbranch_scc1 L_nf2_%=\n\t" \
    "s_mov_b32 s70, 0\n\t" \
    "L_fp_%=:\n\t" \
    "s_waitcnt vmcnt(0)\n\t" \
    "v_readfirstlane_b32 s72, v132\n\t" \
    "v_readfirstlane_b32 s73, v133\n\t" \
    "s_add_u32 s70, s70, 1\n\t" \
    "s_cmp_ge_u32 s70, 0x400000\n\t" \
    "s_cbranch_scc1 L_nf2_%=\n\t" \
    "s_xor_b32 s72, s72, 0x7f4a7c15\n\t" \
    "s_xor_b32 s73, s73, 0x9e3779b9\n\t" \
    "s_or_b32 s72, s72, s73\n\t" \
    "s_cmp_eq_u32 s72, 0\n\t" \
    "s_cbranch_scc1 L_nf2_%=\n\t" \
    "buffer_load_dwordx2 v[132:133], off, s[60:63], s43 sc1\n\t" \
    "s_branch L_fp_%=\n\t" \
    "L_nf2_%=:\n\t" \
    "s_waitcnt lgkmcnt(11)\n\t" \
    "v_mfma_f32_32x32x16_bf16 v[0:15], v[64:67], v[48:51], v[0:15]\n\t" \
    "v_lshlrev_b32 v104, 16, v48\n\t" \
    "v_and_b32 v105, 0xffff0000, v48\n\t" \
    "v_add_f32 v96, v96, v104\n\t" \
    "v_fmac_f32 v100, v104, v104\n\t" \
    "v_add_f32 v96, v96, v105\n\t" \
    "v_fmac_f32 v100, v105, v105\n\t" \
    "v_mfma_f32_32x32x16_bf16 v[32:47], v[64:67], v[88:91], v[32:47]\n\t" \
    "ds_read_b64_tr_b16 v[64:65], v116 offset:0\n\t" \
    "ds_read_b64_tr_b16 v[66:67], v116 offset:512\n\t" \
    "v_lshlrev_b32 v106, 16, v49\n\t" \
    "v_and_b32 v107, 0xffff0000, v49\n\t" \
    "v_add_f32 v97, v97, v106\n\t" \
    "v_fmac_f32 v101, v106, v106\n\t" \
    "v_add_f32 v97, v97, v107\n\t" \
    "v_fmac_f32 v101, v107, v107\n\t" \
    "v_mfma_f32_32x32x16_bf16 v[16:31], v[80:83], v[48:51], v[16:31]\n\t" \
    "v_lshlrev_b32 v108, 16, v50\n\t" \
    "v_and_b32 v109, 0xffff0000, v50\n\t" \
    "v_add_f32 v98, v98, v108\n\t" \
    "v_fmac_f32 v102, v108, v108\n\t" \
    "v_add_f32 v98, v98, v109\n\t" \
    "v_fmac_f32 v102, v109, v109\n\t" \
    "s_cmp_lg_u32 s40, 0\n\t" \
    "s_cbranch_scc1 L_nocs3_%=\n\t" \
    "v_mfma_f32_32x32x16_bf16 v[32:47], v[80:83], v[92:95], v[32:47]\n\t" \
    "L_nocs3_%=:\n\t" \
    "ds_read_b64_tr_b16 v[80:81], v120 offset:0\n\t" \
    "ds_read_b64_tr_b16 v[82:83], v120 offset:256\n\t" \
    "v_lshlrev_b32 v110, 16, v51\n\t" \
    "v_and_b32 v111, 0xffff0000, v51\n\t" \
    "v_add_f32 v99, v99, v110\n\t" \
    "v_fmac_f32 v103, v110, v110\n\t" \
    "v_add_f32 v99, v99, v111\n\t" \
    "v_fmac_f32 v103, v111, v111\n\t" \
    "ds_read_b128 v[48:51], v112 offset:0\n\t" \
    "s_waitcnt lgkmcnt(5)\n\t" \
    "v_mfma_f32_32x32x16_bf16 v[0:15], v[68:71], v[52:55], v[0:15]\n\t" \
    "ds_read_b64_tr_b16 v[68:69], v117 offset:0\n\t" \
    "ds_read_b64_tr_b16 v[70:71], v117 offset:512\n\t" \
    "v_lshlrev_b32 v104, 16, v52\n\t" \
    "v_and_b32 v105, 0xffff0000, v52\n\t" \
    "v_add_f32 v96, v96, v104\n\t" \
    "v_fmac_f32 v100, v104, v104\n\t" \
    "v_add_f32 v96, v96, v105\n\t" \
    "v_fmac_f32 v100, v105, v105\n\t" \
    "v_mfma_f32_32x32x16_bf16 v[16:31], v[84:87], v[52:55], v[16:31]\n\t" \
    "ds_read_b64_tr_b16 v[84:85], v121 offset:0\n\t" \
    "ds_read_b64_tr_b16 v[86:87], v121 offset:256\n\t" \
    "v_lshlrev_b32 v106, 16, v53\n\t" \
    "v_and_b32 v107, 0xffff0000, v53\n\t" \
    "v_add_f32 v97, v97, v106\n\t" \
    "v_fmac_f32 v101, v106, v106\n\t" \
    "v_add_f32 v97, v97, v107\n\t" \
    "v_fmac_f32 v101, v107, v107\n\t" \
    "v_mfma_f32_32x32x16_bf16 v[0:15], v[72:75], v[56:59], v[0:15]\n\t" \
    "ds_read_b64_tr_b16 v[72:73], v118 offset:0\n\t" \
    "ds_read_b64_tr_b16 v[74:75], v118 offset:512\n\t" \
    "ds_read_b128 v[56:59], v114 offset:0\n\t" \
    "v_lshlrev_b32 v108, 16, v54\n\t" \
    "v_and_b32 v109, 0xffff0000, v54\n\t" \
    "v_add_f32 v98, v98, v108\n\t" \
    "v_fmac_f32 v102, v108, v108\n\t" \
    "v_add_f32 v98, v98, v109\n\t" \
    "v_fmac_f32 v102, v109, v109\n\t" \
    "v_mfma_f32_32x32x16_bf16 v[0:15], v[76:79], v[60:63], v[0:15]\n\t" \
    "ds_read_b64_tr_b16 v[76:77], v119 offset:0\n\t" \
    "ds_read_b64_tr_b16 v[78:79], v119 offset:512\n\t" \
    "ds_read_b128 v[60:63], v115 offset:0\n\t" \
    "v_lshlrev_b32 v110, 16, v55\n\t" \
    "v_and_b32 v111, 0xffff0000, v55\n\t" \
    "v_add_f32 v99, v99, v110\n\t" \
    "v_fmac_f32 v103, v110, v110\n\t" \
    "v_add_f32 v99, v99, v111\n\t" \
    "v_fmac_f32 v103, v111, v111\n\t" \
    "ds_read_b128 v[52:55], v113 offset:0\n\t" \
    "s_barrier\n\t" \
    "s_waitcnt lgkmcnt(11)\n\t" \
    "v_mfma_f32_32x32x16_bf16 v[0:15], v[64:67], v[48:51], v[0:15]\n\t" \
    "v_lshlrev_b32 v104, 16, v48\n\t" \
    "v_and_b32 v105, 0xffff0000, v48\n\t" \
    "v_add_f32 v96, v96, v104\n\t" \
    "v_fmac_f32 v100, v104, v104\n\t" \
    "v_add_f32 v96, v96, v105\n\t" \
    "v_fmac_f32 v100, v105, v105\n\t" \
    "v_mfma_f32_32x32x16_bf16 v[32:47], v[64:67], v[88:91], v[32:47]\n\t" \
    "ds_read_b64_tr_b16 v[64:65], v116 offset:28672\n\t" \
    "ds_read_b64_tr_b16 v[66:67], v116 offset:29184\n\t" \
    "v_lshlrev_b32 v106, 16, v49\n\t" \
    "v_and_b32 v107, 0xffff0000, v49\n\t" \
    "v_add_f32 v97, v97, v106\n\t" \
    "v_fmac_f32 v101, v106, v106\n\t" \
    "v_add_f32 v97, v97, v107\n\t" \
    "v_fmac_f32 v101, v107, v107\n\t" \
    "v_mfma_f32_32x32x16_bf16 v[16:31], v[80:83], v[48:51], v[16:31]\n\t" \
    "v_lshlrev_b32 v108, 16, v50\n\t" \
    "v_and_b32 v109, 0xffff0000, v50\n\t" \
    "v_add_f32 v98, v98, v108\n\t" \
    "v_fmac_f32 v102, v108, v108\n\t" \
    "v_add_f32 v98, v98, v109\n\t" \
    "v_fmac_f32 v102, v109, v109\n\t" \
    "s_cmp_lg_u32 s40, 0\n\t" \
    "s_cbranch_scc1 L_nocs4_%=\n\t" \
    "v_mfma_f32_32x32x16_bf16 v[32:47], v[80:83], v[92:95], v[32:47]\n\t" \
    "L_nocs4_%=:\n\t" \
    "ds_read_b64_tr_b16 v[80:81], v120 offset:28672\n\t" \
    "ds_read_b64_tr_b16 v[82:83], v120 offset:28928\n\t" \
    "v_lshlrev_b32 v110, 16, v51\n\t" \
    "v_and_b32 v111, 0xffff0000, v51\n\t" \
    "v_add_f32 v99, v99, v110\n\t" \
    "v_fmac_f32 v103, v110, v110\n\t" \
    "v_add_f32 v99, v99, v111\n\t" \
    "v_fmac_f32 v103, v111, v111\n\t" \
    "ds_read_b128 v[48:51], v112 offset:28672\n\t" \
    "s_waitcnt lgkmcnt(5)\n\t" \
    "v_mfma_f32_32x32x16_bf16 v[0:15], v[68:71], v[52:55], v[0:15]\n\t" \
    "ds_read_b64_tr_b16 v[68:69], v117 offset:28672\n\t" \
    "ds_read_b64_tr_b16 v[70:71], v117 offset:29184\n\t" \
    "v_lshlrev_b32 v104, 16, v52\n\t" \
    "v_and_b32 v105, 0xffff0000, v52\n\t" \
    "v_add_f32 v96, v96, v104\n\t" \
    "v_fmac_f32 v100, v104, v104\n\t" \
    "v_add_f32 v96, v96, v105\n\t" \
    "v_fmac_f32 v100, v105, v105\n\t" \
    "v_mfma_f32_32x32x16_bf16 v[16:31], v[84:87], v[52:55], v[16:31]\n\t" \
    "ds_read_b64_tr_b16 v[84:85], v121 offset:28672\n\t" \
    "ds_read_b64_tr_b16 v[86:87], v121 offset:28928\n\t" \
    "v_lshlrev_b32 v106, 16, v53\n\t" \
    "v_and_b32 v107, 0xffff0000, v53\n\t" \
    "v_add_f32 v97, v97, v106\n\t" \
    "v_fmac_f32 v101, v106, v106\n\t" \
    "v_add_f32 v97, v97, v107\n\t" \
    "v_fmac_f32 v101, v107, v107\n\t" \
    "v_mfma_f32_32x32x16_bf16 v[0:15], v[72:75], v[56:59], v[0:15]\n\t" \
    "ds_read_b64_tr_b16 v[72:73], v118 offset:28672\n\t" \
    "ds_read_b64_tr_b16 v[74:75], v118 offset:29184\n\t" \
    "ds_read_b128 v[56:59], v114 offset:28672\n\t" \
    "v_lshlrev_b32 v108, 16, v54\n\t" \
    "v_and_b32 v109, 0xffff0000, v54\n\t" \
    "v_add_f32 v98, v98, v108\n\t" \
    "v_fmac_f32 v102, v108, v108\n\t" \
    "v_add_f32 v98, v98, v109\n\t" \
    "v_fmac_f32 v102, v109, v109\n\t" \
    "v_mfma_f32_32x32x16_bf16 v[0:15], v[76:79], v[60:63], v[0:15]\n\t" \
    "ds_read_b64_tr_b16 v[76:77], v119 offset:28672\n\t" \
    "ds_read_b64_tr_b16 v[78:79], v119 offset:29184\n\t" \
    "ds_read_b128 v[60:63], v115 offset:28672\n\t" \
    "v_lshlrev_b32 v110, 16, v55\n\t" \
    "v_and_b32 v111, 0xffff0000, v55\n\t" \
    "v_add_f32 v99, v99, v110\n\t" \
    "v_fmac_f32 v103, v110, v110\n\t" \
    "v_add_f32 v99, v99, v111\n\t" \
    "v_fmac_f32 v103, v111, v111\n\t" \
    "ds_read_b128 v[52:55], v113 offset:28672\n\t" \
    "s_barrier\n\t" \
    "v_add_u32 v112, 0xe000, v112\n\t" \
    "v_add_u32 v113, 0xe000, v113\n\t" \
    "v_add_u32 v114, 0xe000, v114\n\t" \
    "v_add_u32 v115, 0xe000, v115\n\t" \
    "v_add_u32 v116, 0xe000, v116\n\t" \
    "v_add_u32 v117, 0xe000, v117\n\t" \
    "v_add_u32 v118, 0xe000, v118\n\t" \
    "v_add_u32 v119, 0xe000, v119\n\t" \
    "v_add_u32 v120, 0xe000, v120\n\t" \
    "v_add_u32 v121, 0xe000, v121\n\t" \
    "s_waitcnt lgkmcnt(11)\n\t" \
    "v_mfma_f32_32x32x16_bf16 v[0:15], v[64:67], v[48:51], v[0:15]\n\t" \
    "v_lshlrev_b32 v104, 16, v48\n\t" \
    "v_and_b32 v105, 0xffff0000, v48\n\t" \
    "v_add_f32 v96, v96, v104\n\t" \
    "v_fmac_f32 v100, v104, v104\n\t" \
    "v_add_f32 v96, v96, v105\n\t" \
    "v_fmac_f32 v100, v105, v105\n\t" \
    "v_mfma_f32_32x32x16_bf16 v[32:47], v[64:67], v[88:91], v[32:47]\n\t" \
    "ds_read_b64_tr_b16 v[64:65], v116 offset:0\n\t" \
    "ds_read_b64_tr_b16 v[66:67], v116 offset:512\n\t" \
    "v_lshlrev_b32 v106, 16, v49\n\t" \
    "v_and_b32 v107, 0xffff0000, v49\n\t" \
    "v_add_f32 v97, v97, v106\n\t" \
    "v_fmac_f32 v101, v106, v106\n\t" \
    "v_add_f32 v97, v97, v107\n\t" \
    "v_fmac_f32 v101, v107, v107\n\t" \
    "v_mfma_f32_32x32x16_bf16 v[16:31], v[80:83], v[48:51], v[16:31]\n\t" \
    "v_lshlrev_b32 v108, 16, v50\n\t" \
    "v_and_b32 v109, 0xffff0000, v50\n\t" \
    "v_add_f32 v98, v98, v108\n\t" \
    "v_fmac_f32 v102, v108, v108\n\t" \
    "v_add_f32 v98, v98, v109\n\t" \
    "v_fmac_f32 v102, v109, v109\n\t" \
    "s_cmp_lg_u32 s40, 0\n\t" \
    "s_cbranch_scc1 L_nocs5_%=\n\t" \
    "v_mfma_f32_32x32x16_bf16 v[32:47], v[80:83], v[92:95], v[32:47]\n\t" \
    "L_nocs5_%=:\n\t" \
    "ds_read_b64_tr_b16 v[80:81], v120 offset:0\n\t" \
    "ds_read_b64_tr_b16 v[82:83], v120 offset:256\n\t" \
    "v_lshlrev_b32 v110, 16, v51\n\t" \
    "v_and_b32 v111, 0xffff0000, v51\n\t" \
    "v_add_f32 v99, v99, v110\n\t" \
    "v_fmac_f32 v103, v110, v110\n\t" \
    "v_add_f32 v99, v99, v111\n\t" \
    "v_fmac_f32 v103, v111, v111\n\t" \
    "ds_read_b128 v[48:51], v112 offset:0\n\t" \
    "s_waitcnt lgkmcnt(5)\n\t" \
    "v_mfma_f32_32x32x16_bf16 v[0:15], v[68:71], v[52:55], v[0:15]\n\t" \
    "ds_read_b64_tr_b16 v[68:69], v117 offset:0\n\t" \
    "ds_read_b64_tr_b16 v[70:71], v117 offset:512\n\t" \
    "v_lshlrev_b32 v104, 16, v52\n\t" \
    "v_and_b32 v105, 0xffff0000, v52\n\t" \
    "v_add_f32 v96, v96, v104\n\t" \
    "v_fmac_f32 v100, v104, v104\n\t" \
    "v_add_f32 v96, v96, v105\n\t" \
    "v_fmac_f32 v100, v105, v105\n\t" \
    "v_mfma_f32_32x32x16_bf16 v[16:31], v[84:87], v[52:55], v[16:31]\n\t" \
    "ds_read_b64_tr_b16 v[84:85], v121 offset:0\n\t" \
    "ds_read_b64_tr_b16 v[86:87], v121 offset:256\n\t" \
    "v_lshlrev_b32 v106, 16, v53\n\t" \
    "v_and_b32 v107, 0xffff0000, v53\n\t" \
    "v_add_f32 v97, v97, v106\n\t" \
    "v_fmac_f32 v101, v106, v106\n\t" \
    "v_add_f32 v97, v97, v107\n\t" \
    "v_fmac_f32 v101, v107, v107\n\t" \
    "v_mfma_f32_32x32x16_bf16 v[0:15], v[72:75], v[56:59], v[0:15]\n\t" \
    "ds_read_b64_tr_b16 v[72:73], v118 offset:0\n\t" \
    "ds_read_b64_tr_b16 v[74:75], v118 offset:512\n\t" \
    "ds_read_b128 v[56:59], v114 offset:0\n\t" \
    "v_lshlrev_b32 v108, 16, v54\n\t" \
    "v_and_b32 v109, 0xffff0000, v54\n\t" \
    "v_add_f32 v98, v98, v108\n\t" \
    "v_fmac_f32 v102, v108, v108\n\t" \
    "v_add_f32 v98, v98, v109\n\t" \
    "v_fmac_f32 v102, v109, v109\n\t" \
    "v_mfma_f32_32x32x16_bf16 v[0:15], v[76:79], v[60:63], v[0:15]\n\t" \
    "ds_read_b64_tr_b16 v[76:77], v119 offset:0\n\t" \
    "ds_read_b64_tr_b16 v[78:79], v119 offset:512\n\t" \
    "ds_read_b128 v[60:63], v115 offset:0\n\t" \
    "v_lshlrev_b32 v110, 16, v55\n\t" \
    "v_and_b32 v111, 0xffff0000, v55\n\t" \
    "v_add_f32 v99, v99, v110\n\t" \
    "v_fmac_f32 v103, v110, v110\n\t" \
    "v_add_f32 v99, v99, v111\n\t" \
    "v_fmac_f32 v103, v111, v111\n\t" \
    "ds_read_b128 v[52:55], v113 offset:0\n\t" \
    "s_barrier\n\t" \
    "s_waitcnt lgkmcnt(11)\n\t" \
    "v_mfma_f32_32x32x16_bf16 v[0:15], v[64:67], v[48:51], v[0:15]\n\t" \
    "v_lshlrev_b32 v104, 16, v48\n\t" \
    "v_and_b32 v105, 0xffff0000, v48\n\t" \
    "v_add_f32 v96, v96, v104\n\t" \
    "v_fmac_f32 v100, v104, v104\n\t" \
    "v_add_f32 v96, v96, v105\n\t" \
    "v_fmac_f32 v100, v105, v105\n\t" \
    "v_mfma_f32_32x32x16_bf16 v[32:47], v[64:67], v[88:91], v[32:47]\n\t" \
    "ds_read_b64_tr_b16 v[64:65], v116 offset:28672\n\t" \
    "ds_read_b64_tr_b16 v[66:67], v116 offset:29184\n\t" \
    "v_lshlrev_b32 v106, 16, v49\n\t" \
    "v_and_b32 v107, 0xffff0000, v49\n\t" \
    "v_add_f32 v97, v97, v106\n\t" \
    "v_fmac_f32 v101, v106, v106\n\t" \
    "v_add_f32 v97, v97, v107\n\t" \
    "v_fmac_f32 v101, v107, v107\n\t" \
    "v_mfma_f32_32x32x16_bf16 v[16:31], v[80:83], v[48:51], v[16:31]\n\t" \
    "v_lshlrev_b32 v108, 16, v50\n\t" \
    "v_and_b32 v109, 0xffff0000, v50\n\t" \
    "v_add_f32 v98, v98, v108\n\t" \
    "v_fmac_f32 v102, v108, v108\n\t" \
    "v_add_f32 v98, v98, v109\n\t" \
    "v_fmac_f32 v102, v109, v109\n\t" \
    "s_cmp_lg_u32 s40, 0\n\t" \
    "s_cbranch_scc1 L_nocs6_%=\n\t" \
    "v_mfma_f32_32x32x16_bf16 v[32:47], v[80:83], v[92:95], v[32:47]\n\t" \
    "L_nocs6_%=:\n\t" \
    "ds_read_b64_tr_b16 v[80:81], v120 offset:28672\n\t" \
    "ds_read_b64_tr_b16 v[82:83], v120 offset:28928\n\t" \
    "v_lshlrev_b32 v110, 16, v51\n\t" \
    "v_and_b32 v111, 0xffff0000, v51\n\t" \
    "v_add_f32 v99, v99, v110\n\t" \
    "v_fmac_f32 v103, v110, v110\n\t" \
    "v_add_f32 v99, v99, v111\n\t" \
    "v_fmac_f32 v103, v111, v111\n\t" \
    "ds_read_b128 v[48:51], v112 offset:28672\n\t" \
    "s_waitcnt lgkmcnt(5)\n\t" \
    "v_mfma_f32_32x32x16_bf16 v[0:15], v[68:71], v[52:55], v[0:15]\n\t" \
    "ds_read_b64_tr_b16 v[68:69], v117 offset:28672\n\t" \
    "ds_read_b64_tr_b16 v[70:71], v117 offset:29184\n\t" \
    "v_lshlrev_b32 v104, 16, v52\n\t" \
    "v_and_b32 v105, 0xffff0000, v52\n\t" \
    "v_add_f32 v96, v96, v104\n\t" \
    "v_fmac_f32 v100, v104, v104\n\t" \
    "v_add_f32 v96, v96, v105\n\t" \
    "v_fmac_f32 v100, v105, v105\n\t" \
    "v_mfma_f32_32x32x16_bf16 v[16:31], v[84:87], v[52:55], v[16:31]\n\t" \
    "ds_read_b64_tr_b16 v[84:85], v121 offset:28672\n\t" \
    "ds_read_b64_tr_b16 v[86:87], v121 offset:28928\n\t" \
    "v_lshlrev_b32 v106, 16, v53\n\t" \
    "v_and_b32 v107, 0xffff0000, v53\n\t" \
    "v_add_f32 v97, v97, v106\n\t" \
    "v_fmac_f32 v101, v106, v106\n\t" \
    "v_add_f32 v97, v97, v107\n\t" \
    "v_fmac_f32 v101, v107, v107\n\t" \
    "v_mfma_f32_32x32x16_bf16 v[0:15], v[72:75], v[56:59], v[0:15]\n\t" \
    "ds_read_b64_tr_b16 v[72:73], v118 offset:28672\n\t" \
    "ds_read_b64_tr_b16 v[74:75], v118 offset:29184\n\t" \
    "ds_read_b128 v[56:59], v114 offset:28672\n\t" \
    "v_lshlrev_b32 v108, 16, v54\n\t" \
    "v_and_b32 v109, 0xffff0000, v54\n\t" \
    "v_add_f32 v98, v98, v108\n\t" \
    "v_fmac_f32 v102, v108, v108\n\t" \
    "v_add_f32 v98, v98, v109\n\t" \
    "v_fmac_f32 v102, v109, v109\n\t" \
    "v_mfma_f32_32x32x16_bf16 v[0:15], v[76:79], v[60:63], v[0:15]\n\t" \
    "ds_read_b64_tr_b16 v[76:77], v119 offset:28672\n\t" \
    "ds_read_b64_tr_b16 v[78:79], v119 offset:29184\n\t" \
    "ds_read_b128 v[60:63], v115 offset:28672\n\t" \
    "v_lshlrev_b32 v110, 16, v55\n\t" \
    "v_and_b32 v111, 0xffff0000, v55\n\t" \
    "v_add_f32 v99, v99, v110\n\t" \
    "v_fmac_f32 v103, v110, v110\n\t" \
    "v_add_f32 v99, v99, v111\n\t" \
    "v_fmac_f32 v103, v111, v111\n\t" \
    "ds_read_b128 v[52:55], v113 offset:28672\n\t" \
    "s_waitcnt lgkmcnt(11)\n\t" \
    "v_mfma_f32_32x32x16_bf16 v[0:15], v[64:67], v[48:51], v[0:15]\n\t" \
    "v_lshlrev_b32 v104, 16, v48\n\t" \
    "v_and_b32 v105, 0xffff0000, v48\n\t" \
    "v_add_f32 v96, v96, v104\n\t" \
    "v_fmac_f32 v100, v104, v104\n\t" \
    "v_add_f32 v96, v96, v105\n\t" \
    "v_fmac_f32 v100, v105, v105\n\t" \
    "v_mfma_f32_32x32x16_bf16 v[32:47], v[64:67], v[88:91], v[32:47]\n\t" \
    "v_lshlrev_b32 v106, 16, v49\n\t" \
    "v_and_b32 v107, 0xffff0000, v49\n\t" \
    "v_add_f32 v97, v97, v106\n\t" \
    "v_fmac_f32 v101, v106, v106\n\t" \
    "v_add_f32 v97, v97, v107\n\t" \
    "v_fmac_f32 v101, v107, v107\n\t" \
    "v_mfma_f32_32x32x16_bf16 v[16:31], v[80:83], v[48:51], v[16:31]\n\t" \
    "v_lshlrev_b32 v108, 16, v50\n\t" \
    "v_and_b32 v109, 0xffff0000, v50\n\t" \
    "v_add_f32 v98, v98, v108\n\t" \
    "v_fmac_f32 v102, v108, v108\n\t" \
    "v_add_f32 v98, v98, v109\n\t" \
    "v_fmac_f32 v102, v109, v109\n\t" \
    "s_cmp_lg_u32 s40, 0\n\t" \
    "s_cbranch_scc1 L_nocs7_%=\n\t" \
    "v_mfma_f32_32x32x16_bf16 v[32:47], v[80:83], v[92:95], v[32:47]\n\t" \
    "L_nocs7_%=:\n\t" \
    "v_lshlrev_b32 v110, 16, v51\n\t" \
    "v_and_b32 v111, 0xffff0000, v51\n\t" \
    "v_add_f32 v99, v99, v110\n\t" \
    "v_fmac_f32 v103, v110, v110\n\t" \
    "v_add_f32 v99, v99, v111\n\t" \
    "v_fmac_f32 v103, v111, v111\n\t" \
    "s_waitcnt lgkmcnt(0)\n\t" \
    "v_mfma_f32_32x32x16_bf16 v[0:15], v[68:71], v[52:55], v[0:15]\n\t" \
    "v_lshlrev_b32 v104, 16, v52\n\t" \
    "v_and_b32 v105, 0xffff0000, v52\n\t" \
    "v_add_f32 v96, v96, v104\n\t" \
    "v_fmac_f32 v100, v104, v104\n\t" \
    "v_add_f32 v96, v96, v105\n\t" \
    "v_fmac_f32 v100, v105, v105\n\t" \
    "v_mfma_f32_32x32x16_bf16 v[16:31], v[84:87], v[52:55], v[16:31]\n\t" \
    "v_lshlrev_b32 v106, 16, v53\n\t" \
    "v_and_b32 v107, 0xffff0000, v53\n\t" \
    "v_add_f32 v97, v97, v106\n\t" \
    "v_fmac_f32 v101, v106, v106\n\t" \
    "v_add_f32 v97, v97, v107\n\t" \
    "v_fmac_f32 v101, v107, v107\n\t" \
    "v_mfma_f32_32x32x16_bf16 v[0:15], v[72:75], v[56:59], v[0:15]\n\t" \
    "v_lshlrev_b32 v108, 16, v54\n\t" \
    "v_and_b32 v109, 0xffff0000, v54\n\t" \
    "v_add_f32 v98, v98, v108\n\t" \
    "v_fmac_f32 v102, v108, v108\n\t" \
    "v_add_f32 v98, v98, v109\n\t" \
    "v_fmac_f32 v102, v109, v109\n\t" \
    "v_mfma_f32_32x32x16_bf16 v[0:15], v[76:79], v[60:63], v[0:15]\n\t" \
    "v_lshlrev_b32 v110, 16, v55\n\t" \
    "v_and_b32 v111, 0xffff0000, v55\n\t" \
    "v_add_f32 v99, v99, v110\n\t" \
    "v_fmac_f32 v103, v110, v110\n\t" \
    "v_add_f32 v99, v99, v111\n\t" \
    "v_fmac_f32 v103, v111, v111\n\t" \
    "s_nop 15\n\t" \
    "v_mbcnt_lo_u32_b32 v104, -1, 0\n\t" \
    "v_mbcnt_hi_u32_b32 v104, -1, v104\n\t" \
    "v_lshlrev_b32 v105, 4, v104\n\t" \
    "v_lshlrev_b32 v106, 2, v104\n\t" \
    "v_add_u32 v107, s44, v105\n\t" \
    "v_add_u32 v108, s46, v106\n\t" \
    "v_add_f32 v96, v96, v97\n\t" \
    "v_add_f32 v98, v98, v99\n\t" \
    "v_add_f32 v100, v100, v101\n\t" \
    "v_add_f32 v102, v102, v103\n\t" \
    "s_cmp_lg_u32 s40, 0\n\t" \
    "s_cbranch_scc1 L_eg1_%=\n\t" \
    "ds_write_b128 v107, v[24:27]\n\t" \
    "ds_write_b128 v107, v[28:31] offset:1024\n\t" \
    "s_mov_b32 s70, 0x00010001\n\t" \
    "s_branch L_egd_%=\n\t" \
    "L_eg1_%=:\n\t" \
    "ds_write_b128 v107, v[16:19]\n\t" \
    "ds_write_b128 v107, v[20:23] offset:1024\n\t" \
    "s_mov_b32 s70, 0x00000001\n\t" \
    "L_egd_%=:\n\t" \
    "v_add_f32 v96, v96, v98\n\t" \
    "v_add_f32 v100, v100, v102\n\t" \
    "s_lshl_b32 s72, s40, 9\n\t" \
    "s_lshl_b32 s73, s48, 7\n\t" \
    "s_add_u32 s72, s72, s73\n\t" \
    "s_add_u32 s72, s72, s47\n\t" \
    "s_add_u32 s72, s72, 20480\n\t" \
    "v_lshrrev_b32 v109, 5, v104\n\t" \
    "v_lshlrev_b32 v109, 4, v109\n\t" \
    "v_and_b32 v107, 16, v104\n\t" \
    "v_lshlrev_b32 v107, 6, v107\n\t" \
    "v_add3_u32 v109, v109, v107, s72\n\t" \
    "ds_write_b32 v108, v96\n\t" \
    "ds_write_b32 v108, v100 offset:256\n\t" \
    "s_mov_b32 exec_lo, s70\n\t" \
    "s_mov_b32 exec_hi, s70\n\t" \
    "ds_write_b128 v109, v[32:35] offset:0\n\t" \
    "ds_write_b128 v109, v[36:39] offset:32\n\t" \
    "ds_write_b128 v109, v[40:43] offset:64\n\t" \
    "ds_write_b128 v109, v[44:47] offset:96\n\t" \
    "s_mov_b64 exec, -1\n\t" \
    "s_waitcnt lgkmcnt(0)\n\t" \
    "s_barrier\n\t" \
    "v_add_u32 v107, s45, v105\n\t" \
    "ds_read_b128 v[112:115], v107\n\t" \
    "ds_read_b128 v[116:119], v107 offset:1024\n\t" \
    "v_and_b32 v108, 31, v104\n\t" \
    "v_lshlrev_b32 v108, 2, v108\n\t" \
    "v_lshrrev_b32 v109, 5, v104\n\t" \
    "v_lshlrev_b32 v109, 9, v109\n\t" \
    "v_add3_u32 v107, v108, v109, s47\n\t" \
    "v_add_u32 v107, 0x4000, v107\n\t" \
    "v_add_u32 v109, 128, v107\n\t" \
    "ds_read2st64_b32 v[60:61], v107 offset0:0 offset1:1\n\t" \
    "ds_read2st64_b32 v[62:63], v109 offset0:0 offset1:1\n\t" \
    "ds_read2st64_b32 v[64:65], v107 offset0:8 offset1:9\n\t" \
    "ds_read2st64_b32 v[66:67], v109 offset0:8 offset1:9\n\t" \
    "ds_read2st64_b32 v[68:69], v107 offset0:4 offset1:5\n\t" \
    "ds_read2st64_b32 v[70:71], v109 offset0:4 offset1:5\n\t" \
    "ds_read2st64_b32 v[72:73], v107 offset0:12 offset1:13\n\t" \
    "ds_read2st64_b32 v[74:75], v109 offset0:12 offset1:13\n\t" \
    "v_add_u32 v108, s47, v108\n\t" \
    "v_add_u32 v108, 20480, v108\n\t" \
    "v_add_u32 v110, 128, v108\n\t" \
    "s_lshl_b32 s72, s40, 9\n\t" \
    "v_add_u32 v111, s72, v108\n\t" \
    "v_add_u32 v120, s72, v110\n\t" \
    "ds_read2st64_b32 v[122:123], v111 offset0:0 offset1:1\n\t" \
    "ds_read2st64_b32 v[124:125], v120 offset0:0 offset1:1\n\t" \
    "ds_read2st64_b32 v[126:127], v108 offset0:4 offset1:5\n\t" \
    "ds_read2st64_b32 v[128:129], v110 offset0:4 offset1:5\n\t" \
    "s_waitcnt lgkmcnt(8)\n\t" \
    "v_add_f32 v60, v60, v62\n\t" \
    "v_add_f32 v64, v64, v66\n\t" \
    "v_add_f32 v60, v60, v64\n\t" \
    "v_add_f32 v61, v61, v63\n\t" \
    "v_add_f32 v65, v65, v67\n\t" \
    "v_add_f32 v61, v61, v65\n\t" \
    "v_mul_f32 v92, 0x3b000000, v60\n\t" \
    "v_mul_f32 v62, v92, v92\n\t" \
    "v_fma_f32 v61, v61, s54, -v62\n\t" \
    "v_max_f32 v61, 0, v61\n\t" \
    "v_add_f32 v88, 0x3727c5ac, v61\n\t" \
    "v_rsq_f32 v76, v88\n\t" \
    "v_mov_b32 v93, v92\n\t" \
    "v_mov_b32 v89, v88\n\t" \
    "v_mov_b32 v77, v76\n\t" \
    "s_nop 1\n\t" \
    "v_permlane32_swap_b32 v92, v93\n\t" \
    "v_permlane32_swap_b32 v88, v89\n\t" \
    "v_permlane32_swap_b32 v76, v77\n\t" \
    "s_waitcnt lgkmcnt(4)\n\t" \
    "v_add_f32 v68, v68, v70\n\t" \
    "v_add_f32 v72, v72, v74\n\t" \
    "v_add_f32 v68, v68, v72\n\t" \
    "v_add_f32 v69, v69, v71\n\t" \
    "v_add_f32 v73, v73, v75\n\t" \
    "v_add_f32 v69, v69, v73\n\t" \
    "v_mul_f32 v94, 0x3b000000, v68\n\t" \
    "v_mul_f32 v70, v94, v94\n\t" \
    "v_fma_f32 v69, v69, s54, -v70\n\t" \
    "v_max_f32 v69, 0, v69\n\t" \
    "v_add_f32 v90, 0x3727c5ac, v69\n\t" \
    "v_rsq_f32 v78, v90\n\t" \
    "v_mov_b32 v95, v94\n\t" \
    "v_mov_b32 v91, v90\n\t" \
    "v_mov_b32 v79, v78\n\t" \
    "s_nop 1\n\t" \
    "v_permlane32_swap_b32 v94, v95\n\t" \
    "v_permlane32_swap_b32 v90, v91\n\t" \
    "v_permlane32_swap_b32 v78, v79\n\t" \
    "s_waitcnt lgkmcnt(0)\n\t" \
    "v_add_f32 v122, v122, v124\n\t" \
    "v_add_f32 v123, v123, v125\n\t" \
    "v_add_f32 v126, v126, v128\n\t" \
    "v_add_f32 v127, v127, v129\n\t" \
    "v_add_f32 v122, v122, v123\n\t" \
    "v_add_f32 v126, v126, v127\n\t" \
    "v_cvt_pk_bf16_f32 v48, v122, v122\n\t" \
    "v_lshlrev_b32 v49, 16, v48\n\t" \
    "v_sub_f32 v49, v122, v49\n\t" \
    "v_cvt_pk_bf16_f32 v49, v49, 0\n\t" \
    "v_mov_b32 v50, 0\n\t" \
    "v_mov_b32 v51, 0\n\t" \
    "v_cvt_pk_bf16_f32 v52, v126, v126\n\t" \
    "v_lshlrev_b32 v53, 16, v52\n\t" \
    "v_sub_f32 v53, v126, v53\n\t" \
    "v_cvt_pk_bf16_f32 v53, v53, 0\n\t" \
    "v_mov_b32 v54, 0\n\t" \
    "v_mov_b32 v55, 0\n\t" \
    "s_set_gpr_idx_on s48, gpr_idx(SRC0)\n\t" \
    "v_mov_b32 v120, v92\n\t" \
    "v_mov_b32 v121, v76\n\t" \
    "s_set_gpr_idx_off\n\t" \
    "v_cvt_pk_bf16_f32 v57, -v120, 0\n\t" \
    "v_lshlrev_b32 v58, 16, v57\n\t" \
    "v_sub_f32 v58, -v120, v58\n\t" \
    "v_cvt_pk_bf16_f32 v56, -v120, v58\n\t" \
    "v_mov_b32 v58, 0\n\t" \
    "v_mov_b32 v59, 0\n\t" \
    "s_mov_b32 exec_lo, 0\n\t" \
    "v_mov_b32 v48, 0\n\t" \
    "v_mov_b32 v49, 0\n\t" \
    "v_mov_b32 v52, 0\n\t" \
    "v_mov_b32 v53, 0\n\t" \
    "v_mov_b32 v56, 0\n\t" \
    "v_mov_b32 v57, 0\n\t" \
    "s_mov_b32 exec_lo, -1\n\t" \
    "s_nop 1\n\t" \
    "v_mfma_f32_32x32x16_bf16 v[0:15], v[48:51], v[56:59], v[0:15]\n\t" \
    "v_mfma_f32_32x32x16_bf16 v[16:31], v[52:55], v[56:59], v[16:31]\n\t" \
    "v_mul_f32 v84, 0xc0135761, v76\n\t" \
    "v_mul_f32 v80, v76, v76\n\t" \
    "v_mul_f32 v80, v80, v76\n\t" \
    "v_mul_f32 v80, 0xbdd2d3e8, v80\n\t" \
    "v_mul_f32 v85, 0xc0135761, v77\n\t" \
    "v_mul_f32 v81, v77, v77\n\t" \
    "v_mul_f32 v81, v81, v77\n\t" \
    "v_mul_f32 v81, 0xbdd2d3e8, v81\n\t" \
    "v_mul_f32 v86, 0xc0135761, v78\n\t" \
    "v_mul_f32 v82, v78, v78\n\t" \
    "v_mul_f32 v82, v82, v78\n\t" \
    "v_mul_f32 v82, 0xbdd2d3e8, v82\n\t" \
    "v_mul_f32 v87, 0xc0135761, v79\n\t" \
    "v_mul_f32 v83, v79, v79\n\t" \
    "v_mul_f32 v83, v83, v79\n\t" \
    "v_mul_f32 v83, 0xbdd2d3e8, v83\n\t" \
    "v_mul_f32 v122, s52, v121\n\t" \
    "v_mul_f32 v123, s53, v121\n\t" \
    "s_nop 7\n\t" \
    "v_mul_f32 v0, v0, v122\n\t" \
    "v_mul_f32 v1, v1, v122\n\t" \
    "v_mul_f32 v2, v2, v122\n\t" \
    "v_mul_f32 v3, v3, v122\n\t" \
    "v_mul_f32 v4, v4, v122\n\t" \
    "v_mul_f32 v5, v5, v122\n\t" \
    "v_mul_f32 v6, v6, v122\n\t" \
    "v_mul_f32 v7, v7, v122\n\t" \
    "v_cvt_pk_bf16_f32 v48, v0, v1\n\t" \
    "v_cvt_pk_bf16_f32 v49, v2, v3\n\t" \
    "v_cvt_pk_bf16_f32 v50, v4, v5\n\t" \
    "v_cvt_pk_bf16_f32 v51, v6, v7\n\t" \
    "buffer_store_dwordx4 v[48:51], v105, s[60:63], s49 offen sc1\n\t" \
    "v_mul_f32 v8, v8, v122\n\t" \
    "v_mul_f32 v9, v9, v122\n\t" \
    "v_mul_f32 v10, v10, v122\n\t" \
    "v_mul_f32 v11, v11, v122\n\t" \
    "v_mul_f32 v12, v12, v122\n\t" \
    "v_mul_f32 v13, v13, v122\n\t" \
    "v_mul_f32 v14, v14, v122\n\t" \
    "v_mul_f32 v15, v15, v122\n\t" \
    "v_cvt_pk_bf16_f32 v52, v8, v9\n\t" \
    "v_cvt_pk_bf16_f32 v53, v10, v11\n\t" \
    "v_cvt_pk_bf16_f32 v54, v12, v13\n\t" \
    "v_cvt_pk_bf16_f32 v55, v14, v15\n\t" \
    "buffer_store_dwordx4 v[52:55], v105, s[60:63], s50 offen sc1\n\t" \
    "s_cmp_lg_u32 s40, 0\n\t" \
    "s_cbranch_scc1 L_eo1_%=\n\t" \
    "v_add_f32 v16, v16, v112\n\t" \
    "v_add_f32 v17, v17, v113\n\t" \
    "v_add_f32 v18, v18, v114\n\t" \
    "v_add_f32 v19, v19, v115\n\t" \
    "v_add_f32 v20, v20, v116\n\t" \
    "v_add_f32 v21, v21, v117\n\t" \
    "v_add_f32 v22, v22, v118\n\t" \
    "v_add_f32 v23, v23, v119\n\t" \
    "v_mul_f32 v16, v16, v123\n\t" \
    "v_mul_f32 v17, v17, v123\n\t" \
    "v_mul_f32 v18, v18, v123\n\t" \
    "v_mul_f32 v19, v19, v123\n\t" \
    "v_mul_f32 v20, v20, v123\n\t" \
    "v_mul_f32 v21, v21, v123\n\t" \
    "v_mul_f32 v22, v22, v123\n\t" \
    "v_mul_f32 v23, v23, v123\n\t" \
    "v_cvt_pk_bf16_f32 v56, v16, v17\n\t" \
    "v_cvt_pk_bf16_f32 v57, v18, v19\n\t" \
    "v_cvt_pk_bf16_f32 v58, v20, v21\n\t" \
    "v_cvt_pk_bf16_f32 v59, v22, v23\n\t" \
    "buffer_store_dwordx4 v[56:59], v105, s[60:63], s51 offen sc1\n\t" \
    "s_branch L_eod_%=\n\t" \
    "L_eo1_%=:\n\t" \
    "v_add_f32 v24, v24, v112\n\t" \
    "v_add_f32 v25, v25, v113\n\t" \
    "v_add_f32 v26, v26, v114\n\t" \
    "v_add_f32 v27, v27, v115\n\t" \
    "v_add_f32 v28, v28, v116\n\t" \
    "v_add_f32 v29, v29, v117\n\t" \
    "v_add_f32 v30, v30, v118\n\t" \
    "v_add_f32 v31, v31, v119\n\t" \
    "v_mul_f32 v24, v24, v123\n\t" \
    "v_mul_f32 v25, v25, v123\n\t" \
    "v_mul_f32 v26, v26, v123\n\t" \
    "v_mul_f32 v27, v27, v123\n\t" \
    "v_mul_f32 v28, v28, v123\n\t" \
    "v_mul_f32 v29, v29, v123\n\t" \
    "v_mul_f32 v30, v30, v123\n\t" \
    "v_mul_f32 v31, v31, v123\n\t" \
    "v_cvt_pk_bf16_f32 v56, v24, v25\n\t" \
    "v_cvt_pk_bf16_f32 v57, v26, v27\n\t" \
    "v_cvt_pk_bf16_f32 v58, v28, v29\n\t" \
    "v_cvt_pk_bf16_f32 v59, v30, v31\n\t" \
    "buffer_store_dwordx4 v[56:59], v105, s[60:63], s51 offen sc1\n\t" \
    "L_eod_%=:\n\t" \
    ""
#define K1_ASM_4 \
    "s_barrier\n\t" \
    "ds_read_b128 v[48:51], v112 offset:0\n\t" \
    "ds_read_b64_tr_b16 v[64:65], v116 offset:0\n\t" \
    "ds_read_b64_tr_b16 v[66:67], v116 offset:512\n\t" \
    "ds_read_b64_tr_b16 v[80:81], v120 offset:0\n\t" \
    "ds_read_b64_tr_b16 v[82:83], v120 offset:256\n\t" \
    "ds_read_b128 v[52:55], v113 offset:0\n\t" \
    "ds_read_b64_tr_b16 v[68:69], v117 offset:0\n\t" \
    "ds_read_b64_tr_b16 v[70:71], v117 offset:512\n\t" \
    "ds_read_b64_tr_b16 v[84:85], v121 offset:0\n\t" \
    "ds_read_b64_tr_b16 v[86:87], v121 offset:256\n\t" \
    "ds_read_b128 v[56:59], v114 offset:0\n\t" \
    "ds_read_b64_tr_b16 v[72:73], v118 offset:0\n\t" \
    "ds_read_b64_tr_b16 v[74:75], v118 offset:512\n\t" \
    "ds_read_b128 v[60:63], v115 offset:0\n\t" \
    "ds_read_b64_tr_b16 v[76:77], v119 offset:0\n\t" \
    "ds_read_b64_tr_b16 v[78:79], v119 offset:512\n\t" \
    "s_barrier\n\t" \
    "s_waitcnt lgkmcnt(13)\n\t" \
    "v_mfma_f32_32x32x16_bf16 v[0:15], v[64:67], v[48:51], v[0:15]\n\t" \
    "v_lshlrev_b32 v104, 16, v48\n\t" \
    "v_and_b32 v105, 0xffff0000, v48\n\t" \
    "v_add_f32 v96, v96, v104\n\t" \
    "v_fmac_f32 v100, v104, v104\n\t" \
    "v_add_f32 v96, v96, v105\n\t" \
    "v_fmac_f32 v100, v105, v105\n\t" \
    "v_mfma_f32_32x32x16_bf16 v[32:47], v[64:67], v[88:91], v[32:47]\n\t" \
    "ds_read_b64_tr_b16 v[64:65], v116 offset:28672\n\t" \
    "ds_read_b64_tr_b16 v[66:67], v116 offset:29184\n\t" \
    "v_lshlrev_b32 v106, 16, v49\n\t" \
    "v_and_b32 v107, 0xffff0000, v49\n\t" \
    "v_add_f32 v97, v97, v106\n\t" \
    "v_fmac_f32 v101, v106, v106\n\t" \
    "v_add_f32 v97, v97, v107\n\t" \
    "v_fmac_f32 v101, v107, v107\n\t" \
    "s_waitcnt lgkmcnt(13)\n\t" \
    "v_mfma_f32_32x32x16_bf16 v[16:31], v[48:51], v[80:83], v[16:31]\n\t" \
    "v_lshlrev_b32 v108, 16, v50\n\t" \
    "v_and_b32 v109, 0xffff0000, v50\n\t" \
    "v_add_f32 v98, v98, v108\n\t" \
    "v_fmac_f32 v102, v108, v108\n\t" \
    "v_add_f32 v98, v98, v109\n\t" \
    "v_fmac_f32 v102, v109, v109\n\t" \
    "s_cmp_lg_u32 s40, 0\n\t" \
    "s_cbranch_scc1 L_nocs0_%=\n\t" \
    "v_mfma_f32_32x32x16_bf16 v[32:47], v[80:83], v[92:95], v[32:47]\n\t" \
    "L_nocs0_%=:\n\t" \
    "ds_read_b64_tr_b16 v[80:81], v120 offset:28672\n\t" \
    "ds_read_b64_tr_b16 v[82:83], v120 offset:28928\n\t" \
    "v_lshlrev_b32 v110, 16, v51\n\t" \
    "v_and_b32 v111, 0xffff0000, v51\n\t" \
    "v_add_f32 v99, v99, v110\n\t" \
    "v_fmac_f32 v103, v110, v110\n\t" \
    "v_add_f32 v99, v99, v111\n\t" \
    "v_fmac_f32 v103, v111, v111\n\t" \
    "ds_read_b128 v[48:51], v112 offset:28672\n\t" \
    "s_waitcnt lgkmcnt(13)\n\t" \
    "v_mfma_f32_32x32x16_bf16 v[0:15], v[68:71], v[52:55], v[0:15]\n\t" \
    "ds_read_b64_tr_b16 v[68:69], v117 offset:28672\n\t" \
    "ds_read_b64_tr_b16 v[70:71], v117 offset:29184\n\t" \
    "v_lshlrev_b32 v104, 16, v52\n\t" \
    "v_and_b32 v105, 0xffff0000, v52\n\t" \
    "v_add_f32 v96, v96, v104\n\t" \
    "v_fmac_f32 v100, v104, v104\n\t" \
    "v_add_f32 v96, v96, v105\n\t" \
    "v_fmac_f32 v100, v105, v105\n\t" \
    "s_waitcnt lgkmcnt(13)\n\t" \
    "v_mfma_f32_32x32x16_bf16 v[16:31], v[52:55], v[84:87], v[16:31]\n\t" \
    "ds_read_b64_tr_b16 v[84:85], v121 offset:28672\n\t" \
    "ds_read_b64_tr_b16 v[86:87], v121 offset:28928\n\t" \
    "v_lshlrev_b32 v106, 16, v53\n\t" \
    "v_and_b32 v107, 0xffff0000, v53\n\t" \
    "v_add_f32 v97, v97, v106\n\t" \
    "v_fmac_f32 v101, v106, v106\n\t" \
    "v_add_f32 v97, v97, v107\n\t" \
    "v_fmac_f32 v101, v107, v107\n\t" \
    "s_waitcnt lgkmcnt(12)\n\t" \
    "v_mfma_f32_32x32x16_bf16 v[0:15], v[72:75], v[56:59], v[0:15]\n\t" \
    "ds_read_b64_tr_b16 v[72:73], v118 offset:28672\n\t" \
    "ds_read_b64_tr_b16 v[74:75], v118 offset:29184\n\t" \
    "ds_read_b128 v[56:59], v114 offset:28672\n\t" \
    "v_lshlrev_b32 v108, 16, v54\n\t" \
    "v_and_b32 v109, 0xffff0000, v54\n\t" \
    "v_add_f32 v98, v98, v108\n\t" \
    "v_fmac_f32 v102, v108, v108\n\t" \
    "v_add_f32 v98, v98, v109\n\t" \
    "v_fmac_f32 v102, v109, v109\n\t" \
    "s_waitcnt lgkmcnt(12)\n\t" \
    "v_mfma_f32_32x32x16_bf16 v[0:15], v[76:79], v[60:63], v[0:15]\n\t" \
    "ds_read_b64_tr_b16 v[76:77], v119 offset:28672\n\t" \
    "ds_read_b64_tr_b16 v[78:79], v119 offset:29184\n\t" \
    "ds_read_b128 v[60:63], v115 offset:28672\n\t" \
    "v_lshlrev_b32 v110, 16, v55\n\t" \
    "v_and_b32 v111, 0xffff0000, v55\n\t" \
    "v_add_f32 v99, v99, v110\n\t" \
    "v_fmac_f32 v103, v110, v110\n\t" \
    "v_add_f32 v99, v99, v111\n\t" \
    "v_fmac_f32 v103, v111, v111\n\t" \
    "ds_read_b128 v[52:55], v113 offset:28672\n\t" \
    "s_barrier\n\t" \
    "v_add_u32 v112, 0xe000, v112\n\t" \
    "v_add_u32 v113, 0xe000, v113\n\t" \
    "v_add_u32 v114, 0xe000, v114\n\t" \
    "v_add_u32 v115, 0xe000, v115\n\t" \
    "v_add_u32 v116, 0xe000, v116\n\t" \
    "v_add_u32 v117, 0xe000, v117\n\t" \
    "v_add_u32 v118, 0xe000, v118\n\t" \
    "v_add_u32 v119, 0xe000, v119\n\t" \
    "v_add_u32 v120, 0xe000, v120\n\t" \
    "v_add_u32 v121, 0xe000, v121\n\t" \
    "s_waitcnt lgkmcnt(11)\n\t" \
    "v_mfma_f32_32x32x16_bf16 v[0:15], v[64:67], v[48:51], v[0:15]\n\t" \
    "v_lshlrev_b32 v104, 16, v48\n\t" \
    "v_and_b32 v105, 0xffff0000, v48\n\t" \
    "v_add_f32 v96, v96, v104\n\t" \
    "v_fmac_f32 v100, v104, v104\n\t" \
    "v_add_f32 v96, v96, v105\n\t" \
    "v_fmac_f32 v100, v105, v105\n\t" \
    "v_mfma_f32_32x32x16_bf16 v[32:47], v[64:67], v[88:91], v[32:47]\n\t" \
    "ds_read_b64_tr_b16 v[64:65], v116 offset:0\n\t" \
    "ds_read_b64_tr_b16 v[66:67], v116 offset:512\n\t" \
    "v_lshlrev_b32 v106, 16, v49\n\t" \
    "v_and_b32 v107, 0xffff0000, v49\n\t" \
    "v_add_f32 v97, v97, v106\n\t" \
    "v_fmac_f32 v101, v106, v106\n\t" \
    "v_add_f32 v97, v97, v107\n\t" \
    "v_fmac_f32 v101, v107, v107\n\t" \
    "v_mfma_f32_32x32x16_bf16 v[16:31], v[48:51], v[80:83], v[16:31]\n\t" \
    "v_lshlrev_b32 v108, 16, v50\n\t" \
    "v_and_b32 v109, 0xffff0000, v50\n\t" \
    "v_add_f32 v98, v98, v108\n\t" \
    "v_fmac_f32 v102, v108, v108\n\t" \
    "v_add_f32 v98, v98, v109\n\t" \
    "v_fmac_f32 v102, v109, v109\n\t" \
    "s_cmp_lg_u32 s40, 0\n\t" \
    "s_cbranch_scc1 L_nocs1_%=\n\t" \
    "v_mfma_f32_32x32x16_bf16 v[32:47], v[80:83], v[92:95], v[32:47]\n\t" \
    "L_nocs1_%=:\n\t" \
    "ds_read_b64_tr_b16 v[80:81], v120 offset:0\n\t" \
    "ds_read_b64_tr_b16 v[82:83], v120 offset:256\n\t" \
    "v_lshlrev_b32 v110, 16, v51\n\t" \
    "v_and_b32 v111, 0xffff0000, v51\n\t" \
    "v_add_f32 v99, v99, v110\n\t" \
    "v_fmac_f32 v103, v110, v110\n\t" \
    "v_add_f32 v99, v99, v111\n\t" \
    "v_fmac_f32 v103, v111, v111\n\t" \
    "ds_read_b128 v[48:51], v112 offset:0\n\t" \
    "s_waitcnt lgkmcnt(5)\n\t" \
    "v_mfma_f32_32x32x16_bf16 v[0:15], v[68:71], v[52:55], v[0:15]\n\t" \
    "ds_read_b64_tr_b16 v[68:69], v117 offset:0\n\t" \
    "ds_read_b64_tr_b16 v[70:71], v117 offset:512\n\t" \
    "v_lshlrev_b32 v104, 16, v52\n\t" \
    "v_and_b32 v105, 0xffff0000, v52\n\t" \
    "v_add_f32 v96, v96, v104\n\t" \
    "v_fmac_f32 v100, v104, v104\n\t" \
    "v_add_f32 v96, v96, v105\n\t" \
    "v_fmac_f32 v100, v105, v105\n\t" \
    "v_mfma_f32_32x32x16_bf16 v[16:31], v[52:55], v[84:87], v[16:31]\n\t" \
    "ds_read_b64_tr_b16 v[84:85], v121 offset:0\n\t" \
    "ds_read_b64_tr_b16 v[86:87], v121 offset:256\n\t" \
    "v_lshlrev_b32 v106, 16, v53\n\t" \
    "v_and_b32 v107, 0xffff0000, v53\n\t" \
    "v_add_f32 v97, v97, v106\n\t" \
    "v_fmac_f32 v101, v106, v106\n\t" \
    "v_add_f32 v97, v97, v107\n\t" \
    "v_fmac_f32 v101, v107, v107\n\t" \
    "v_mfma_f32_32x32x16_bf16 v[0:15], v[72:75], v[56:59], v[0:15]\n\t" \
    "ds_read_b64_tr_b16 v[72:73], v118 offset:0\n\t" \
    "ds_read_b64_tr_b16 v[74:75], v118 offset:512\n\t" \
    "ds_read_b128 v[56:59], v114 offset:0\n\t" \
    "v_lshlrev_b32 v108, 16, v54\n\t" \
    "v_and_b32 v109, 0xffff0000, v54\n\t" \
    "v_add_f32 v98, v98, v108\n\t" \
    "v_fmac_f32 v102, v108, v108\n\t" \
    "v_add_f32 v98, v98, v109\n\t" \
    "v_fmac_f32 v102, v109, v109\n\t" \
    "v_mfma_f32_32x32x16_bf16 v[0:15], v[76:79], v[60:63], v[0:15]\n\t" \
    "ds_read_b64_tr_b16 v[76:77], v119 offset:0\n\t" \
    "ds_read_b64_tr_b16 v[78:79], v119 offset:512\n\t" \
    "ds_read_b128 v[60:63], v115 offset:0\n\t" \
    "v_lshlrev_b32 v110, 16, v55\n\t" \
    "v_and_b32 v111, 0xffff0000, v55\n\t" \
    "v_add_f32 v99, v99, v110\n\t" \
    "v_fmac_f32 v103, v110, v110\n\t" \
    "v_add_f32 v99, v99, v111\n\t" \
    "v_fmac_f32 v103, v111, v111\n\t" \
    "ds_read_b128 v[52:55], v113 offset:0\n\t" \
    "s_barrier\n\t" \
    "s_cmp_eq_u32 s42, 0\n\t" \
    "s_cbranch_scc1 L_nf1_%=\n\t" \
    "buffer_load_dwordx2 v[132:133], off, s[60:63], s43 sc1\n\t" \
    "L_nf1_%=:\n\t" \
    "s_waitcnt lgkmcnt(11)\n\t" \
    "v_mfma_f32_32x32x16_bf16 v[0:15], v[64:67], v[48:51], v[0:15]\n\t" \
    "v_lshlrev_b32 v104, 16, v48\n\t" \
    "v_and_b32 v105, 0xffff0000, v48\n\t" \
    "v_add_f32 v96, v96, v104\n\t" \
    "v_fmac_f32 v100, v104, v104\n\t" \
    "v_add_f32 v96, v96, v105\n\t" \
    "v_fmac_f32 v100, v105, v105\n\t" \
    "v_mfma_f32_32x32x16_bf16 v[32:47], v[64:67], v[88:91], v[32:47]\n\t" \
    "ds_read_b64_tr_b16 v[64:65], v116 offset:28672\n\t" \
    "ds_read_b64_tr_b16 v[66:67], v116 offset:29184\n\t" \
    "v_lshlrev_b32 v106, 16, v49\n\t" \
    "v_and_b32 v107, 0xffff0000, v49\n\t" \
    "v_add_f32 v97, v97, v106\n\t" \
    "v_fmac_f32 v101, v106, v106\n\t" \
    "v_add_f32 v97, v97, v107\n\t" \
    "v_fmac_f32 v101, v107, v107\n\t" \
    "v_mfma_f32_32x32x16_bf16 v[16:31], v[48:51], v[80:83], v[16:31]\n\t" \
    "v_lshlrev_b32 v108, 16, v50\n\t" \
    "v_and_b32 v109, 0xffff0000, v50\n\t" \
    "v_add_f32 v98, v98, v108\n\t" \
    "v_fmac_f32 v102, v108, v108\n\t" \
    "v_add_f32 v98, v98, v109\n\t" \
    "v_fmac_f32 v102, v109, v109\n\t" \
    "s_cmp_lg_u32 s40, 0\n\t" \
    "s_cbranch_scc1 L_nocs2_%=\n\t" \
    "v_mfma_f32_32x32x16_bf16 v[32:47], v[80:83], v[92:95], v[32:47]\n\t" \
    "L_nocs2_%=:\n\t" \
    "ds_read_b64_tr_b16 v[80:81], v120 offset:28672\n\t" \
    "ds_read_b64_tr_b16 v[82:83], v120 offset:28928\n\t" \
    "v_lshlrev_b32 v110, 16, v51\n\t" \
    "v_and_b32 v111, 0xffff0000, v51\n\t" \
    "v_add_f32 v99, v99, v110\n\t" \
    "v_fmac_f32 v103, v110, v110\n\t" \
    "v_add_f32 v99, v99, v111\n\t" \
    "v_fmac_f32 v103, v111, v111\n\t" \
    "ds_read_b128 v[48:51], v112 offset:28672\n\t" \
    "s_waitcnt lgkmcnt(5)\n\t" \
    "v_mfma_f32_32x32x16_bf16 v[0:15], v[68:71], v[52:55], v[0:15]\n\t" \
    "ds_read_b64_tr_b16 v[68:69], v117 offset:28672\n\t" \
    "ds_read_b64_tr_b16 v[70:71], v117 offset:29184\n\t" \
    "v_lshlrev_b32 v104, 16, v52\n\t" \
    "v_and_b32 v105, 0xffff0000, v52\n\t" \
    "v_add_f32 v96, v96, v104\n\t" \
    "v_fmac_f32 v100, v104, v104\n\t" \
    "v_add_f32 v96, v96, v105\n\t" \
    "v_fmac_f32 v100, v105, v105\n\t" \
    "v_mfma_f32_32x32x16_bf16 v[16:31], v[52:55], v[84:87], v[16:31]\n\t" \
    "ds_read_b64_tr_b16 v[84:85], v121 offset:28672\n\t" \
    "ds_read_b64_tr_b16 v[86:87], v121 offset:28928\n\t" \
    "v_lshlrev_b32 v106, 16, v53\n\t" \
    "v_and_b32 v107, 0xffff0000, v53\n\t" \
    "v_add_f32 v97, v97, v106\n\t" \
    "v_fmac_f32 v101, v106, v106\n\t" \
    "v_add_f32 v97, v97, v107\n\t" \
    "v_fmac_f32 v101, v107, v107\n\t" \
    "v_mfma_f32_32x32x16_bf16 v[0:15], v[72:75], v[56:59], v[0:15]\n\t" \
    "ds_read_b64_tr_b16 v[72:73], v118 offset:28672\n\t" \
    "ds_read_b64_tr_b16 v[74:75], v118 offset:29184\n\t" \
    "ds_read_b128 v[56:59], v114 offset:28672\n\t" \
    "v_lshlrev_b32 v108, 16, v54\n\t" \
    "v_and_b32 v109, 0xffff0000, v54\n\t" \
    "v_add_f32 v98, v98, v108\n\t" \
    "v_fmac_f32 v102, v108, v108\n\t" \
    "v_add_f32 v98, v98, v109\n\t" \
    "v_fmac_f32 v102, v109, v109\n\t" \
    "v_mfma_f32_32x32x16_bf16 v[0:15], v[76:79], v[60:63], v[0:15]\n\t" \
    "ds_read_b64_tr_b16 v[76:77], v119 offset:28672\n\t" \
    "ds_read_b64_tr_b16 v[78:79], v119 offset:29184\n\t" \
    "ds_read_b128 v[60:63], v115 offset:28672\n\t" \
    "v_lshlrev_b32 v110, 16, v55\n\t" \
    "v_and_b32 v111, 0xffff0000, v55\n\t" \
    "v_add_f32 v99, v99, v110\n\t" \
    "v_fmac_f32 v103, v110, v110\n\t" \
    "v_add_f32 v99, v99, v111\n\t" \
    "v_fmac_f32 v103, v111, v111\n\t" \
    "ds_read_b128 v[52:55], v113 offset:28672\n\t" \
    "s_barrier\n\t" \
    "v_add_u32 v112, 0xffff2000, v112\n\t" \
    "v_add_u32 v113, 0xffff2000, v113\n\t" \
    "v_add_u32 v114, 0xffff2000, v114\n\t" \
    "v_add_u32 v115, 0xffff2000, v115\n\t" \
    "v_add_u32 v116, 0xffff2000, v116\n\t" \
    "v_add_u32 v117, 0xffff2000, v117\n\t" \
    "v_add_u32 v118, 0xffff2000, v118\n\t" \
    "v_add_u32 v119, 0xffff2000, v119\n\t" \
    "v_add_u32 v120, 0xffff2000, v120\n\t" \
    "v_add_u32 v121, 0xffff2000, v121\n\t" \
    "s_cmp_eq_u32 s42, 0\n\t" \
    "s_cbranch_scc1 L_nf2_%=\n\t" \
    "s_mov_b32 s70, 0\n\t" \
    "L_fp_%=:\n\t" \
    "s_waitcnt vmcnt(0)\n\t" \
    "v_readfirstlane_b32 s72, v132\n\t" \
    "v_readfirstlane_b32 s73, v133\n\t" \
    "s_add_u32 s70, s70, 1\n\t" \
    "s_cmp_ge_u32 s70, 0x400000\n\t" \
    "s_cbranch_scc1 L_nf2_%=\n\t" \
    "s_xor_b32 s72, s72, 0x7f4a7c15\n\t" \
    "s_xor_b32 s73, s73, 0x9e3779b9\n\t" \
    "s_or_b32 s72, s72, s73\n\t" \
    "s_cmp_eq_u32 s72, 0\n\t" \
    "s_cbranch_scc1 L_nf2_%=\n\t" \
    "buffer_load_dwordx2 v[132:133], off, s[60:63], s43 sc1\n\t" \
    "s_branch L_fp_%=\n\t" \
    "L_nf2_%=:\n\t" \
    "s_waitcnt lgkmcnt(11)\n\t" \
    "v_mfma_f32_32x32x16_bf16 v[0:15], v[64:67], v[48:51], v[0:15]\n\t" \
    "v_lshlrev_b32 v104, 16, v48\n\t" \
    "v_and_b32 v105, 0xffff0000, v48\n\t" \
    "v_add_f32 v96, v96, v104\n\t" \
    "v_fmac_f32 v100, v104, v104\n\t" \
    "v_add_f32 v96, v96, v105\n\t" \
    "v_fmac_f32 v100, v105, v105\n\t" \
    "v_mfma_f32_32x32x16_bf16 v[32:47], v[64:67], v[88:91], v[32:47]\n\t" \
    "ds_read_b64_tr_b16 v[64:65], v116 offset:0\n\t" \
    "ds_read_b64_tr_b16 v[66:67], v116 offset:512\n\t" \
    "v_lshlrev_b32 v106, 16, v49\n\t" \
    "v_and_b32 v107, 0xffff0000, v49\n\t" \
    "v_add_f32 v97, v97, v106\n\t" \
    "v_fmac_f32 v101, v106, v106\n\t" \
    "v_add_f32 v97, v97, v107\n\t" \
    "v_fmac_f32 v101, v107, v107\n\t" \
    "v_mfma_f32_32x32x16_bf16 v[16:31], v[48:51], v[80:83], v[16:31]\n\t" \
    "v_lshlrev_b32 v108, 16, v50\n\t" \
    "v_and_b32 v109, 0xffff0000, v50\n\t" \
    "v_add_f32 v98, v98, v108\n\t" \
    "v_fmac_f32 v102, v108, v108\n\t" \
    "v_add_f32 v98, v98, v109\n\t" \
    "v_fmac_f32 v102, v109, v109\n\t" \
    "s_cmp_lg_u32 s40, 0\n\t" \
    "s_cbranch_scc1 L_nocs3_%=\n\t" \
    "v_mfma_f32_32x32x16_bf16 v[32:47], v[80:83], v[92:95], v[32:47]\n\t" \
    "L_nocs3_%=:\n\t" \
    "ds_read_b64_tr_b16 v[80:81], v120 offset:0\n\t" \
    "ds_read_b64_tr_b16 v[82:83], v120 offset:256\n\t" \
    "v_lshlrev_b32 v110, 16, v51\n\t" \
    "v_and_b32 v111, 0xffff0000, v51\n\t" \
    "v_add_f32 v99, v99, v110\n\t" \
    "v_fmac_f32 v103, v110, v110\n\t" \
    "v_add_f32 v99, v99, v111\n\t" \
    "v_fmac_f32 v103, v111, v111\n\t" \
    "ds_read_b128 v[48:51], v112 offset:0\n\t" \
    "s_waitcnt lgkmcnt(5)\n\t" \
    "v_mfma_f32_32x32x16_bf16 v[0:15], v[68:71], v[52:55], v[0:15]\n\t" \
    "ds_read_b64_tr_b16 v[68:69], v117 offset:0\n\t" \
    "ds_read_b64_tr_b16 v[70:71], v117 offset:512\n\t" \
    "v_lshlrev_b32 v104, 16, v52\n\t" \
    "v_and_b32 v105, 0xffff0000, v52\n\t" \
    "v_add_f32 v96, v96, v104\n\t" \
    "v_fmac_f32 v100, v104, v104\n\t" \
    "v_add_f32 v96, v96, v105\n\t" \
    "v_fmac_f32 v100, v105, v105\n\t" \
    "v_mfma_f32_32x32x16_bf16 v[16:31], v[52:55], v[84:87], v[16:31]\n\t" \
    "ds_read_b64_tr_b16 v[84:85], v121 offset:0\n\t" \
    "ds_read_b64_tr_b16 v[86:87], v121 offset:256\n\t" \
    "v_lshlrev_b32 v106, 16, v53\n\t" \
    "v_and_b32 v107, 0xffff0000, v53\n\t" \
    "v_add_f32 v97, v97, v106\n\t" \
    "v_fmac_f32 v101, v106, v106\n\t" \
    "v_add_f32 v97, v97, v107\n\t" \
    "v_fmac_f32 v101, v107, v107\n\t" \
    "v_mfma_f32_32x32x16_bf16 v[0:15], v[72:75], v[56:59], v[0:15]\n\t" \
    "ds_read_b64_tr_b16 v[72:73], v118 offset:0\n\t" \
    "ds_read_b64_tr_b16 v[74:75], v118 offset:512\n\t" \
    "ds_read_b128 v[56:59], v114 offset:0\n\t" \
    "v_lshlrev_b32 v108, 16, v54\n\t" \
    "v_and_b32 v109, 0xffff0000, v54\n\t" \
    "v_add_f32 v98, v98, v108\n\t" \
    "v_fmac_f32 v102, v108, v108\n\t" \
    "v_add_f32 v98, v98, v109\n\t" \
    "v_fmac_f32 v102, v109, v109\n\t" \
    "v_mfma_f32_32x32x16_bf16 v[0:15], v[76:79], v[60:63], v[0:15]\n\t" \
    "ds_read_b64_tr_b16 v[76:77], v119 offset:0\n\t" \
    "ds_read_b64_tr_b16 v[78:79], v119 offset:512\n\t" \
    "ds_read_b128 v[60:63], v115 offset:0\n\t" \
    "v_lshlrev_b32 v110, 16, v55\n\t" \
    "v_and_b32 v111, 0xffff0000, v55\n\t" \
    "v_add_f32 v99, v99, v110\n\t" \
    "v_fmac_f32 v103, v110, v110\n\t" \
    "v_add_f32 v99, v99, v111\n\t" \
    "v_fmac_f32 v103, v111, v111\n\t" \
    "ds_read_b128 v[52:55], v113 offset:0\n\t" \
    "s_barrier\n\t" \
    "s_waitcnt lgkmcnt(11)\n\t" \
    "v_mfma_f32_32x32x16_bf16 v[0:15], v[64:67], v[48:51], v[0:15]\n\t" \
    "v_lshlrev_b32 v104, 16, v48\n\t" \
    "v_and_b32 v105, 0xffff0000, v48\n\t" \
    "v_add_f32 v96, v96, v104\n\t" \
    "v_fmac_f32 v100, v104, v104\n\t" \
    "v_add_f32 v96, v96, v105\n\t" \
    "v_fmac_f32 v100, v105, v105\n\t" \
    "v_mfma_f32_32x32x16_bf16 v[32:47], v[64:67], v[88:91], v[32:47]\n\t" \
    "ds_read_b64_tr_b16 v[64:65], v116 offset:28672\n\t" \
    "ds_read_b64_tr_b16 v[66:67], v116 offset:29184\n\t" \
    "v_lshlrev_b32 v106, 16, v49\n\t" \
    "v_and_b32 v107, 0xffff0000, v49\n\t" \
    "v_add_f32 v97, v97, v106\n\t" \
    "v_fmac_f32 v101, v106, v106\n\t" \
    "v_add_f32 v97, v97, v107\n\t" \
    "v_fmac_f32 v101, v107, v107\n\t" \
    "v_mfma_f32_32x32x16_bf16 v[16:31], v[48:51], v[80:83], v[16:31]\n\t" \
    "v_lshlrev_b32 v108, 16, v50\n\t" \
    "v_and_b32 v109, 0xffff0000, v50\n\t" \
    "v_add_f32 v98, v98, v108\n\t" \
    "v_fmac_f32 v102, v108, v108\n\t" \
    "v_add_f32 v98, v98, v109\n\t" \
    "v_fmac_f32 v102, v109, v109\n\t" \
    "s_cmp_lg_u32 s40, 0\n\t" \
    "s_cbranch_scc1 L_nocs4_%=\n\t" \
    "v_mfma_f32_32x32x16_bf16 v[32:47], v[80:83], v[92:95], v[32:47]\n\t" \
    "L_nocs4_%=:\n\t" \
    "ds_read_b64_tr_b16 v[80:81], v120 offset:28672\n\t" \
    "ds_read_b64_tr_b16 v[82:83], v120 offset:28928\n\t" \
    "v_lshlrev_b32 v110, 16, v51\n\t" \
    "v_and_b32 v111, 0xffff0000, v51\n\t" \
    "v_add_f32 v99, v99, v110\n\t" \
    "v_fmac_f32 v103, v110, v110\n\t" \
    "v_add_f32 v99, v99, v111\n\t" \
    "v_fmac_f32 v103, v111, v111\n\t" \
    "ds_read_b128 v[48:51], v112 offset:28672\n\t" \
    "s_waitcnt lgkmcnt(5)\n\t" \
    "v_mfma_f32_32x32x16_bf16 v[0:15], v[68:71], v[52:55], v[0:15]\n\t" \
    "ds_read_b64_tr_b16 v[68:69], v117 offset:28672\n\t" \
    "ds_read_b64_tr_b16 v[70:71], v117 offset:29184\n\t" \
    "v_lshlrev_b32 v104, 16, v52\n\t" \
    "v_and_b32 v105, 0xffff0000, v52\n\t" \
    "v_add_f32 v96, v96, v104\n\t" \
    "v_fmac_f32 v100, v104, v104\n\t" \
    "v_add_f32 v96, v96, v105\n\t" \
    "v_fmac_f32 v100, v105, v105\n\t" \
    "v_mfma_f32_32x32x16_bf16 v[16:31], v[52:55], v[84:87], v[16:31]\n\t" \
    "ds_read_b64_tr_b16 v[84:85], v121 offset:28672\n\t" \
    "ds_read_b64_tr_b16 v[86:87], v121 offset:28928\n\t" \
    "v_lshlrev_b32 v106, 16, v53\n\t" \
    "v_and_b32 v107, 0xffff0000, v53\n\t" \
    "v_add_f32 v97, v97, v106\n\t" \
    "v_fmac_f32 v101, v106, v106\n\t" \
    "v_add_f32 v97, v97, v107\n\t" \
    "v_fmac_f32 v101, v107, v107\n\t" \
    "v_mfma_f32_32x32x16_bf16 v[0:15], v[72:75], v[56:59], v[0:15]\n\t" \
    "ds_read_b64_tr_b16 v[72:73], v118 offset:28672\n\t" \
    "ds_read_b64_tr_b16 v[74:75], v118 offset:29184\n\t" \
    "ds_read_b128 v[56:59], v114 offset:28672\n\t" \
    "v_lshlrev_b32 v108, 16, v54\n\t" \
    "v_and_b32 v109, 0xffff0000, v54\n\t" \
    "v_add_f32 v98, v98, v108\n\t" \
    "v_fmac_f32 v102, v108, v108\n\t" \
    "v_add_f32 v98, v98, v109\n\t" \
    "v_fmac_f32 v102, v109, v109\n\t" \
    "v_mfma_f32_32x32x16_bf16 v[0:15], v[76:79], v[60:63], v[0:15]\n\t" \
    "ds_read_b64_tr_b16 v[76:77], v119 offset:28672\n\t" \
    "ds_read_b64_tr_b16 v[78:79], v119 offset:29184\n\t" \
    "ds_read_b128 v[60:63], v115 offset:28672\n\t" \
    "v_lshlrev_b32 v110, 16, v55\n\t" \
    "v_and_b32 v111, 0xffff0000, v55\n\t" \
    "v_add_f32 v99, v99, v110\n\t" \
    "v_fmac_f32 v103, v110, v110\n\t" \
    "v_add_f32 v99, v99, v111\n\t" \
    "v_fmac_f32 v103, v111, v111\n\t" \
    "ds_read_b128 v[52:55], v113 offset:28672\n\t" \
    "s_barrier\n\t" \
    "v_add_u32 v112, 0xe000, v112\n\t" \
    "v_add_u32 v113, 0xe000, v113\n\t" \
    "v_add_u32 v114, 0xe000, v114\n\t" \
    "v_add_u32 v115, 0xe000, v115\n\t" \
    "v_add_u32 v116, 0xe000, v116\n\t" \
    "v_add_u32 v117, 0xe000, v117\n\t" \
    "v_add_u32 v118, 0xe000, v118\n\t" \
    "v_add_u32 v119, 0xe000, v119\n\t" \
    "v_add_u32 v120, 0xe000, v120\n\t" \
    "v_add_u32 v121, 0xe000, v121\n\t" \
    "s_waitcnt lgkmcnt(11)\n\t" \
    "v_mfma_f32_32x32x16_bf16 v[0:15], v[64:67], v[48:51], v[0:15]\n\t" \
    "v_lshlrev_b32 v104, 16, v48\n\t" \
    "v_and_b32 v105, 0xffff0000, v48\n\t" \
    "v_add_f32 v96, v96, v104\n\t" \
    "v_fmac_f32 v100, v104, v104\n\t" \
    "v_add_f32 v96, v96, v105\n\t" \
    "v_fmac_f32 v100, v105, v105\n\t" \
    "v_mfma_f32_32x32x16_bf16 v[32:47], v[64:67], v[88:91], v[32:47]\n\t" \
    "ds_read_b64_tr_b16 v[64:65], v116 offset:0\n\t" \
    "ds_read_b64_tr_b16 v[66:67], v116 offset:512\n\t" \
    "v_lshlrev_b32 v106, 16, v49\n\t" \
    "v_and_b32 v107, 0xffff0000, v49\n\t" \
    "v_add_f32 v97, v97, v106\n\t" \
    "v_fmac_f32 v101, v106, v106\n\t" \
    "v_add_f32 v97, v97, v107\n\t" \
    "v_fmac_f32 v101, v107, v107\n\t" \
    "v_mfma_f32_32x32x16_bf16 v[16:31], v[48:51], v[80:83], v[16:31]\n\t" \
    "v_lshlrev_b32 v108, 16, v50\n\t" \
    "v_and_b32 v109, 0xffff0000, v50\n\t" \
    "v_add_f32 v98, v98, v108\n\t" \
    "v_fmac_f32 v102, v108, v108\n\t" \
    "v_add_f32 v98, v98, v109\n\t" \
    "v_fmac_f32 v102, v109, v109\n\t" \
    "s_cmp_lg_u32 s40, 0\n\t" \
    "s_cbranch_scc1 L_nocs5_%=\n\t" \
    "v_mfma_f32_32x32x16_bf16 v[32:47], v[80:83], v[92:95], v[32:47]\n\t" \
    "L_nocs5_%=:\n\t" \
    "ds_read_b64_tr_b16 v[80:81], v120 offset:0\n\t" \
    "ds_read_b64_tr_b16 v[82:83], v120 offset:256\n\t" \
    "v_lshlrev_b32 v110, 16, v51\n\t" \
    "v_and_b32 v111, 0xffff0000, v51\n\t" \
    "v_add_f32 v99, v99, v110\n\t" \
    "v_fmac_f32 v103, v110, v110\n\t" \
    "v_add_f32 v99, v99, v111\n\t" \
    "v_fmac_f32 v103, v111, v111\n\t" \
    "ds_read_b128 v[48:51], v112 offset:0\n\t" \
    "s_waitcnt lgkmcnt(5)\n\t" \
    "v_mfma_f32_32x32x16_bf16 v[0:15], v[68:71], v[52:55], v[0:15]\n\t" \
    "ds_read_b64_tr_b16 v[68:69], v117 offset:0\n\t" \
    "ds_read_b64_tr_b16 v[70:71], v117 offset:512\n\t" \
    "v_lshlrev_b32 v104, 16, v52\n\t" \
    "v_and_b32 v105, 0xffff0000, v52\n\t" \
    "v_add_f32 v96, v96, v104\n\t" \
    "v_fmac_f32 v100, v104, v104\n\t" \
    "v_add_f32 v96, v96, v105\n\t" \
    "v_fmac_f32 v100, v105, v105\n\t" \
    "v_mfma_f32_32x32x16_bf16 v[16:31], v[52:55], v[84:87], v[16:31]\n\t" \
    "ds_read_b64_tr_b16 v[84:85], v121 offset:0\n\t" \
    "ds_read_b64_tr_b16 v[86:87], v121 offset:256\n\t" \
    "v_lshlrev_b32 v106, 16, v53\n\t" \
    "v_and_b32 v107, 0xffff0000, v53\n\t" \
    "v_add_f32 v97, v97, v106\n\t" \
    "v_fmac_f32 v101, v106, v106\n\t" \
    "v_add_f32 v97, v97, v107\n\t" \
    "v_fmac_f32 v101, v107, v107\n\t" \
    "v_mfma_f32_32x32x16_bf16 v[0:15], v[72:75], v[56:59], v[0:15]\n\t" \
    "ds_read_b64_tr_b16 v[72:73], v118 offset:0\n\t" \
    "ds_read_b64_tr_b16 v[74:75], v118 offset:512\n\t" \
    "ds_read_b128 v[56:59], v114 offset:0\n\t" \
    "v_lshlrev_b32 v108, 16, v54\n\t" \
    "v_and_b32 v109, 0xffff0000, v54\n\t" \
    "v_add_f32 v98, v98, v108\n\t" \
    "v_fmac_f32 v102, v108, v108\n\t" \
    "v_add_f32 v98, v98, v109\n\t" \
    "v_fmac_f32 v102, v109, v109\n\t" \
    "v_mfma_f32_32x32x16_bf16 v[0:15], v[76:79], v[60:63], v[0:15]\n\t" \
    "ds_read_b64_tr_b16 v[76:77], v119 offset:0\n\t" \
    "ds_read_b64_tr_b16 v[78:79], v119 offset:512\n\t" \
    "ds_read_b128 v[60:63], v115 offset:0\n\t" \
    "v_lshlrev_b32 v110, 16, v55\n\t" \
    "v_and_b32 v111, 0xffff0000, v55\n\t" \
    "v_add_f32 v99, v99, v110\n\t" \
    "v_fmac_f32 v103, v110, v110\n\t" \
    "v_add_f32 v99, v99, v111\n\t" \
    "v_fmac_f32 v103, v111, v111\n\t" \
    "ds_read_b128 v[52:55], v113 offset:0\n\t" \
    "s_barrier\n\t" \
    "s_waitcnt lgkmcnt(11)\n\t" \
    "v_mfma_f32_32x32x16_bf16 v[0:15], v[64:67], v[48:51], v[0:15]\n\t" \
    "v_lshlrev_b32 v104, 16, v48\n\t" \
    "v_and_b32 v105, 0xffff0000, v48\n\t" \
    "v_add_f32 v96, v96, v104\n\t" \
    "v_fmac_f32 v100, v104, v104\n\t" \
    "v_add_f32 v96, v96, v105\n\t" \
    "v_fmac_f32 v100, v105, v105\n\t" \
    "v_mfma_f32_32x32x16_bf16 v[32:47], v[64:67], v[88:91], v[32:47]\n\t" \
    "ds_read_b64_tr_b16 v[64:65], v116 offset:28672\n\t" \
    "ds_read_b64_tr_b16 v[66:67], v116 offset:29184\n\t" \
    "v_lshlrev_b32 v106, 16, v49\n\t" \
    "v_and_b32 v107, 0xffff0000, v49\n\t" \
    "v_add_f32 v97, v97, v106\n\t" \
    "v_fmac_f32 v101, v106, v106\n\t" \
    "v_add_f32 v97, v97, v107\n\t" \
    "v_fmac_f32 v101, v107, v107\n\t" \
    "v_mfma_f32_32x32x16_bf16 v[16:31], v[48:51], v[80:83], v[16:31]\n\t" \
    "v_lshlrev_b32 v108, 16, v50\n\t" \
    "v_and_b32 v109, 0xffff0000, v50\n\t" \
    "v_add_f32 v98, v98, v108\n\t" \
    "v_fmac_f32 v102, v108, v108\n\t" \
    "v_add_f32 v98, v98, v109\n\t" \
    "v_fmac_f32 v102, v109, v109\n\t" \
    "s_cmp_lg_u32 s40, 0\n\t" \
    "s_cbranch_scc1 L_nocs6_%=\n\t" \
    "v_mfma_f32_32x32x16_bf16 v[32:47], v[80:83], v[92:95], v[32:47]\n\t" \
    "L_nocs6_%=:\n\t" \
    "ds_read_b64_tr_b16 v[80:81], v120 offset:28672\n\t" \
    "ds_read_b64_tr_b16 v[82:83], v120 offset:28928\n\t" \
    "v_lshlrev_b32 v110, 16, v51\n\t" \
    "v_and_b32 v111, 0xffff0000, v51\n\t" \
    "v_add_f32 v99, v99, v110\n\t" \
    "v_fmac_f32 v103, v110, v110\n\t" \
    "v_add_f32 v99, v99, v111\n\t" \
    "v_fmac_f32 v103, v111, v111\n\t" \
    "ds_read_b128 v[48:51], v112 offset:28672\n\t" \
    "s_waitcnt lgkmcnt(5)\n\t" \
    "v_mfma_f32_32x32x16_bf16 v[0:15], v[68:71], v[52:55], v[0:15]\n\t" \
    "ds_read_b64_tr_b16 v[68:69], v117 offset:28672\n\t" \
    "ds_read_b64_tr_b16 v[70:71], v117 offset:29184\n\t" \
    "v_lshlrev_b32 v104, 16, v52\n\t" \
    "v_and_b32 v105, 0xffff0000, v52\n\t" \
    "v_add_f32 v96, v96, v104\n\t" \
    "v_fmac_f32 v100, v104, v104\n\t" \
    "v_add_f32 v96, v96, v105\n\t" \
    "v_fmac_f32 v100, v105, v105\n\t" \
    "v_mfma_f32_32x32x16_bf16 v[16:31], v[52:55], v[84:87], v[16:31]\n\t" \
    "ds_read_b64_tr_b16 v[84:85], v121 offset:28672\n\t" \
    "ds_read_b64_tr_b16 v[86:87], v121 offset:28928\n\t" \
    "v_lshlrev_b32 v106, 16, v53\n\t" \
    "v_and_b32 v107, 0xffff0000, v53\n\t" \
    "v_add_f32 v97, v97, v106\n\t" \
    "v_fmac_f32 v101, v106, v106\n\t" \
    "v_add_f32 v97, v97, v107\n\t" \
    "v_fmac_f32 v101, v107, v107\n\t" \
    "v_mfma_f32_32x32x16_bf16 v[0:15], v[72:75], v[56:59], v[0:15]\n\t" \
    "ds_read_b64_tr_b16 v[72:73], v118 offset:28672\n\t" \
    "ds_read_b64_tr_b16 v[74:75], v118 offset:29184\n\t" \
    "ds_read_b128 v[56:59], v114 offset:28672\n\t" \
    "v_lshlrev_b32 v108, 16, v54\n\t" \
    "v_and_b32 v109, 0xffff0000, v54\n\t" \
    "v_add_f32 v98, v98, v108\n\t" \
    "v_fmac_f32 v102, v108, v108\n\t" \
    "v_add_f32 v98, v98, v109\n\t" \
    "v_fmac_f32 v102, v109, v109\n\t" \
    "v_mfma_f32_32x32x16_bf16 v[0:15], v[76:79], v[60:63], v[0:15]\n\t" \
    "ds_read_b64_tr_b16 v[76:77], v119 offset:28672\n\t" \
    "ds_read_b64_tr_b16 v[78:79], v119 offset:29184\n\t" \
    "ds_read_b128 v[60:63], v115 offset:28672\n\t" \
    "v_lshlrev_b32 v110, 16, v55\n\t" \
    "v_and_b32 v111, 0xffff0000, v55\n\t" \
    "v_add_f32 v99, v99, v110\n\t" \
    "v_fmac_f32 v103, v110, v110\n\t" \
    "v_add_f32 v99, v99, v111\n\t" \
    "v_fmac_f32 v103, v111, v111\n\t" \
    "ds_read_b128 v[52:55], v113 offset:28672\n\t" \
    "s_waitcnt lgkmcnt(11)\n\t" \
    "v_mfma_f32_32x32x16_bf16 v[0:15], v[64:67], v[48:51], v[0:15]\n\t" \
    "v_lshlrev_b32 v104, 16, v48\n\t" \
    "v_and_b32 v105, 0xffff0000, v48\n\t" \
    "v_add_f32 v96, v96, v104\n\t" \
    "v_fmac_f32 v100, v104, v104\n\t" \
    "v_add_f32 v96, v96, v105\n\t" \
    "v_fmac_f32 v100, v105, v105\n\t" \
    "v_mfma_f32_32x32x16_bf16 v[32:47], v[64:67], v[88:91], v[32:47]\n\t" \
    "v_lshlrev_b32 v106, 16, v49\n\t" \
    "v_and_b32 v107, 0xffff0000, v49\n\t" \
    "v_add_f32 v97, v97, v106\n\t" \
    "v_fmac_f32 v101, v106, v106\n\t" \
    "v_add_f32 v97, v97, v107\n\t" \
    "v_fmac_f32 v101, v107, v107\n\t" \
    "v_mfma_f32_32x32x16_bf16 v[16:31], v[48:51], v[80:83], v[16:31]\n\t" \
    "v_lshlrev_b32 v108, 16, v50\n\t" \
    "v_and_b32 v109, 0xffff0000, v50\n\t" \
    "v_add_f32 v98, v98, v108\n\t" \
    "v_fmac_f32 v102, v108, v108\n\t" \
    "v_add_f32 v98, v98, v109\n\t" \
    "v_fmac_f32 v102, v109, v109\n\t" \
    "s_cmp_lg_u32 s40, 0\n\t" \
    "s_cbranch_scc1 L_nocs7_%=\n\t" \
    "v_mfma_f32_32x32x16_bf16 v[32:47], v[80:83], v[92:95], v[32:47]\n\t" \
    "L_nocs7_%=:\n\t" \
    "v_lshlrev_b32 v110, 16, v51\n\t" \
    "v_and_b32 v111, 0xffff0000, v51\n\t" \
    "v_add_f32 v99, v99, v110\n\t" \
    "v_fmac_f32 v103, v110, v110\n\t" \
    "v_add_f32 v99, v99, v111\n\t" \
    "v_fmac_f32 v103, v111, v111\n\t" \
    "s_waitcnt lgkmcnt(0)\n\t" \
    "v_mfma_f32_32x32x16_bf16 v[0:15], v[68:71], v[52:55], v[0:15]\n\t" \
    "v_lshlrev_b32 v104, 16, v52\n\t" \
    "v_and_b32 v105, 0xffff0000, v52\n\t" \
    "v_add_f32 v96, v96, v104\n\t" \
    "v_fmac_f32 v100, v104, v104\n\t" \
    "v_add_f32 v96, v96, v105\n\t" \
    "v_fmac_f32 v100, v105, v105\n\t" \
    "v_mfma_f32_32x32x16_bf16 v[16:31], v[52:55], v[84:87], v[16:31]\n\t" \
    "v_lshlrev_b32 v106, 16, v53\n\t" \
    "v_and_b32 v107, 0xffff0000, v53\n\t" \
    "v_add_f32 v97, v97, v106\n\t" \
    "v_fmac_f32 v101, v106, v106\n\t" \
    "v_add_f32 v97, v97, v107\n\t" \
    "v_fmac_f32 v101, v107, v107\n\t" \
    "v_mfma_f32_32x32x16_bf16 v[0:15], v[72:75], v[56:59], v[0:15]\n\t" \
    "v_lshlrev_b32 v108, 16, v54\n\t" \
    "v_and_b32 v109, 0xffff0000, v54\n\t" \
    "v_add_f32 v98, v98, v108\n\t" \
    "v_fmac_f32 v102, v108, v108\n\t" \
    "v_add_f32 v98, v98, v109\n\t" \
    "v_fmac_f32 v102, v109, v109\n\t" \
    "v_mfma_f32_32x32x16_bf16 v[0:15], v[76:79], v[60:63], v[0:15]\n\t" \
    "v_lshlrev_b32 v110, 16, v55\n\t" \
    "v_and_b32 v111, 0xffff0000, v55\n\t" \
    "v_add_f32 v99, v99, v110\n\t" \
    "v_fmac_f32 v103, v110, v110\n\t" \
    "v_add_f32 v99, v99, v111\n\t" \
    "v_fmac_f32 v103, v111, v111\n\t" \
    "s_nop 15\n\t" \
    "v_mbcnt_lo_u32_b32 v104, -1, 0\n\t" \
    "v_mbcnt_hi_u32_b32 v104, -1, v104\n\t" \
    "v_lshlrev_b32 v105, 4, v104\n\t" \
    "v_lshlrev_b32 v106, 2, v104\n\t" \
    "v_add_u32 v107, s44, v105\n\t" \
    "v_add_u32 v108, s46, v106\n\t" \
    "v_add_f32 v96, v96, v97\n\t" \
    "v_add_f32 v98, v98, v99\n\t" \
    "v_add_f32 v100, v100, v101\n\t" \
    "v_add_f32 v102, v102, v103\n\t" \
    "s_cmp_lg_u32 s40, 0\n\t" \
    "s_cbranch_scc1 L_eg1_%=\n\t" \
    "ds_write_b128 v107, v[24:27]\n\t" \
    "ds_write_b128 v107, v[28:31] offset:1024\n\t" \
    "s_mov_b32 s70, 0x00010001\n\t" \
    "s_branch L_egd_%=\n\t" \
    "L_eg1_%=:\n\t" \
    "ds_write_b128 v107, v[16:19]\n\t" \
    "ds_write_b128 v107, v[20:23] offset:1024\n\t" \
    "s_mov_b32 s70, 0x00000001\n\t" \
    "L_egd_%=:\n\t" \
    "v_add_f32 v96, v96, v98\n\t" \
    "v_add_f32 v100, v100, v102\n\t" \
    "s_lshl_b32 s72, s40, 9\n\t" \
    "s_lshl_b32 s73, s48, 7\n\t" \
    "s_add_u32 s72, s72, s73\n\t" \
    "s_add_u32 s72, s72, s47\n\t" \
    "s_add_u32 s72, s72, 20480\n\t" \
    "v_lshrrev_b32 v109, 5, v104\n\t" \
    "v_lshlrev_b32 v109, 4, v109\n\t" \
    "v_and_b32 v107, 16, v104\n\t" \
    "v_lshlrev_b32 v107, 6, v107\n\t" \
    "v_add3_u32 v109, v109, v107, s72\n\t" \
    "ds_write_b32 v108, v96\n\t" \
    "ds_write_b32 v108, v100 offset:256\n\t" \
    "s_mov_b32 exec_lo, s70\n\t" \
    "s_mov_b32 exec_hi, s70\n\t" \
    "ds_write_b128 v109, v[32:35] offset:0\n\t" \
    "ds_write_b128 v109, v[36:39] offset:32\n\t" \
    "ds_write_b128 v109, v[40:43] offset:64\n\t" \
    "ds_write_b128 v109, v[44:47] offset:96\n\t" \
    "s_mov_b64 exec, -1\n\t" \
    "s_waitcnt lgkmcnt(0)\n\t" \
    "s_barrier\n\t" \
    "v_add_u32 v107, s45, v105\n\t" \
    "ds_read_b128 v[112:115], v107\n\t" \
    "ds_read_b128 v[116:119], v107 offset:1024\n\t" \
    "v_and_b32 v108, 31, v104\n\t" \
    "v_lshlrev_b32 v108, 2, v108\n\t" \
    "v_lshrrev_b32 v109, 5, v104\n\t" \
    "v_lshlrev_b32 v109, 9, v109\n\t" \
    "v_add3_u32 v107, v108, v109, s47\n\t" \
    "v_add_u32 v107, 0x4000, v107\n\t" \
    "v_add_u32 v109, 128, v107\n\t" \
    "ds_read2st64_b32 v[60:61], v107 offset0:0 offset1:1\n\t" \
    "ds_read2st64_b32 v[62:63], v109 offset0:0 offset1:1\n\t" \
    "ds_read2st64_b32 v[64:65], v107 offset0:8 offset1:9\n\t" \
    "ds_read2st64_b32 v[66:67], v109 offset0:8 offset1:9\n\t" \
    "ds_read2st64_b32 v[68:69], v107 offset0:4 offset1:5\n\t" \
    "ds_read2st64_b32 v[70:71], v109 offset0:4 offset1:5\n\t" \
    "ds_read2st64_b32 v[72:73], v107 offset0:12 offset1:13\n\t" \
    "ds_read2st64_b32 v[74:75], v109 offset0:12 offset1:13\n\t" \
    "v_add_u32 v108, s47, v108\n\t" \
    "v_add_u32 v108, 20480, v108\n\t" \
    "v_add_u32 v110, 128, v108\n\t" \
    "s_lshl_b32 s72, s40, 9\n\t" \
    "v_add_u32 v111, s72, v108\n\t" \
    "v_add_u32 v120, s72, v110\n\t" \
    "ds_read2st64_b32 v[122:123], v111 offset0:0 offset1:1\n\t" \
    "ds_read2st64_b32 v[124:125], v120 offset0:0 offset1:1\n\t" \
    "ds_read2st64_b32 v[126:127], v108 offset0:4 offset1:5\n\t" \
    "ds_read2st64_b32 v[128:129], v110 offset0:4 offset1:5\n\t" \
    "s_waitcnt lgkmcnt(8)\n\t" \
    "v_add_f32 v60, v60, v62\n\t" \
    "v_add_f32 v64, v64, v66\n\t" \
    "v_add_f32 v60, v60, v64\n\t" \
    "v_add_f32 v61, v61, v63\n\t" \
    "v_add_f32 v65, v65, v67\n\t" \
    "v_add_f32 v61, v61, v65\n\t" \
    "v_mul_f32 v92, 0x3b000000, v60\n\t" \
    "v_mul_f32 v62, v92, v92\n\t" \
    "v_fma_f32 v61, v61, s54, -v62\n\t" \
    "v_max_f32 v61, 0, v61\n\t" \
    "v_add_f32 v88, 0x3727c5ac, v61\n\t" \
    "v_rsq_f32 v76, v88\n\t" \
    "v_mov_b32 v93, v92\n\t" \
    "v_mov_b32 v89, v88\n\t" \
    "v_mov_b32 v77, v76\n\t" \
    "s_nop 1\n\t" \
    "v_permlane32_swap_b32 v92, v93\n\t" \
    "v_permlane32_swap_b32 v88, v89\n\t" \
    "v_permlane32_swap_b32 v76, v77\n\t" \
    "s_waitcnt lgkmcnt(4)\n\t" \
    "v_add_f32 v68, v68, v70\n\t" \
    "v_add_f32 v72, v72, v74\n\t" \
    "v_add_f32 v68, v68, v72\n\t" \
    "v_add_f32 v69, v69, v71\n\t" \
    "v_add_f32 v73, v73, v75\n\t" \
    "v_add_f32 v69, v69, v73\n\t" \
    "v_mul_f32 v94, 0x3b000000, v68\n\t" \
    "v_mul_f32 v70, v94, v94\n\t" \
    "v_fma_f32 v69, v69, s54, -v70\n\t" \
    "v_max_f32 v69, 0, v69\n\t" \
    "v_add_f32 v90, 0x3727c5ac, v69\n\t" \
    "v_rsq_f32 v78, v90\n\t" \
    "v_mov_b32 v95, v94\n\t" \
    "v_mov_b32 v91, v90\n\t" \
    "v_mov_b32 v79, v78\n\t" \
    "s_nop 1\n\t" \
    "v_permlane32_swap_b32 v94, v95\n\t" \
    "v_permlane32_swap_b32 v90, v91\n\t" \
    "v_permlane32_swap_b32 v78, v79\n\t" \
    "s_waitcnt lgkmcnt(0)\n\t" \
    "v_add_f32 v122, v122, v124\n\t" \
    "v_add_f32 v123, v123, v125\n\t" \
    "v_add_f32 v126, v126, v128\n\t" \
    "v_add_f32 v127, v127, v129\n\t" \
    "v_add_f32 v122, v122, v123\n\t" \
    "v_add_f32 v126, v126, v127\n\t" \
    "v_cvt_pk_bf16_f32 v48, v122, v122\n\t" \
    "v_lshlrev_b32 v49, 16, v48\n\t" \
    "v_sub_f32 v49, v122, v49\n\t" \
    "v_cvt_pk_bf16_f32 v49, v49, 0\n\t" \
    "v_mov_b32 v50, 0\n\t" \
    "v_mov_b32 v51, 0\n\t" \
    "v_cvt_pk_bf16_f32 v52, v126, v126\n\t" \
    "v_lshlrev_b32 v53, 16, v52\n\t" \
    "v_sub_f32 v53, v126, v53\n\t" \
    "v_cvt_pk_bf16_f32 v53, v53, 0\n\t" \
    "v_mov_b32 v54, 0\n\t" \
    "v_mov_b32 v55, 0\n\t" \
    "s_set_gpr_idx_on s48, gpr_idx(SRC0)\n\t" \
    "v_mov_b32 v120, v92\n\t" \
    "v_mov_b32 v121, v76\n\t" \
    "s_set_gpr_idx_off\n\t" \
    "v_cvt_pk_bf16_f32 v57, -v120, 0\n\t" \
    "v_lshlrev_b32 v58, 16, v57\n\t" \
    "v_sub_f32 v58, -v120, v58\n\t" \
    "v_cvt_pk_bf16_f32 v56, -v120, v58\n\t" \
    "v_mov_b32 v58, 0\n\t" \
    "v_mov_b32 v59, 0\n\t" \
    "s_mov_b32 exec_lo, 0\n\t" \
    "v_mov_b32 v48, 0\n\t" \
    "v_mov_b32 v49, 0\n\t" \
    "v_mov_b32 v52, 0\n\t" \
    "v_mov_b32 v53, 0\n\t" \
    "v_mov_b32 v56, 0\n\t" \
    "v_mov_b32 v57, 0\n\t" \
    "s_mov_b32 exec_lo, -1\n\t" \
    "s_nop 1\n\t" \
    "v_mfma_f32_32x32x16_bf16 v[0:15], v[48:51], v[56:59], v[0:15]\n\t" \
    "v_mfma_f32_32x32x16_bf16 v[16:31], v[56:59], v[52:55], v[16:31]\n\t" \
    "v_mul_f32 v84, 0xc0135761, v76\n\t" \
    "v_mul_f32 v80, v76, v76\n\t" \
    "v_mul_f32 v80, v80, v76\n\t" \
    "v_mul_f32 v80, 0xbdd2d3e8, v80\n\t" \
    "v_mul_f32 v85, 0xc0135761, v77\n\t" \
    "v_mul_f32 v81, v77, v77\n\t" \
    "v_mul_f32 v81, v81, v77\n\t" \
    "v_mul_f32 v81, 0xbdd2d3e8, v81\n\t" \
    "v_mul_f32 v86, 0xc0135761, v78\n\t" \
    "v_mul_f32 v82, v78, v78\n\t" \
    "v_mul_f32 v82, v82, v78\n\t" \
    "v_mul_f32 v82, 0xbdd2d3e8, v82\n\t" \
    "v_mul_f32 v87, 0xc0135761, v79\n\t" \
    "v_mul_f32 v83, v79, v79\n\t" \
    "v_mul_f32 v83, v83, v79\n\t" \
    "v_mul_f32 v83, 0xbdd2d3e8, v83\n\t" \
    "v_and_b32 v108, 31, v104\n\t" \
    "v_lshlrev_b32 v108, 2, v108\n\t" \
    "s_lshl_b32 s72, s55, 7\n\t" \
    "s_add_u32 s72, s72, s47\n\t" \
    "s_add_u32 s72, s72, 22016\n\t" \
    "v_add_u32 v108, s72, v108\n\t" \
    "ds_write_b32 v108, v121\n\t" \
    "v_lshrrev_b32 v109, 5, v104\n\t" \
    "v_lshlrev_b32 v109, 4, v109\n\t" \
    "v_add_u32 v109, s72, v109\n\t" \
    "s_waitcnt lgkmcnt(0)\n\t" \
    "ds_read_b128 v[60:63], v109 offset:0\n\t" \
    "ds_read_b128 v[64:67], v109 offset:32\n\t" \
    "ds_read_b128 v[68:71], v109 offset:64\n\t" \
    "ds_read_b128 v[72:75], v109 offset:96\n\t" \
    "v_and_b32 v110, 15, v104\n\t" \
    "v_lshlrev_b32 v110, 4, v110\n\t" \
    "v_lshrrev_b32 v111, 5, v104\n\t" \
    "v_lshl_add_u32 v110, v111, 8, v110\n\t" \
    "v_and_b32 v111, 16, v104\n\t" \
    "v_lshl_add_u32 v110, v111, 12, v110\n\t" \
    "v_mul_f32 v122, s52, v121\n\t" \
    "v_mul_f32 v123, s53, v121\n\t" \
    "s_nop 7\n\t" \
    "s_waitcnt lgkmcnt(0)\n\t" \
    "v_mul_f32 v0, v0, v122\n\t" \
    "v_mul_f32 v1, v1, v122\n\t" \
    "v_mul_f32 v2, v2, v122\n\t" \
    "v_mul_f32 v3, v3, v122\n\t" \
    "v_mul_f32 v4, v4, v122\n\t" \
    "v_mul_f32 v5, v5, v122\n\t" \
    "v_mul_f32 v6, v6, v122\n\t" \
    "v_mul_f32 v7, v7, v122\n\t" \
    "v_cvt_pk_bf16_f32 v48, v0, v1\n\t" \
    "v_cvt_pk_bf16_f32 v49, v2, v3\n\t" \
    "v_cvt_pk_bf16_f32 v50, v4, v5\n\t" \
    "v_cvt_pk_bf16_f32 v51, v6, v7\n\t" \
    "buffer_store_dwordx4 v[48:51], v105, s[60:63], s49 offen sc1\n\t" \
    "v_mul_f32 v8, v8, v122\n\t" \
    "v_mul_f32 v9, v9, v122\n\t" \
    "v_mul_f32 v10, v10, v122\n\t" \
    "v_mul_f32 v11, v11, v122\n\t" \
    "v_mul_f32 v12, v12, v122\n\t" \
    "v_mul_f32 v13, v13, v122\n\t" \
    "v_mul_f32 v14, v14, v122\n\t" \
    "v_mul_f32 v15, v15, v122\n\t" \
    "v_cvt_pk_bf16_f32 v52, v8, v9\n\t" \
    "v_cvt_pk_bf16_f32 v53, v10, v11\n\t" \
    "v_cvt_pk_bf16_f32 v54, v12, v13\n\t" \
    "v_cvt_pk_bf16_f32 v55, v14, v15\n\t" \
    "buffer_store_dwordx4 v[52:55], v105, s[60:63], s50 offen sc1\n\t" \
    "s_cmp_lg_u32 s40, 0\n\t" \
    "s_cbranch_scc1 L_eo1_%=\n\t" \
    "v_add_f32 v16, v16, v112\n\t" \
    "v_add_f32 v17, v17, v113\n\t" \
    "v_add_f32 v18, v18, v114\n\t" \
    "v_add_f32 v19, v19, v115\n\t" \
    "v_add_f32 v20, v20, v116\n\t" \
    "v_add_f32 v21, v21, v117\n\t" \
    "v_add_f32 v22, v22, v118\n\t" \
    "v_add_f32 v23, v23, v119\n\t" \
    "v_mul_f32 v16, v16, v60\n\t" \
    "v_mul_f32 v17, v17, v61\n\t" \
    "v_mul_f32 v18, v18, v62\n\t" \
    "v_mul_f32 v19, v19, v63\n\t" \
    "v_mul_f32 v20, v20, v64\n\t" \
    "v_mul_f32 v21, v21, v65\n\t" \
    "v_mul_f32 v22, v22, v66\n\t" \
    "v_mul_f32 v23, v23, v67\n\t" \
    "v_cvt_pk_bf16_f32 v56, v16, v17\n\t" \
    "v_cvt_pk_bf16_f32 v57, v18, v19\n\t" \
    "v_cvt_pk_bf16_f32 v58, v20, v21\n\t" \
    "v_cvt_pk_bf16_f32 v59, v22, v23\n\t" \
    "buffer_store_dwordx4 v[56:59], v110, s[60:63], s51 offen sc1\n\t" \
    "s_branch L_eod_%=\n\t" \
    "L_eo1_%=:\n\t" \
    "v_add_f32 v24, v24, v112\n\t" \
    "v_add_f32 v25, v25, v113\n\t" \
    "v_add_f32 v26, v26, v114\n\t" \
    "v_add_f32 v27, v27, v115\n\t" \
    "v_add_f32 v28, v28, v116\n\t" \
    "v_add_f32 v29, v29, v117\n\t" \
    "v_add_f32 v30, v30, v118\n\t" \
    "v_add_f32 v31, v31, v119\n\t" \
    "v_mul_f32 v24, v24, v68\n\t" \
    "v_mul_f32 v25, v25, v69\n\t" \
    "v_mul_f32 v26, v26, v70\n\t" \
    "v_mul_f32 v27, v27, v71\n\t" \
    "v_mul_f32 v28, v28, v72\n\t" \
    "v_mul_f32 v29, v29, v73\n\t" \
    "v_mul_f32 v30, v30, v74\n\t" \
    "v_mul_f32 v31, v31, v75\n\t" \
    "v_cvt_pk_bf16_f32 v56, v24, v25\n\t" \
    "v_cvt_pk_bf16_f32 v57, v26, v27\n\t" \
    "v_cvt_pk_bf16_f32 v58, v28, v29\n\t" \
    "v_cvt_pk_bf16_f32 v59, v30, v31\n\t" \
    "buffer_store_dwordx4 v[56:59], v110, s[60:63], s51 offen sc1\n\t" \
    "L_eod_%=:\n\t" \
    ""
#define K1_ASM_7 \
    "s_barrier\n\t" \
    "ds_read_b128 v[48:51], v112 offset:0\n\t" \
    "ds_read_b64_tr_b16 v[64:65], v116 offset:0\n\t" \
    "ds_read_b64_tr_b16 v[66:67], v116 offset:512\n\t" \
    "ds_read_b64_tr_b16 v[80:81], v120 offset:0\n\t" \
    "ds_read_b64_tr_b16 v[82:83], v120 offset:256\n\t" \
    "ds_read_b128 v[52:55], v113 offset:0\n\t" \
    "ds_read_b64_tr_b16 v[68:69], v117 offset:0\n\t" \
    "ds_read_b64_tr_b16 v[70:71], v117 offset:512\n\t" \
    "ds_read_b64_tr_b16 v[84:85], v121 offset:0\n\t" \
    "ds_read_b64_tr_b16 v[86:87], v121 offset:256\n\t" \
    "ds_read_b128 v[56:59], v114 offset:0\n\t" \
    "ds_read_b64_tr_b16 v[72:73], v118 offset:0\n\t" \
    "ds_read_b64_tr_b16 v[74:75], v118 offset:512\n\t" \
    "ds_read_b128 v[60:63], v115 offset:0\n\t" \
    "ds_read_b64_tr_b16 v[76:77], v119 offset:0\n\t" \
    "ds_read_b64_tr_b16 v[78:79], v119 offset:512\n\t" \
    "s_barrier\n\t" \
    "s_waitcnt lgkmcnt(13)\n\t" \
    "v_mfma_f32_32x32x16_bf16 v[0:15], v[48:51], v[64:67], v[0:15]\n\t" \
    "v_lshlrev_b32 v104, 16, v48\n\t" \
    "v_and_b32 v105, 0xffff0000, v48\n\t" \
    "v_add_f32 v96, v96, v104\n\t" \
    "v_fmac_f32 v100, v104, v104\n\t" \
    "v_add_f32 v96, v96, v105\n\t" \
    "v_fmac_f32 v100, v105, v105\n\t" \
    "v_mfma_f32_32x32x16_bf16 v[32:47], v[64:67], v[88:91], v[32:47]\n\t" \
    "ds_read_b64_tr_b16 v[64:65], v116 offset:28672\n\t" \
    "ds_read_b64_tr_b16 v[66:67], v116 offset:29184\n\t" \
    "v_lshlrev_b32 v106, 16, v49\n\t" \
    "v_and_b32 v107, 0xffff0000, v49\n\t" \
    "v_add_f32 v97, v97, v106\n\t" \
    "v_fmac_f32 v101, v106, v106\n\t" \
    "v_add_f32 v97, v97, v107\n\t" \
    "v_fmac_f32 v101, v107, v107\n\t" \
    "s_waitcnt lgkmcnt(13)\n\t" \
    "v_mfma_f32_32x32x16_bf16 v[16:31], v[48:51], v[80:83], v[16:31]\n\t" \
    "v_lshlrev_b32 v108, 16, v50\n\t" \
    "v_and_b32 v109, 0xffff0000, v50\n\t" \
    "v_add_f32 v98, v98, v108\n\t" \
    "v_fmac_f32 v102, v108, v108\n\t" \
    "v_add_f32 v98, v98, v109\n\t" \
    "v_fmac_f32 v102, v109, v109\n\t" \
    "s_cmp_lg_u32 s40, 0\n\t" \
    "s_cbranch_scc1 L_nocs0_%=\n\t" \
    "v_mfma_f32_32x32x16_bf16 v[32:47], v[80:83], v[92:95], v[32:47]\n\t" \
    "L_nocs0_%=:\n\t" \
    "ds_read_b64_tr_b16 v[80:81], v120 offset:28672\n\t" \
    "ds_read_b64_tr_b16 v[82:83], v120 offset:28928\n\t" \
    "v_lshlrev_b32 v110, 16, v51\n\t" \
    "v_and_b32 v111, 0xffff0000, v51\n\t" \
    "v_add_f32 v99, v99, v110\n\t" \
    "v_fmac_f32 v103, v110, v110\n\t" \
    "v_add_f32 v99, v99, v111\n\t" \
    "v_fmac_f32 v103, v111, v111\n\t" \
    "ds_read_b128 v[48:51], v112 offset:28672\n\t" \
    "s_waitcnt lgkmcnt(13)\n\t" \
    "v_mfma_f32_32x32x16_bf16 v[0:15], v[52:55], v[68:71], v[0:15]\n\t" \
    "ds_read_b64_tr_b16 v[68:69], v117 offset:28672\n\t" \
    "ds_read_b64_tr_b16 v[70:71], v117 offset:29184\n\t" \
    "v_lshlrev_b32 v104, 16, v52\n\t" \
    "v_and_b32 v105, 0xffff0000, v52\n\t" \
    "v_add_f32 v96, v96, v104\n\t" \
    "v_fmac_f32 v100, v104, v104\n\t" \
    "v_add_f32 v96, v96, v105\n\t" \
    "v_fmac_f32 v100, v105, v105\n\t" \
    "s_waitcnt lgkmcnt(13)\n\t" \
    "v_mfma_f32_32x32x16_bf16 v[16:31], v[52:55], v[84:87], v[16:31]\n\t" \
    "ds_read_b64_tr_b16 v[84:85], v121 offset:28672\n\t" \
    "ds_read_b64_tr_b16 v[86:87], v121 offset:28928\n\t" \
    "v_lshlrev_b32 v106, 16, v53\n\t" \
    "v_and_b32 v107, 0xffff0000, v53\n\t" \
    "v_add_f32 v97, v97, v106\n\t" \
    "v_fmac_f32 v101, v106, v106\n\t" \
    "v_add_f32 v97, v97, v107\n\t" \
    "v_fmac_f32 v101, v107, v107\n\t" \
    "s_waitcnt lgkmcnt(12)\n\t" \
    "v_mfma_f32_32x32x16_bf16 v[0:15], v[56:59], v[72:75], v[0:15]\n\t" \
    "ds_read_b64_tr_b16 v[72:73], v118 offset:28672\n\t" \
    "ds_read_b64_tr_b16 v[74:75], v118 offset:29184\n\t" \
    "ds_read_b128 v[56:59], v114 offset:28672\n\t" \
    "v_lshlrev_b32 v108, 16, v54\n\t" \
    "v_and_b32 v109, 0xffff0000, v54\n\t" \
    "v_add_f32 v98, v98, v108\n\t" \
    "v_fmac_f32 v102, v108, v108\n\t" \
    "v_add_f32 v98, v98, v109\n\t" \
    "v_fmac_f32 v102, v109, v109\n\t" \
    "s_waitcnt lgkmcnt(12)\n\t" \
    "v_mfma_f32_32x32x16_bf16 v[0:15], v[60:63], v[76:79], v[0:15]\n\t" \
    "ds_read_b64_tr_b16 v[76:77], v119 offset:28672\n\t" \
    "ds_read_b64_tr_b16 v[78:79], v119 offset:29184\n\t" \
    "ds_read_b128 v[60:63], v115 offset:28672\n\t" \
    "v_lshlrev_b32 v110, 16, v55\n\t" \
    "v_and_b32 v111, 0xffff0000, v55\n\t" \
    "v_add_f32 v99, v99, v110\n\t" \
    "v_fmac_f32 v103, v110, v110\n\t" \
    "v_add_f32 v99, v99, v111\n\t" \
    "v_fmac_f32 v103, v111, v111\n\t" \
    "ds_read_b128 v[52:55], v113 offset:28672\n\t" \
    "s_barrier\n\t" \
    "v_add_u32 v112, 0xe000, v112\n\t" \
    "v_add_u32 v113, 0xe000, v113\n\t" \
    "v_add_u32 v114, 0xe000, v114\n\t" \
    "v_add_u32 v115, 0xe000, v115\n\t" \
    "v_add_u32 v116, 0xe000, v116\n\t" \
    "v_add_u32 v117, 0xe000, v117\n\t" \
    "v_add_u32 v118, 0xe000, v118\n\t" \
    "v_add_u32 v119, 0xe000, v119\n\t" \
    "v_add_u32 v120, 0xe000, v120\n\t" \
    "v_add_u32 v121, 0xe000, v121\n\t" \
    "s_waitcnt lgkmcnt(11)\n\t" \
    "v_mfma_f32_32x32x16_bf16 v[0:15], v[48:51], v[64:67], v[0:15]\n\t" \
    "v_lshlrev_b32 v104, 16, v48\n\t" \
    "v_and_b32 v105, 0xffff0000, v48\n\t" \
    "v_add_f32 v96, v96, v104\n\t" \
    "v_fmac_f32 v100, v104, v104\n\t" \
    "v_add_f32 v96, v96, v105\n\t" \
    "v_fmac_f32 v100, v105, v105\n\t" \
    "v_mfma_f32_32x32x16_bf16 v[32:47], v[64:67], v[88:91], v[32:47]\n\t" \
    "ds_read_b64_tr_b16 v[64:65], v116 offset:0\n\t" \
    "ds_read_b64_tr_b16 v[66:67], v116 offset:512\n\t" \
    "v_lshlrev_b32 v106, 16, v49\n\t" \
    "v_and_b32 v107, 0xffff0000, v49\n\t" \
    "v_add_f32 v97, v97, v106\n\t" \
    "v_fmac_f32 v101, v106, v106\n\t" \
    "v_add_f32 v97, v97, v107\n\t" \
    "v_fmac_f32 v101, v107, v107\n\t" \
    "v_mfma_f32_32x32x16_bf16 v[16:31], v[48:51], v[80:83], v[16:31]\n\t" \
    "v_lshlrev_b32 v108, 16, v50\n\t" \
    "v_and_b32 v109, 0xffff0000, v50\n\t" \
    "v_add_f32 v98, v98, v108\n\t" \
    "v_fmac_f32 v102, v108, v108\n\t" \
    "v_add_f32 v98, v98, v109\n\t" \
    "v_fmac_f32 v102, v109, v109\n\t" \
    "s_cmp_lg_u32 s40, 0\n\t" \
    "s_cbranch_scc1 L_nocs1_%=\n\t" \
    "v_mfma_f32_32x32x16_bf16 v[32:47], v[80:83], v[92:95], v[32:47]\n\t" \
    "L_nocs1_%=:\n\t" \
    "ds_read_b64_tr_b16 v[80:81], v120 offset:0\n\t" \
    "ds_read_b64_tr_b16 v[82:83], v120 offset:256\n\t" \
    "v_lshlrev_b32 v110, 16, v51\n\t" \
    "v_and_b32 v111, 0xffff0000, v51\n\t" \
    "v_add_f32 v99, v99, v110\n\t" \
    "v_fmac_f32 v103, v110, v110\n\t" \
    "v_add_f32 v99, v99, v111\n\t" \
    "v_fmac_f32 v103, v111, v111\n\t" \
    "ds_read_b128 v[48:51], v112 offset:0\n\t" \
    "s_waitcnt lgkmcnt(5)\n\t" \
    "v_mfma_f32_32x32x16_bf16 v[0:15], v[52:55], v[68:71], v[0:15]\n\t" \
    "ds_read_b64_tr_b16 v[68:69], v117 offset:0\n\t" \
    "ds_read_b64_tr_b16 v[70:71], v117 offset:512\n\t" \
    "v_lshlrev_b32 v104, 16, v52\n\t" \
    "v_and_b32 v105, 0xffff0000, v52\n\t" \
    "v_add_f32 v96, v96, v104\n\t" \
    "v_fmac_f32 v100, v104, v104\n\t" \
    "v_add_f32 v96, v96, v105\n\t" \
    "v_fmac_f32 v100, v105, v105\n\t" \
    "v_mfma_f32_32x32x16_bf16 v[16:31], v[52:55], v[84:87], v[16:31]\n\t" \
    "ds_read_b64_tr_b16 v[84:85], v121 offset:0\n\t" \
    "ds_read_b64_tr_b16 v[86:87], v121 offset:256\n\t" \
    "v_lshlrev_b32 v106, 16, v53\n\t" \
    "v_and_b32 v107, 0xffff0000, v53\n\t" \
    "v_add_f32 v97, v97, v106\n\t" \
    "v_fmac_f32 v101, v106, v106\n\t" \
    "v_add_f32 v97, v97, v107\n\t" \
    "v_fmac_f32 v101, v107, v107\n\t" \
    "v_mfma_f32_32x32x16_bf16 v[0:15], v[56:59], v[72:75], v[0:15]\n\t" \
    "ds_read_b64_tr_b16 v[72:73], v118 offset:0\n\t" \
    "ds_read_b64_tr_b16 v[74:75], v118 offset:512\n\t" \
    "ds_read_b128 v[56:59], v114 offset:0\n\t" \
    "v_lshlrev_b32 v108, 16, v54\n\t" \
    "v_and_b32 v109, 0xffff0000, v54\n\t" \
    "v_add_f32 v98, v98, v108\n\t" \
    "v_fmac_f32 v102, v108, v108\n\t" \
    "v_add_f32 v98, v98, v109\n\t" \
    "v_fmac_f32 v102, v109, v109\n\t" \
    "v_mfma_f32_32x32x16_bf16 v[0:15], v[60:63], v[76:79], v[0:15]\n\t" \
    "ds_read_b64_tr_b16 v[76:77], v119 offset:0\n\t" \
    "ds_read_b64_tr_b16 v[78:79], v119 offset:512\n\t" \
    "ds_read_b128 v[60:63], v115 offset:0\n\t" \
    "v_lshlrev_b32 v110, 16, v55\n\t" \
    "v_and_b32 v111, 0xffff0000, v55\n\t" \
    "v_add_f32 v99, v99, v110\n\t" \
    "v_fmac_f32 v103, v110, v110\n\t" \
    "v_add_f32 v99, v99, v111\n\t" \
    "v_fmac_f32 v103, v111, v111\n\t" \
    "ds_read_b128 v[52:55], v113 offset:0\n\t" \
    "s_barrier\n\t" \
    "s_cmp_eq_u32 s42, 0\n\t" \
    "s_cbranch_scc1 L_nf1_%=\n\t" \
    "buffer_load_dwordx2 v[132:133], off, s[60:63], s43 sc1\n\t" \
    "L_nf1_%=:\n\t" \
    "s_waitcnt lgkmcnt(11)\n\t" \
    "v_mfma_f32_32x32x16_bf16 v[0:15], v[48:51], v[64:67], v[0:15]\n\t" \
    "v_lshlrev_b32 v104, 16, v48\n\t" \
    "v_and_b32 v105, 0xffff0000, v48\n\t" \
    "v_add_f32 v96, v96, v104\n\t" \
    "v_fmac_f32 v100, v104, v104\n\t" \
    "v_add_f32 v96, v96, v105\n\t" \
    "v_fmac_f32 v100, v105, v105\n\t" \
    "v_mfma_f32_32x32x16_bf16 v[32:47], v[64:67], v[88:91], v[32:47]\n\t" \
    "ds_read_b64_tr_b16 v[64:65], v116 offset:28672\n\t" \
    "ds_read_b64_tr_b16 v[66:67], v116 offset:29184\n\t" \
    "v_lshlrev_b32 v106, 16, v49\n\t" \
    "v_and_b32 v107, 0xffff0000, v49\n\t" \
    "v_add_f32 v97, v97, v106\n\t" \
    "v_fmac_f32 v101, v106, v106\n\t" \
    "v_add_f32 v97, v97, v107\n\t" \
    "v_fmac_f32 v101, v107, v107\n\t" \
    "v_mfma_f32_32x32x16_bf16 v[16:31], v[48:51], v[80:83], v[16:31]\n\t" \
    "v_lshlrev_b32 v108, 16, v50\n\t" \
    "v_and_b32 v109, 0xffff0000, v50\n\t" \
    "v_add_f32 v98, v98, v108\n\t" \
    "v_fmac_f32 v102, v108, v108\n\t" \
    "v_add_f32 v98, v98, v109\n\t" \
    "v_fmac_f32 v102, v109, v109\n\t" \
    "s_cmp_lg_u32 s40, 0\n\t" \
    "s_cbranch_scc1 L_nocs2_%=\n\t" \
    "v_mfma_f32_32x32x16_bf16 v[32:47], v[80:83], v[92:95], v[32:47]\n\t" \
    "L_nocs2_%=:\n\t" \
    "ds_read_b64_tr_b16 v[80:81], v120 offset:28672\n\t" \
    "ds_read_b64_tr_b16 v[82:83], v120 offset:28928\n\t" \
    "v_lshlrev_b32 v110, 16, v51\n\t" \
    "v_and_b32 v111, 0xffff0000, v51\n\t" \
    "v_add_f32 v99, v99, v110\n\t" \
    "v_fmac_f32 v103, v110, v110\n\t" \
    "v_add_f32 v99, v99, v111\n\t" \
    "v_fmac_f32 v103, v111, v111\n\t" \
    "ds_read_b128 v[48:51], v112 offset:28672\n\t" \
    "s_waitcnt lgkmcnt(5)\n\t" \
    "v_mfma_f32_32x32x16_bf16 v[0:15], v[52:55], v[68:71], v[0:15]\n\t" \
    "ds_read_b64_tr_b16 v[68:69], v117 offset:28672\n\t" \
    "ds_read_b64_tr_b16 v[70:71], v117 offset:29184\n\t" \
    "v_lshlrev_b32 v104, 16, v52\n\t" \
    "v_and_b32 v105, 0xffff0000, v52\n\t" \
    "v_add_f32 v96, v96, v104\n\t" \
    "v_fmac_f32 v100, v104, v104\n\t" \
    "v_add_f32 v96, v96, v105\n\t" \
    "v_fmac_f32 v100, v105, v105\n\t" \
    "v_mfma_f32_32x32x16_bf16 v[16:31], v[52:55], v[84:87], v[16:31]\n\t" \
    "ds_read_b64_tr_b16 v[84:85], v121 offset:28672\n\t" \
    "ds_read_b64_tr_b16 v[86:87], v121 offset:28928\n\t" \
    "v_lshlrev_b32 v106, 16, v53\n\t" \
    "v_and_b32 v107, 0xffff0000, v53\n\t" \
    "v_add_f32 v97, v97, v106\n\t" \
    "v_fmac_f32 v101, v106, v106\n\t" \
    "v_add_f32 v97, v97, v107\n\t" \
    "v_fmac_f32 v101, v107, v107\n\t" \
    "v_mfma_f32_32x32x16_bf16 v[0:15], v[56:59], v[72:75], v[0:15]\n\t" \
    "ds_read_b64_tr_b16 v[72:73], v118 offset:28672\n\t" \
    "ds_read_b64_tr_b16 v[74:75], v118 offset:29184\n\t" \
    "ds_read_b128 v[56:59], v114 offset:28672\n\t" \
    "v_lshlrev_b32 v108, 16, v54\n\t" \
    "v_and_b32 v109, 0xffff0000, v54\n\t" \
    "v_add_f32 v98, v98, v108\n\t" \
    "v_fmac_f32 v102, v108, v108\n\t" \
    "v_add_f32 v98, v98, v109\n\t" \
    "v_fmac_f32 v102, v109, v109\n\t" \
    "v_mfma_f32_32x32x16_bf16 v[0:15], v[60:63], v[76:79], v[0:15]\n\t" \
    "ds_read_b64_tr_b16 v[76:77], v119 offset:28672\n\t" \
    "ds_read_b64_tr_b16 v[78:79], v119 offset:29184\n\t" \
    "ds_read_b128 v[60:63], v115 offset:28672\n\t" \
    "v_lshlrev_b32 v110, 16, v55\n\t" \
    "v_and_b32 v111, 0xffff0000, v55\n\t" \
    "v_add_f32 v99, v99, v110\n\t" \
    "v_fmac_f32 v103, v110, v110\n\t" \
    "v_add_f32 v99, v99, v111\n\t" \
    "v_fmac_f32 v103, v111, v111\n\t" \
    "ds_read_b128 v[52:55], v113 offset:28672\n\t" \
    "s_barrier\n\t" \
    "v_add_u32 v112, 0xffff2000, v112\n\t" \
    "v_add_u32 v113, 0xffff2000, v113\n\t" \
    "v_add_u32 v114, 0xffff2000, v114\n\t" \
    "v_add_u32 v115, 0xffff2000, v115\n\t" \
    "v_add_u32 v116, 0xffff2000, v116\n\t" \
    "v_add_u32 v117, 0xffff2000, v117\n\t" \
    "v_add_u32 v118, 0xffff2000, v118\n\t" \
    "v_add_u32 v119, 0xffff2000, v119\n\t" \
    "v_add_u32 v120, 0xffff2000, v120\n\t" \
    "v_add_u32 v121, 0xffff2000, v121\n\t" \
    "s_cmp_eq_u32 s42, 0\n\t" \
    "s_cbranch_scc1 L_nf2_%=\n\t" \
    "s_mov_b32 s70, 0\n\t" \
    "L_fp_%=:\n\t" \
    "s_waitcnt vmcnt(0)\n\t" \
    "v_readfirstlane_b32 s72, v132\n\t" \
    "v_readfirstlane_b32 s73, v133\n\t" \
    "s_add_u32 s70, s70, 1\n\t" \
    "s_cmp_ge_u32 s70, 0x400000\n\t" \
    "s_cbranch_scc1 L_nf2_%=\n\t" \
    "s_xor_b32 s72, s72, 0x7f4a7c15\n\t" \
    "s_xor_b32 s73, s73, 0x9e3779b9\n\t" \
    "s_or_b32 s72, s72, s73\n\t" \
    "s_cmp_eq_u32 s72, 0\n\t" \
    "s_cbranch_scc1 L_nf2_%=\n\t" \
    "buffer_load_dwordx2 v[132:133], off, s[60:63], s43 sc1\n\t" \
    "s_branch L_fp_%=\n\t" \
    "L_nf2_%=:\n\t" \
    "s_waitcnt lgkmcnt(11)\n\t" \
    "v_mfma_f32_32x32x16_bf16 v[0:15], v[48:51], v[64:67], v[0:15]\n\t" \
    "v_lshlrev_b32 v104, 16, v48\n\t" \
    "v_and_b32 v105, 0xffff0000, v48\n\t" \
    "v_add_f32 v96, v96, v104\n\t" \
    "v_fmac_f32 v100, v104, v104\n\t" \
    "v_add_f32 v96, v96, v105\n\t" \
    "v_fmac_f32 v100, v105, v105\n\t" \
    "v_mfma_f32_32x32x16_bf16 v[32:47], v[64:67], v[88:91], v[32:47]\n\t" \
    "ds_read_b64_tr_b16 v[64:65], v116 offset:0\n\t" \
    "ds_read_b64_tr_b16 v[66:67], v116 offset:512\n\t" \
    "v_lshlrev_b32 v106, 16, v49\n\t" \
    "v_and_b32 v107, 0xffff0000, v49\n\t" \
    "v_add_f32 v97, v97, v106\n\t" \
    "v_fmac_f32 v101, v106, v106\n\t" \
    "v_add_f32 v97, v97, v107\n\t" \
    "v_fmac_f32 v101, v107, v107\n\t" \
    "v_mfma_f32_32x32x16_bf16 v[16:31], v[48:51], v[80:83], v[16:31]\n\t" \
    "v_lshlrev_b32 v108, 16, v50\n\t" \
    "v_and_b32 v109, 0xffff0000, v50\n\t" \
    "v_add_f32 v98, v98, v108\n\t" \
    "v_fmac_f32 v102, v108, v108\n\t" \
    "v_add_f32 v98, v98, v109\n\t" \
    "v_fmac_f32 v102, v109, v109\n\t" \
    "s_cmp_lg_u32 s40, 0\n\t" \
    "s_cbranch_scc1 L_nocs3_%=\n\t" \
    "v_mfma_f32_32x32x16_bf16 v[32:47], v[80:83], v[92:95], v[32:47]\n\t" \
    "L_nocs3_%=:\n\t" \
    "ds_read_b64_tr_b16 v[80:81], v120 offset:0\n\t" \
    "ds_read_b64_tr_b16 v[82:83], v120 offset:256\n\t" \
    "v_lshlrev_b32 v110, 16, v51\n\t" \
    "v_and_b32 v111, 0xffff0000, v51\n\t" \
    "v_add_f32 v99, v99, v110\n\t" \
    "v_fmac_f32 v103, v110, v110\n\t" \
    "v_add_f32 v99, v99, v111\n\t" \
    "v_fmac_f32 v103, v111, v111\n\t" \
    "ds_read_b128 v[48:51], v112 offset:0\n\t" \
    "s_waitcnt lgkmcnt(5)\n\t" \
    "v_mfma_f32_32x32x16_bf16 v[0:15], v[52:55], v[68:71], v[0:15]\n\t" \
    "ds_read_b64_tr_b16 v[68:69], v117 offset:0\n\t" \
    "ds_read_b64_tr_b16 v[70:71], v117 offset:512\n\t" \
    "v_lshlrev_b32 v104, 16, v52\n\t" \
    "v_and_b32 v105, 0xffff0000, v52\n\t" \
    "v_add_f32 v96, v96, v104\n\t" \
    "v_fmac_f32 v100, v104, v104\n\t" \
    "v_add_f32 v96, v96, v105\n\t" \
    "v_fmac_f32 v100, v105, v105\n\t" \
    "v_mfma_f32_32x32x16_bf16 v[16:31], v[52:55], v[84:87], v[16:31]\n\t" \
    "ds_read_b64_tr_b16 v[84:85], v121 offset:0\n\t" \
    "ds_read_b64_tr_b16 v[86:87], v121 offset:256\n\t" \
    "v_lshlrev_b32 v106, 16, v53\n\t" \
    "v_and_b32 v107, 0xffff0000, v53\n\t" \
    "v_add_f32 v97, v97, v106\n\t" \
    "v_fmac_f32 v101, v106, v106\n\t" \
    "v_add_f32 v97, v97, v107\n\t" \
    "v_fmac_f32 v101, v107, v107\n\t" \
    "v_mfma_f32_32x32x16_bf16 v[0:15], v[56:59], v[72:75], v[0:15]\n\t" \
    "ds_read_b64_tr_b16 v[72:73], v118 offset:0\n\t" \
    "ds_read_b64_tr_b16 v[74:75], v118 offset:512\n\t" \
    "ds_read_b128 v[56:59], v114 offset:0\n\t" \
    "v_lshlrev_b32 v108, 16, v54\n\t" \
    "v_and_b32 v109, 0xffff0000, v54\n\t" \
    "v_add_f32 v98, v98, v108\n\t" \
    "v_fmac_f32 v102, v108, v108\n\t" \
    "v_add_f32 v98, v98, v109\n\t" \
    "v_fmac_f32 v102, v109, v109\n\t" \
    "v_mfma_f32_32x32x16_bf16 v[0:15], v[60:63], v[76:79], v[0:15]\n\t" \
    "ds_read_b64_tr_b16 v[76:77], v119 offset:0\n\t" \
    "ds_read_b64_tr_b16 v[78:79], v119 offset:512\n\t" \
    "ds_read_b128 v[60:63], v115 offset:0\n\t" \
    "v_lshlrev_b32 v110, 16, v55\n\t" \
    "v_and_b32 v111, 0xffff0000, v55\n\t" \
    "v_add_f32 v99, v99, v110\n\t" \
    "v_fmac_f32 v103, v110, v110\n\t" \
    "v_add_f32 v99, v99, v111\n\t" \
    "v_fmac_f32 v103, v111, v111\n\t" \
    "ds_read_b128 v[52:55], v113 offset:0\n\t" \
    "s_barrier\n\t" \
    "s_waitcnt lgkmcnt(11)\n\t" \
    "v_mfma_f32_32x32x16_bf16 v[0:15], v[48:51], v[64:67], v[0:15]\n\t" \
    "v_lshlrev_b32 v104, 16, v48\n\t" \
    "v_and_b32 v105, 0xffff0000, v48\n\t" \
    "v_add_f32 v96, v96, v104\n\t" \
    "v_fmac_f32 v100, v104, v104\n\t" \
    "v_add_f32 v96, v96, v105\n\t" \
    "v_fmac_f32 v100, v105, v105\n\t" \
    "v_mfma_f32_32x32x16_bf16 v[32:47], v[64:67], v[88:91], v[32:47]\n\t" \
    "ds_read_b64_tr_b16 v[64:65], v116 offset:28672\n\t" \
    "ds_read_b64_tr_b16 v[66:67], v116 offset:29184\n\t" \
    "v_lshlrev_b32 v106, 16, v49\n\t" \
    "v_and_b32 v107, 0xffff0000, v49\n\t" \
    "v_add_f32 v97, v97, v106\n\t" \
    "v_fmac_f32 v101, v106, v106\n\t" \
    "v_add_f32 v97, v97, v107\n\t" \
    "v_fmac_f32 v101, v107, v107\n\t" \
    "v_mfma_f32_32x32x16_bf16 v[16:31], v[48:51], v[80:83], v[16:31]\n\t" \
    "v_lshlrev_b32 v108, 16, v50\n\t" \
    "v_and_b32 v109, 0xffff0000, v50\n\t" \
    "v_add_f32 v98, v98, v108\n\t" \
    "v_fmac_f32 v102, v108, v108\n\t" \
    "v_add_f32 v98, v98, v109\n\t" \
    "v_fmac_f32 v102, v109, v109\n\t" \
    "s_cmp_lg_u32 s40, 0\n\t" \
    "s_cbranch_scc1 L_nocs4_%=\n\t" \
    "v_mfma_f32_32x32x16_bf16 v[32:47], v[80:83], v[92:95], v[32:47]\n\t" \
    "L_nocs4_%=:\n\t" \
    "ds_read_b64_tr_b16 v[80:81], v120 offset:28672\n\t" \
    "ds_read_b64_tr_b16 v[82:83], v120 offset:28928\n\t" \
    "v_lshlrev_b32 v110, 16, v51\n\t" \
    "v_and_b32 v111, 0xffff0000, v51\n\t" \
    "v_add_f32 v99, v99, v110\n\t" \
    "v_fmac_f32 v103, v110, v110\n\t" \
    "v_add_f32 v99, v99, v111\n\t" \
    "v_fmac_f32 v103, v111, v111\n\t" \
    "ds_read_b128 v[48:51], v112 offset:28672\n\t" \
    "s_waitcnt lgkmcnt(5)\n\t" \
    "v_mfma_f32_32x32x16_bf16 v[0:15], v[52:55], v[68:71], v[0:15]\n\t" \
    "ds_read_b64_tr_b16 v[68:69], v117 offset:28672\n\t" \
    "ds_read_b64_tr_b16 v[70:71], v117 offset:29184\n\t" \
    "v_lshlrev_b32 v104, 16, v52\n\t" \
    "v_and_b32 v105, 0xffff0000, v52\n\t" \
    "v_add_f32 v96, v96, v104\n\t" \
    "v_fmac_f32 v100, v104, v104\n\t" \
    "v_add_f32 v96, v96, v105\n\t" \
    "v_fmac_f32 v100, v105, v105\n\t" \
    "v_mfma_f32_32x32x16_bf16 v[16:31], v[52:55], v[84:87], v[16:31]\n\t" \
    "ds_read_b64_tr_b16 v[84:85], v121 offset:28672\n\t" \
    "ds_read_b64_tr_b16 v[86:87], v121 offset:28928\n\t" \
    "v_lshlrev_b32 v106, 16, v53\n\t" \
    "v_and_b32 v107, 0xffff0000, v53\n\t" \
    "v_add_f32 v97, v97, v106\n\t" \
    "v_fmac_f32 v101, v106, v106\n\t" \
    "v_add_f32 v97, v97, v107\n\t" \
    "v_fmac_f32 v101, v107, v107\n\t" \
    "v_mfma_f32_32x32x16_bf16 v[0:15], v[56:59], v[72:75], v[0:15]\n\t" \
    "ds_read_b64_tr_b16 v[72:73], v118 offset:28672\n\t" \
    "ds_read_b64_tr_b16 v[74:75], v118 offset:29184\n\t" \
    "ds_read_b128 v[56:59], v114 offset:28672\n\t" \
    "v_lshlrev_b32 v108, 16, v54\n\t" \
    "v_and_b32 v109, 0xffff0000, v54\n\t" \
    "v_add_f32 v98, v98, v108\n\t" \
    "v_fmac_f32 v102, v108, v108\n\t" \
    "v_add_f32 v98, v98, v109\n\t" \
    "v_fmac_f32 v102, v109, v109\n\t" \
    "v_mfma_f32_32x32x16_bf16 v[0:15], v[60:63], v[76:79], v[0:15]\n\t" \
    "ds_read_b64_tr_b16 v[76:77], v119 offset:28672\n\t" \
    "ds_read_b64_tr_b16 v[78:79], v119 offset:29184\n\t" \
    "ds_read_b128 v[60:63], v115 offset:28672\n\t" \
    "v_lshlrev_b32 v110, 16, v55\n\t" \
    "v_and_b32 v111, 0xffff0000, v55\n\t" \
    "v_add_f32 v99, v99, v110\n\t" \
    "v_fmac_f32 v103, v110, v110\n\t" \
    "v_add_f32 v99, v99, v111\n\t" \
    "v_fmac_f32 v103, v111, v111\n\t" \
    "ds_read_b128 v[52:55], v113 offset:28672\n\t" \
    "s_barrier\n\t" \
    "v_add_u32 v112, 0xe000, v112\n\t" \
    "v_add_u32 v113, 0xe000, v113\n\t" \
    "v_add_u32 v114, 0xe000, v114\n\t" \
    "v_add_u32 v115, 0xe000, v115\n\t" \
    "v_add_u32 v116, 0xe000, v116\n\t" \
    "v_add_u32 v117, 0xe000, v117\n\t" \
    "v_add_u32 v118, 0xe000, v118\n\t" \
    "v_add_u32 v119, 0xe000, v119\n\t" \
    "v_add_u32 v120, 0xe000, v120\n\t" \
    "v_add_u32 v121, 0xe000, v121\n\t" \
    "s_waitcnt lgkmcnt(11)\n\t" \
    "v_mfma_f32_32x32x16_bf16 v[0:15], v[48:51], v[64:67], v[0:15]\n\t" \
    "v_lshlrev_b32 v104, 16, v48\n\t" \
    "v_and_b32 v105, 0xffff0000, v48\n\t" \
    "v_add_f32 v96, v96, v104\n\t" \
    "v_fmac_f32 v100, v104, v104\n\t" \
    "v_add_f32 v96, v96, v105\n\t" \
    "v_fmac_f32 v100, v105, v105\n\t" \
    "v_mfma_f32_32x32x16_bf16 v[32:47], v[64:67], v[88:91], v[32:47]\n\t" \
    "ds_read_b64_tr_b16 v[64:65], v116 offset:0\n\t" \
    "ds_read_b64_tr_b16 v[66:67], v116 offset:512\n\t" \
    "v_lshlrev_b32 v106, 16, v49\n\t" \
    "v_and_b32 v107, 0xffff0000, v49\n\t" \
    "v_add_f32 v97, v97, v106\n\t" \
    "v_fmac_f32 v101, v106, v106\n\t" \
    "v_add_f32 v97, v97, v107\n\t" \
    "v_fmac_f32 v101, v107, v107\n\t" \
    "v_mfma_f32_32x32x16_bf16 v[16:31], v[48:51], v[80:83], v[16:31]\n\t" \
    "v_lshlrev_b32 v108, 16, v50\n\t" \
    "v_and_b32 v109, 0xffff0000, v50\n\t" \
    "v_add_f32 v98, v98, v108\n\t" \
    "v_fmac_f32 v102, v108, v108\n\t" \
    "v_add_f32 v98, v98, v109\n\t" \
    "v_fmac_f32 v102, v109, v109\n\t" \
    "s_cmp_lg_u32 s40, 0\n\t" \
    "s_cbranch_scc1 L_nocs5_%=\n\t" \
    "v_mfma_f32_32x32x16_bf16 v[32:47], v[80:83], v[92:95], v[32:47]\n\t" \
    "L_nocs5_%=:\n\t" \
    "ds_read_b64_tr_b16 v[80:81], v120 offset:0\n\t" \
    "ds_read_b64_tr_b16 v[82:83], v120 offset:256\n\t" \
    "v_lshlrev_b32 v110, 16, v51\n\t" \
    "v_and_b32 v111, 0xffff0000, v51\n\t" \
    "v_add_f32 v99, v99, v110\n\t" \
    "v_fmac_f32 v103, v110, v110\n\t" \
    "v_add_f32 v99, v99, v111\n\t" \
    "v_fmac_f32 v103, v111, v111\n\t" \
    "ds_read_b128 v[48:51], v112 offset:0\n\t" \
    "s_waitcnt lgkmcnt(5)\n\t" \
    "v_mfma_f32_32x32x16_bf16 v[0:15], v[52:55], v[68:71], v[0:15]\n\t" \
    "ds_read_b64_tr_b16 v[68:69], v117 offset:0\n\t" \
    "ds_read_b64_tr_b16 v[70:71], v117 offset:512\n\t" \
    "v_lshlrev_b32 v104, 16, v52\n\t" \
    "v_and_b32 v105, 0xffff0000, v52\n\t" \
    "v_add_f32 v96, v96, v104\n\t" \
    "v_fmac_f32 v100, v104, v104\n\t" \
    "v_add_f32 v96, v96, v105\n\t" \
    "v_fmac_f32 v100, v105, v105\n\t" \
    "v_mfma_f32_32x32x16_bf16 v[16:31], v[52:55], v[84:87], v[16:31]\n\t" \
    "ds_read_b64_tr_b16 v[84:85], v121 offset:0\n\t" \
    "ds_read_b64_tr_b16 v[86:87], v121 offset:256\n\t" \
    "v_lshlrev_b32 v106, 16, v53\n\t" \
    "v_and_b32 v107, 0xffff0000, v53\n\t" \
    "v_add_f32 v97, v97, v106\n\t" \
    "v_fmac_f32 v101, v106, v106\n\t" \
    "v_add_f32 v97, v97, v107\n\t" \
    "v_fmac_f32 v101, v107, v107\n\t" \
    "v_mfma_f32_32x32x16_bf16 v[0:15], v[56:59], v[72:75], v[0:15]\n\t" \
    "ds_read_b64_tr_b16 v[72:73], v118 offset:0\n\t" \
    "ds_read_b64_tr_b16 v[74:75], v118 offset:512\n\t" \
    "ds_read_b128 v[56:59], v114 offset:0\n\t" \
    "v_lshlrev_b32 v108, 16, v54\n\t" \
    "v_and_b32 v109, 0xffff0000, v54\n\t" \
    "v_add_f32 v98, v98, v108\n\t" \
    "v_fmac_f32 v102, v108, v108\n\t" \
    "v_add_f32 v98, v98, v109\n\t" \
    "v_fmac_f32 v102, v109, v109\n\t" \
    "v_mfma_f32_32x32x16_bf16 v[0:15], v[60:63], v[76:79], v[0:15]\n\t" \
    "ds_read_b64_tr_b16 v[76:77], v119 offset:0\n\t" \
    "ds_read_b64_tr_b16 v[78:79], v119 offset:512\n\t" \
    "ds_read_b128 v[60:63], v115 offset:0\n\t" \
    "v_lshlrev_b32 v110, 16, v55\n\t" \
    "v_and_b32 v111, 0xffff0000, v55\n\t" \
    "v_add_f32 v99, v99, v110\n\t" \
    "v_fmac_f32 v103, v110, v110\n\t" \
    "v_add_f32 v99, v99, v111\n\t" \
    "v_fmac_f32 v103, v111, v111\n\t" \
    "ds_read_b128 v[52:55], v113 offset:0\n\t" \
    "s_barrier\n\t" \
    "s_waitcnt lgkmcnt(11)\n\t" \
    "v_mfma_f32_32x32x16_bf16 v[0:15], v[48:51], v[64:67], v[0:15]\n\t" \
    "v_lshlrev_b32 v104, 16, v48\n\t" \
    "v_and_b32 v105, 0xffff0000, v48\n\t" \
    "v_add_f32 v96, v96, v104\n\t" \
    "v_fmac_f32 v100, v104, v104\n\t" \
    "v_add_f32 v96, v96, v105\n\t" \
    "v_fmac_f32 v100, v105, v105\n\t" \
    "v_mfma_f32_32x32x16_bf16 v[32:47], v[64:67], v[88:91], v[32:47]\n\t" \
    "ds_read_b64_tr_b16 v[64:65], v116 offset:28672\n\t" \
    "ds_read_b64_tr_b16 v[66:67], v116 offset:29184\n\t" \
    "v_lshlrev_b32 v106, 16, v49\n\t" \
    "v_and_b32 v107, 0xffff0000, v49\n\t" \
    "v_add_f32 v97, v97, v106\n\t" \
    "v_fmac_f32 v101, v106, v106\n\t" \
    "v_add_f32 v97, v97, v107\n\t" \
    "v_fmac_f32 v101, v107, v107\n\t" \
    "v_mfma_f32_32x32x16_bf16 v[16:31], v[48:51], v[80:83], v[16:31]\n\t" \
    "v_lshlrev_b32 v108, 16, v50\n\t" \
    "v_and_b32 v109, 0xffff0000, v50\n\t" \
    "v_add_f32 v98, v98, v108\n\t" \
    "v_fmac_f32 v102, v108, v108\n\t" \
    "v_add_f32 v98, v98, v109\n\t" \
    "v_fmac_f32 v102, v109, v109\n\t" \
    "s_cmp_lg_u32 s40, 0\n\t" \
    "s_cbranch_scc1 L_nocs6_%=\n\t" \
    "v_mfma_f32_32x32x16_bf16 v[32:47], v[80:83], v[92:95], v[32:47]\n\t" \
    "L_nocs6_%=:\n\t" \
    "ds_read_b64_tr_b16 v[80:81], v120 offset:28672\n\t" \
    "ds_read_b64_tr_b16 v[82:83], v120 offset:28928\n\t" \
    "v_lshlrev_b32 v110, 16, v51\n\t" \
    "v_and_b32 v111, 0xffff0000, v51\n\t" \
    "v_add_f32 v99, v99, v110\n\t" \
    "v_fmac_f32 v103, v110, v110\n\t" \
    "v_add_f32 v99, v99, v111\n\t" \
    "v_fmac_f32 v103, v111, v111\n\t" \
    "ds_read_b128 v[48:51], v112 offset:28672\n\t" \
    "s_waitcnt lgkmcnt(5)\n\t" \
    "v_mfma_f32_32x32x16_bf16 v[0:15], v[52:55], v[68:71], v[0:15]\n\t" \
    "ds_read_b64_tr_b16 v[68:69], v117 offset:28672\n\t" \
    "ds_read_b64_tr_b16 v[70:71], v117 offset:29184\n\t" \
    "v_lshlrev_b32 v104, 16, v52\n\t" \
    "v_and_b32 v105, 0xffff0000, v52\n\t" \
    "v_add_f32 v96, v96, v104\n\t" \
    "v_fmac_f32 v100, v104, v104\n\t" \
    "v_add_f32 v96, v96, v105\n\t" \
    "v_fmac_f32 v100, v105, v105\n\t" \
    "v_mfma_f32_32x32x16_bf16 v[16:31], v[52:55], v[84:87], v[16:31]\n\t" \
    "ds_read_b64_tr_b16 v[84:85], v121 offset:28672\n\t" \
    "ds_read_b64_tr_b16 v[86:87], v121 offset:28928\n\t" \
    "v_lshlrev_b32 v106, 16, v53\n\t" \
    "v_and_b32 v107, 0xffff0000, v53\n\t" \
    "v_add_f32 v97, v97, v106\n\t" \
    "v_fmac_f32 v101, v106, v106\n\t" \
    "v_add_f32 v97, v97, v107\n\t" \
    "v_fmac_f32 v101, v107, v107\n\t" \
    "v_mfma_f32_32x32x16_bf16 v[0:15], v[56:59], v[72:75], v[0:15]\n\t" \
    "ds_read_b64_tr_b16 v[72:73], v118 offset:28672\n\t" \
    "ds_read_b64_tr_b16 v[74:75], v118 offset:29184\n\t" \
    "ds_read_b128 v[56:59], v114 offset:28672\n\t" \
    "v_lshlrev_b32 v108, 16, v54\n\t" \
    "v_and_b32 v109, 0xffff0000, v54\n\t" \
    "v_add_f32 v98, v98, v108\n\t" \
    "v_fmac_f32 v102, v108, v108\n\t" \
    "v_add_f32 v98, v98, v109\n\t" \
    "v_fmac_f32 v102, v109, v109\n\t" \
    "v_mfma_f32_32x32x16_bf16 v[0:15], v[60:63], v[76:79], v[0:15]\n\t" \
    "ds_read_b64_tr_b16 v[76:77], v119 offset:28672\n\t" \
    "ds_read_b64_tr_b16 v[78:79], v119 offset:29184\n\t" \
    "ds_read_b128 v[60:63], v115 offset:28672\n\t" \
    "v_lshlrev_b32 v110, 16, v55\n\t" \
    "v_and_b32 v111, 0xffff0000, v55\n\t" \
    "v_add_f32 v99, v99, v110\n\t" \
    "v_fmac_f32 v103, v110, v110\n\t" \
    "v_add_f32 v99, v99, v111\n\t" \
    "v_fmac_f32 v103, v111, v111\n\t" \
    "ds_read_b128 v[52:55], v113 offset:28672\n\t" \
    "s_waitcnt lgkmcnt(11)\n\t" \
    "v_mfma_f32_32x32x16_bf16 v[0:15], v[48:51], v[64:67], v[0:15]\n\t" \
    "v_lshlrev_b32 v104, 16, v48\n\t" \
    "v_and_b32 v105, 0xffff0000, v48\n\t" \
    "v_add_f32 v96, v96, v104\n\t" \
    "v_fmac_f32 v100, v104, v104\n\t" \
    "v_add_f32 v96, v96, v105\n\t" \
    "v_fmac_f32 v100, v105, v105\n\t" \
    "v_mfma_f32_32x32x16_bf16 v[32:47], v[64:67], v[88:91], v[32:47]\n\t" \
    "v_lshlrev_b32 v106, 16, v49\n\t" \
    "v_and_b32 v107, 0xffff0000, v49\n\t" \
    "v_add_f32 v97, v97, v106\n\t" \
    "v_fmac_f32 v101, v106, v106\n\t" \
    "v_add_f32 v97, v97, v107\n\t" \
    "v_fmac_f32 v101, v107, v107\n\t" \
    "v_mfma_f32_32x32x16_bf16 v[16:31], v[48:51], v[80:83], v[16:31]\n\t" \
    "v_lshlrev_b32 v108, 16, v50\n\t" \
    "v_and_b32 v109, 0xffff0000, v50\n\t" \
    "v_add_f32 v98, v98, v108\n\t" \
    "v_fmac_f32 v102, v108, v108\n\t" \
    "v_add_f32 v98, v98, v109\n\t" \
    "v_fmac_f32 v102, v109, v109\n\t" \
    "s_cmp_lg_u32 s40, 0\n\t" \
    "s_cbranch_scc1 L_nocs7_%=\n\t" \
    "v_mfma_f32_32x32x16_bf16 v[32:47], v[80:83], v[92:95], v[32:47]\n\t" \
    "L_nocs7_%=:\n\t" \
    "v_lshlrev_b32 v110, 16, v51\n\t" \
    "v_and_b32 v111, 0xffff0000, v51\n\t" \
    "v_add_f32 v99, v99, v110\n\t" \
    "v_fmac_f32 v103, v110, v110\n\t" \
    "v_add_f32 v99, v99, v111\n\t" \
    "v_fmac_f32 v103, v111, v111\n\t" \
    "s_waitcnt lgkmcnt(0)\n\t" \
    "v_mfma_f32_32x32x16_bf16 v[0:15], v[52:55], v[68:71], v[0:15]\n\t" \
    "v_lshlrev_b32 v104, 16, v52\n\t" \
    "v_and_b32 v105, 0xffff0000, v52\n\t" \
    "v_add_f32 v96, v96, v104\n\t" \
    "v_fmac_f32 v100, v104, v104\n\t" \
    "v_add_f32 v96, v96, v105\n\t" \
    "v_fmac_f32 v100, v105, v105\n\t" \
    "v_mfma_f32_32x32x16_bf16 v[16:31], v[52:55], v[84:87], v[16:31]\n\t" \
    "v_lshlrev_b32 v106, 16, v53\n\t" \
    "v_and_b32 v107, 0xffff0000, v53\n\t" \
    "v_add_f32 v97, v97, v106\n\t" \
    "v_fmac_f32 v101, v106, v106\n\t" \
    "v_add_f32 v97, v97, v107\n\t" \
    "v_fmac_f32 v101, v107, v107\n\t" \
    "v_mfma_f32_32x32x16_bf16 v[0:15], v[56:59], v[72:75], v[0:15]\n\t" \
    "v_lshlrev_b32 v108, 16, v54\n\t" \
    "v_and_b32 v109, 0xffff0000, v54\n\t" \
    "v_add_f32 v98, v98, v108\n\t" \
    "v_fmac_f32 v102, v108, v108\n\t" \
    "v_add_f32 v98, v98, v109\n\t" \
    "v_fmac_f32 v102, v109, v109\n\t" \
    "v_mfma_f32_32x32x16_bf16 v[0:15], v[60:63], v[76:79], v[0:15]\n\t" \
    "v_lshlrev_b32 v110, 16, v55\n\t" \
    "v_and_b32 v111, 0xffff0000, v55\n\t" \
    "v_add_f32 v99, v99, v110\n\t" \
    "v_fmac_f32 v103, v110, v110\n\t" \
    "v_add_f32 v99, v99, v111\n\t" \
    "v_fmac_f32 v103, v111, v111\n\t" \
    "s_nop 15\n\t" \
    "v_mbcnt_lo_u32_b32 v104, -1, 0\n\t" \
    "v_mbcnt_hi_u32_b32 v104, -1, v104\n\t" \
    "v_lshlrev_b32 v105, 4, v104\n\t" \
    "v_lshlrev_b32 v106, 2, v104\n\t" \
    "v_add_u32 v107, s44, v105\n\t" \
    "v_add_u32 v108, s46, v106\n\t" \
    "v_add_f32 v96, v96, v97\n\t" \
    "v_add_f32 v98, v98, v99\n\t" \
    "v_add_f32 v100, v100, v101\n\t" \
    "v_add_f32 v102, v102, v103\n\t" \
    "s_cmp_lg_u32 s40, 0\n\t" \
    "s_cbranch_scc1 L_eg1_%=\n\t" \
    "ds_write_b128 v107, v[24:27]\n\t" \
    "ds_write_b128 v107, v[28:31] offset:1024\n\t" \
    "s_mov_b32 s70, 0x00010001\n\t" \
    "s_branch L_egd_%=\n\t" \
    "L_eg1_%=:\n\t" \
    "ds_write_b128 v107, v[16:19]\n\t" \
    "ds_write_b128 v107, v[20:23] offset:1024\n\t" \
    "s_mov_b32 s70, 0x00000001\n\t" \
    "L_egd_%=:\n\t" \
    "v_add_f32 v96, v96, v98\n\t" \
    "v_add_f32 v100, v100, v102\n\t" \
    "s_lshl_b32 s72, s40, 9\n\t" \
    "s_lshl_b32 s73, s48, 7\n\t" \
    "s_add_u32 s72, s72, s73\n\t" \
    "s_add_u32 s72, s72, s47\n\t" \
    "s_add_u32 s72, s72, 20480\n\t" \
    "v_lshrrev_b32 v109, 5, v104\n\t" \
    "v_lshlrev_b32 v109, 4, v109\n\t" \
    "v_and_b32 v107, 16, v104\n\t" \
    "v_lshlrev_b32 v107, 6, v107\n\t" \
    "v_add3_u32 v109, v109, v107, s72\n\t" \
    "ds_write_b32 v108, v96\n\t" \
    "ds_write_b32 v108, v100 offset:256\n\t" \
    "s_mov_b32 exec_lo, s70\n\t" \
    "s_mov_b32 exec_hi, s70\n\t" \
    "ds_write_b128 v109, v[32:35] offset:0\n\t" \
    "ds_write_b128 v109, v[36:39] offset:32\n\t" \
    "ds_write_b128 v109, v[40:43] offset:64\n\t" \
    "ds_write_b128 v109, v[44:47] offset:96\n\t" \
    "s_mov_b64 exec, -1\n\t" \
    "s_waitcnt lgkmcnt(0)\n\t" \
    "s_barrier\n\t" \
    "v_add_u32 v107, s45, v105\n\t" \
    "ds_read_b128 v[112:115], v107\n\t" \
    "ds_read_b128 v[116:119], v107 offset:1024\n\t" \
    "v_and_b32 v108, 31, v104\n\t" \
    "v_lshlrev_b32 v108, 2, v108\n\t" \
    "v_lshrrev_b32 v109, 5, v104\n\t" \
    "v_lshlrev_b32 v109, 9, v109\n\t" \
    "v_add3_u32 v107, v108, v109, s47\n\t" \
    "v_add_u32 v107, 0x4000, v107\n\t" \
    "v_add_u32 v109, 128, v107\n\t" \
    "ds_read2st64_b32 v[60:61], v107 offset0:0 offset1:1\n\t" \
    "ds_read2st64_b32 v[62:63], v109 offset0:0 offset1:1\n\t" \
    "ds_read2st64_b32 v[64:65], v107 offset0:8 offset1:9\n\t" \
    "ds_read2st64_b32 v[66:67], v109 offset0:8 offset1:9\n\t" \
    "ds_read2st64_b32 v[68:69], v107 offset0:4 offset1:5\n\t" \
    "ds_read2st64_b32 v[70:71], v109 offset0:4 offset1:5\n\t" \
    "ds_read2st64_b32 v[72:73], v107 offset0:12 offset1:13\n\t" \
    "ds_read2st64_b32 v[74:75], v109 offset0:12 offset1:13\n\t" \
    "v_add_u32 v108, s47, v108\n\t" \
    "v_add_u32 v108, 20480, v108\n\t" \
    "v_add_u32 v110, 128, v108\n\t" \
    "s_lshl_b32 s72, s40, 9\n\t" \
    "v_add_u32 v111, s72, v108\n\t" \
    "v_add_u32 v120, s72, v110\n\t" \
    "ds_read2st64_b32 v[122:123], v111 offset0:0 offset1:1\n\t" \
    "ds_read2st64_b32 v[124:125], v120 offset0:0 offset1:1\n\t" \
    "ds_read2st64_b32 v[126:127], v108 offset0:4 offset1:5\n\t" \
    "ds_read2st64_b32 v[128:129], v110 offset0:4 offset1:5\n\t" \
    "s_waitcnt lgkmcnt(8)\n\t" \
    "v_add_f32 v60, v60, v62\n\t" \
    "v_add_f32 v64, v64, v66\n\t" \
    "v_add_f32 v60, v60, v64\n\t" \
    "v_add_f32 v61, v61, v63\n\t" \
    "v_add_f32 v65, v65, v67\n\t" \
    "v_add_f32 v61, v61, v65\n\t" \
    "v_mul_f32 v92, 0x3b000000, v60\n\t" \
    "v_mul_f32 v62, v92, v92\n\t" \
    "v_fma_f32 v61, v61, s54, -v62\n\t" \
    "v_max_f32 v61, 0, v61\n\t" \
    "v_add_f32 v88, 0x3727c5ac, v61\n\t" \
    "v_rsq_f32 v76, v88\n\t" \
    "v_mov_b32 v93, v92\n\t" \
    "v_mov_b32 v89, v88\n\t" \
    "v_mov_b32 v77, v76\n\t" \
    "s_nop 1\n\t" \
    "v_permlane32_swap_b32 v92, v93\n\t" \
    "v_permlane32_swap_b32 v88, v89\n\t" \
    "v_permlane32_swap_b32 v76, v77\n\t" \
    "s_waitcnt lgkmcnt(4)\n\t" \
    "v_add_f32 v68, v68, v70\n\t" \
    "v_add_f32 v72, v72, v74\n\t" \
    "v_add_f32 v68, v68, v72\n\t" \
    "v_add_f32 v69, v69, v71\n\t" \
    "v_add_f32 v73, v73, v75\n\t" \
    "v_add_f32 v69, v69, v73\n\t" \
    "v_mul_f32 v94, 0x3b000000, v68\n\t" \
    "v_mul_f32 v70, v94, v94\n\t" \
    "v_fma_f32 v69, v69, s54, -v70\n\t" \
    "v_max_f32 v69, 0, v69\n\t" \
    "v_add_f32 v90, 0x3727c5ac, v69\n\t" \
    "v_rsq_f32 v78, v90\n\t" \
    "v_mov_b32 v95, v94\n\t" \
    "v_mov_b32 v91, v90\n\t" \
    "v_mov_b32 v79, v78\n\t" \
    "s_nop 1\n\t" \
    "v_permlane32_swap_b32 v94, v95\n\t" \
    "v_permlane32_swap_b32 v90, v91\n\t" \
    "v_permlane32_swap_b32 v78, v79\n\t" \
    "s_waitcnt lgkmcnt(0)\n\t" \
    "v_add_f32 v122, v122, v124\n\t" \
    "v_add_f32 v123, v123, v125\n\t" \
    "v_add_f32 v126, v126, v128\n\t" \
    "v_add_f32 v127, v127, v129\n\t" \
    "v_add_f32 v122, v122, v123\n\t" \
    "v_add_f32 v126, v126, v127\n\t" \
    "v_cvt_pk_bf16_f32 v48, v122, v122\n\t" \
    "v_lshlrev_b32 v49, 16, v48\n\t" \
    "v_sub_f32 v49, v122, v49\n\t" \
    "v_cvt_pk_bf16_f32 v49, v49, 0\n\t" \
    "v_mov_b32 v50, 0\n\t" \
    "v_mov_b32 v51, 0\n\t" \
    "v_cvt_pk_bf16_f32 v52, v126, v126\n\t" \
    "v_lshlrev_b32 v53, 16, v52\n\t" \
    "v_sub_f32 v53, v126, v53\n\t" \
    "v_cvt_pk_bf16_f32 v53, v53, 0\n\t" \
    "v_mov_b32 v54, 0\n\t" \
    "v_mov_b32 v55, 0\n\t" \
    "s_set_gpr_idx_on s48, gpr_idx(SRC0)\n\t" \
    "v_mov_b32 v120, v92\n\t" \
    "v_mov_b32 v121, v76\n\t" \
    "s_set_gpr_idx_off\n\t" \
    "v_cvt_pk_bf16_f32 v57, -v120, 0\n\t" \
    "v_lshlrev_b32 v58, 16, v57\n\t" \
    "v_sub_f32 v58, -v120, v58\n\t" \
    "v_cvt_pk_bf16_f32 v56, -v120, v58\n\t" \
    "v_mov_b32 v58, 0\n\t" \
    "v_mov_b32 v59, 0\n\t" \
    "s_mov_b32 exec_lo, 0\n\t" \
    "v_mov_b32 v48, 0\n\t" \
    "v_mov_b32 v49, 0\n\t" \
    "v_mov_b32 v52, 0\n\t" \
    "v_mov_b32 v53, 0\n\t" \
    "v_mov_b32 v56, 0\n\t" \
    "v_mov_b32 v57, 0\n\t" \
    "s_mov_b32 exec_lo, -1\n\t" \
    "s_nop 1\n\t" \
    "v_mfma_f32_32x32x16_bf16 v[0:15], v[56:59], v[48:51], v[0:15]\n\t" \
    "v_mfma_f32_32x32x16_bf16 v[16:31], v[56:59], v[52:55], v[16:31]\n\t" \
    "v_mul_f32 v84, 0xc0135761, v76\n\t" \
    "v_mul_f32 v80, v76, v76\n\t" \
    "v_mul_f32 v80, v80, v76\n\t" \
    "v_mul_f32 v80, 0xbdd2d3e8, v80\n\t" \
    "v_mul_f32 v85, 0xc0135761, v77\n\t" \
    "v_mul_f32 v81, v77, v77\n\t" \
    "v_mul_f32 v81, v81, v77\n\t" \
    "v_mul_f32 v81, 0xbdd2d3e8, v81\n\t" \
    "v_mul_f32 v86, 0xc0135761, v78\n\t" \
    "v_mul_f32 v82, v78, v78\n\t" \
    "v_mul_f32 v82, v82, v78\n\t" \
    "v_mul_f32 v82, 0xbdd2d3e8, v82\n\t" \
    "v_mul_f32 v87, 0xc0135761, v79\n\t" \
    "v_mul_f32 v83, v79, v79\n\t" \
    "v_mul_f32 v83, v83, v79\n\t" \
    "v_mul_f32 v83, 0xbdd2d3e8, v83\n\t" \
    "v_and_b32 v108, 31, v104\n\t" \
    "v_lshlrev_b32 v108, 2, v108\n\t" \
    "s_lshl_b32 s72, s55, 7\n\t" \
    "s_add_u32 s72, s72, s47\n\t" \
    "s_add_u32 s72, s72, 22016\n\t" \
    "v_add_u32 v108, s72, v108\n\t" \
    "ds_write_b32 v108, v121\n\t" \
    "v_lshrrev_b32 v109, 5, v104\n\t" \
    "v_lshlrev_b32 v109, 4, v109\n\t" \
    "v_add_u32 v109, s72, v109\n\t" \
    "s_waitcnt lgkmcnt(0)\n\t" \
    "ds_read_b128 v[60:63], v109 offset:0\n\t" \
    "ds_read_b128 v[64:67], v109 offset:32\n\t" \
    "ds_read_b128 v[68:71], v109 offset:64\n\t" \
    "ds_read_b128 v[72:75], v109 offset:96\n\t" \
    "v_and_b32 v110, 15, v104\n\t" \
    "v_lshlrev_b32 v110, 4, v110\n\t" \
    "v_lshrrev_b32 v111, 5, v104\n\t" \
    "v_lshl_add_u32 v110, v111, 8, v110\n\t" \
    "v_and_b32 v111, 16, v104\n\t" \
    "v_lshl_add_u32 v110, v111, 12, v110\n\t" \
    "s_nop 7\n\t" \
    "s_waitcnt lgkmcnt(0)\n\t" \
    "v_mul_f32 v0, v0, v60\n\t" \
    "v_mul_f32 v1, v1, v61\n\t" \
    "v_mul_f32 v2, v2, v62\n\t" \
    "v_mul_f32 v3, v3, v63\n\t" \
    "v_mul_f32 v4, v4, v64\n\t" \
    "v_mul_f32 v5, v5, v65\n\t" \
    "v_mul_f32 v6, v6, v66\n\t" \
    "v_mul_f32 v7, v7, v67\n\t" \
    "v_cvt_pk_bf16_f32 v48, v0, v1\n\t" \
    "v_cvt_pk_bf16_f32 v49, v2, v3\n\t" \
    "v_cvt_pk_bf16_f32 v50, v4, v5\n\t" \
    "v_cvt_pk_bf16_f32 v51, v6, v7\n\t" \
    "buffer_store_dwordx4 v[48:51], v110, s[60:63], s49 offen sc1\n\t" \
    "v_mul_f32 v8, v8, v68\n\t" \
    "v_mul_f32 v9, v9, v69\n\t" \
    "v_mul_f32 v10, v10, v70\n\t" \
    "v_mul_f32 v11, v11, v71\n\t" \
    "v_mul_f32 v12, v12, v72\n\t" \
    "v_mul_f32 v13, v13, v73\n\t" \
    "v_mul_f32 v14, v14, v74\n\t" \
    "v_mul_f32 v15, v15, v75\n\t" \
    "v_cvt_pk_bf16_f32 v52, v8, v9\n\t" \
    "v_cvt_pk_bf16_f32 v53, v10, v11\n\t" \
    "v_cvt_pk_bf16_f32 v54, v12, v13\n\t" \
    "v_cvt_pk_bf16_f32 v55, v14, v15\n\t" \
    "buffer_store_dwordx4 v[52:55], v110, s[60:63], s50 offen sc1\n\t" \
    "s_cmp_lg_u32 s40, 0\n\t" \
    "s_cbranch_scc1 L_eo1_%=\n\t" \
    "v_add_f32 v16, v16, v112\n\t" \
    "v_add_f32 v17, v17, v113\n\t" \
    "v_add_f32 v18, v18, v114\n\t" \
    "v_add_f32 v19, v19, v115\n\t" \
    "v_add_f32 v20, v20, v116\n\t" \
    "v_add_f32 v21, v21, v117\n\t" \
    "v_add_f32 v22, v22, v118\n\t" \
    "v_add_f32 v23, v23, v119\n\t" \
    "v_mul_f32 v16, v16, v60\n\t" \
    "v_mul_f32 v17, v17, v61\n\t" \
    "v_mul_f32 v18, v18, v62\n\t" \
    "v_mul_f32 v19, v19, v63\n\t" \
    "v_mul_f32 v20, v20, v64\n\t" \
    "v_mul_f32 v21, v21, v65\n\t" \
    "v_mul_f32 v22, v22, v66\n\t" \
    "v_mul_f32 v23, v23, v67\n\t" \
    "v_cvt_pk_bf16_f32 v56, v16, v17\n\t" \
    "v_cvt_pk_bf16_f32 v57, v18, v19\n\t" \
    "v_cvt_pk_bf16_f32 v58, v20, v21\n\t" \
    "v_cvt_pk_bf16_f32 v59, v22, v23\n\t" \
    "buffer_store_dwordx4 v[56:59], v110, s[60:63], s51 offen sc1\n\t" \
    "s_branch L_eod_%=\n\t" \
    "L_eo1_%=:\n\t" \
    "v_add_f32 v24, v24, v112\n\t" \
    "v_add_f32 v25, v25, v113\n\t" \
    "v_add_f32 v26, v26, v114\n\t" \
    "v_add_f32 v27, v27, v115\n\t" \
    "v_add_f32 v28, v28, v116\n\t" \
    "v_add_f32 v29, v29, v117\n\t" \
    "v_add_f32 v30, v30, v118\n\t" \
    "v_add_f32 v31, v31, v119\n\t" \
    "v_mul_f32 v24, v24, v68\n\t" \
    "v_mul_f32 v25, v25, v69\n\t" \
    "v_mul_f32 v26, v26, v70\n\t" \
    "v_mul_f32 v27, v27, v71\n\t" \
    "v_mul_f32 v28, v28, v72\n\t" \
    "v_mul_f32 v29, v29, v73\n\t" \
    "v_mul_f32 v30, v30, v74\n\t" \
    "v_mul_f32 v31, v31, v75\n\t" \
    "v_cvt_pk_bf16_f32 v56, v24, v25\n\t" \
    "v_cvt_pk_bf16_f32 v57, v26, v27\n\t" \
    "v_cvt_pk_bf16_f32 v58, v28, v29\n\t" \
    "v_cvt_pk_bf16_f32 v59, v30, v31\n\t" \
    "buffer_store_dwordx4 v[56:59], v110, s[60:63], s51 offen sc1\n\t" \
    "L_eod_%=:\n\t" \
    ""
struct G1C { f32x4 k3, k1, q, mean; };
template <int VMASK>
DI void k1_body(char* lds_, const u16* __restrict__ x, const u16* __restrict__ W, char* __restrict__ ws, int mb, int nb, G1C& gc, int w, unsigned oslot) {
  const int mt = w & 3, g = w >> 2;
  typedef __attribute__((ext_vector_type(4))) unsigned srd_t;
  const unsigned long long wsa_ = (unsigned long long)ws;
  const __amdgpu_buffer_rsrc_t rws = mk_rsrc(ws, 32u * MB);
  f32x16 aco, acs, cs;
  f32x4 sum4 = {0.f, 0.f, 0.f, 0.f}, sq4 = sum4;
  {
  const int lane = lane_fresh();
  const unsigned lds0 = (unsigned)(uintptr_t)lds_;
  const int h = lane >> 5, i16 = lane & 15, q4 = i16 >> 2, p4 = i16 & 3, blk = (lane >> 4) & 1;
  const srd_t rsw = {rfl((unsigned)wsa_), rfl((unsigned)(wsa_ >> 32) & 0xffffu), 32u * MB, 0x00020000u};
  st16(rws, oslot + lane * 16, u32x4{~0u, ~0u, ~0u, ~0u});
  const int sw = (lane >> 1) & 7;
  u32x4 xa, wa; u32x2 wsa;
#pragma unroll
  for (int j = 0; j < 4; ++j) {
    const int ks = (mt + 2 * g + j) & 3;
    xa[j] = lds0 + (mt * 32 + (lane & 31)) * 128 + (((2 * ks + h) ^ sw) * 16);
    const unsigned wo = lds0 + 16384 + ks * 1024 + (8 * h + q4) * 64 + blk * 32 + p4 * 8;
    wa[j] = lds0 + 16384 + ks * 2048 + (8 * h + q4) * 128 + ((g ^ (q4 >> 1)) & 1) * 64 + blk * 32 + p4 * 8;
    if (j < 2) wsa[j] = wo + 8192;
  }
  const unsigned mlo = (lane & 16) ? 0u : 0x3f803f80u, mhi = (lane & 16) ? 0x3f803f80u : 0u;
  const u32x4 ml = {mlo, mlo, mlo, mlo}, mh = {mhi, mhi, mhi, mhi};
#pragma unroll
  for (int r = 0; r < 16; ++r) { aco[r] = 0.f; acs[r] = 0.f; cs[r] = 0.f; }
#define K1_DGOUT
  const int mtg = mb * 4 + mt;
  auto ct_of = [&](int n) { return nb * 3 + (W128 && (nb & 1) ? (n == 2 ? 0 : n + 1) : n); };
  auto so_of = [&](int n, int hh, bool isv) {
    const int ct = ct_of(n), which = ct >> 4, hp = (ct & 15) * 2;
    return isv ? (unsigned)WS_V + (unsigned)(hp * 65536 + mtg * 1024 + hh * 512) : (unsigned)(which == 0 ? WS_Q : WS_K) + (unsigned)((hp + hh) * 65536 + mtg * 1024);
  };
  auto sc_of = [&](int n) { return __float_as_uint((ct_of(n) >> 4) == 0 ? 0.25f * 1.4426950408889634f : 1.f); };
  const srd_t ea = {rfl(lds0 + w * 2048), rfl(lds0 + (w ^ 4) * 2048), rfl(lds0 + 16384 + w * 512), rfl(lds0)};
  const srd_t eb = {rfl((unsigned)mt), rfl(so_of(g, 0, (VMASK & 1) != 0)), rfl(so_of(g, 1, (VMASK & 1) != 0)), rfl(so_of(2, g, (VMASK & 4) != 0))};
  const srd_t ec = {rfl(sc_of(g)), rfl(sc_of(2)), 0x3b000000u, rfl((unsigned)w)};
  u32x4 ml2 = ml, mh2 = mh;
#define K1_OPS \
               : "+{v[0:15]}"(aco), "+{v[16:31]}"(acs), "+{v[32:47]}"(cs), "+{v[96:99]}"(sum4), "+{v[100:103]}"(sq4), \
                 "+{v[112:115]}"(xa), "+{v[116:119]}"(wa), "+{v[120:121]}"(wsa), "+{v[88:91]}"(ml2), "+{v[92:95]}"(mh2), \
                 "=&{v[80:83]}"(gc.k3), "=&{v[84:87]}"(gc.k1) K1_DGOUT \
               : "{s40}"(g), "{s[60:63]}"(rsw), \
                 "{s42}"(rfl(w == 7 ? 1u : 0u)), "{s43}"((unsigned)(WS_CNT + CN_FLAG * 256)), "{s[44:47]}"(ea), "{s[48:51]}"(eb), "{s[52:55]}"(ec) \
               : "v48", "v49", "v50", "v51", "v52", "v53", "v54", "v55", "v56", "v57", "v58", "v59", "v60", "v61", "v62", "v63", \
                 "v64", "v65", "v66", "v67", "v68", "v69", "v70", "v71", "v72", "v73", "v74", "v75", "v76", "v77", "v78", "v79", \
                 "v104", "v105", "v106", "v107", "v108", "v109", "v110", "v111", "v122", "v123", "v124", "v125", "v126", "v127", "v128", "v129", \
                 "v132", "v133", "s70", "s72", "s73", "s74", "s75", "s76", "s77", "scc", "memory"
  if (VMASK == 0) asm volatile(K1_ASM_0 K1_OPS);
  else if (VMASK == 4) asm volatile(K1_ASM_4 K1_OPS);
  else asm volatile(K1_ASM_7 K1_OPS);
  gc.q = __builtin_bit_cast(f32x4, ml2); gc.mean = __builtin_bit_cast(f32x4, mh2);
  }
  (void)aco; (void)acs; (void)cs;
  return;
  const int lane = lane_fresh();
  const int h = lane >> 5, blk = (lane >> 4) & 1;
  K1CY(0);
  { const int dg_w = w; (void)dg_w; TS(1); }
  float* ex = (float*)lds_;
  float* exs = ex + 8 * 512;
  float* exc = exs + 8 * 128;
  float* tab = exc + 384 + w * 64;
  {
    f32x4* dst = (f32x4*)(ex + w * 512);
    dst[lane] = g ? f32x4{acs[0], acs[1], acs[2], acs[3]} : f32x4{acs[8], acs[9], acs[10], acs[11]};
    dst[64 + lane] = g ? f32x4{acs[4], acs[5], acs[6], acs[7]} : f32x4{acs[12], acs[13], acs[14], acs[15]};
    float* ds = exs + w * 128;
    ds[lane] = (sum4[0] + sum4[1]) + (sum4[2] + sum4[3]); ds[64 + lane] = (sq4[0] + sq4[1]) + (sq4[2] + sq4[3]);
    if ((lane & 15) == 0 && (blk == 0 || g == 0)) {
      float* c = exc + ((blk ? 2 : g) * 4 + mt) * 32 + 4 * h;
#pragma unroll
      for (int q = 0; q < 4; ++q) *(f32x4*)(c + 8 * q) = f32x4{cs[4 * q], cs[4 * q + 1], cs[4 * q + 2], cs[4 * q + 3]};
    }
  }
  K1CY(1);
  bar_only();
  K1CY(2);
  { const int dg_w = w; (void)dg_w; TS(2); }
  { const int dg_w = w; (void)dg_w; TS(3); }
  const int wo = w ^ 4;
  float hs[8];
  {
    const f32x4* src = (const f32x4*)(ex + wo * 512);
    const f32x4 p0 = src[lane], p1 = src[64 + lane];
#pragma unroll
    for (int e = 0; e < 4; ++e) { hs[e] = (g ? acs[8 + e] : acs[e]) + p0[e]; hs[4 + e] = (g ? acs[12 + e] : acs[4 + e]) + p1[e]; }
  }
  float ra, rb;
  {
    float a4[4], m4[4], q4_[4];
#pragma unroll
    for (int p = 0; p < 2; ++p) {
      const float* d0 = exs + (2 * p + h) * 128 + (lane & 31);
      const float* d1 = d0 + 4 * 128;
      const float sum = (d0[0] + d0[32]) + (d1[0] + d1[32]), sq = (d0[64] + d0[96]) + (d1[64] + d1[96]);
      const float mean = sum * (1.f / D);
      const float ve = fmaxf(sq * (1.f / D) - mean * mean, 0.f) + 1e-5f;
      const float a = __builtin_amdgcn_rsqf(ve);
      const u32x2 sa = __builtin_amdgcn_permlane32_swap(__float_as_uint(a), __float_as_uint(a), false, false);
      const u32x2 sm = __builtin_amdgcn_permlane32_swap(__float_as_uint(mean), __float_as_uint(mean), false, false);
      const u32x2 sv = __builtin_amdgcn_permlane32_swap(__float_as_uint(ve), __float_as_uint(ve), false, false);
      a4[2 * p] = __uint_as_float(sa[0]); a4[2 * p + 1] = __uint_as_float(sa[1]);
      m4[2 * p] = __uint_as_float(sm[0]); m4[2 * p + 1] = __uint_as_float(sm[1]);
      q4_[2 * p] = __uint_as_float(sv[0]); q4_[2 * p + 1] = __uint_as_float(sv[1]);
    }
#pragma unroll
    for (int m = 0; m < 4; ++m) {
      gc.k3[m] = -0.1029432397f * a4[m] * a4[m] * a4[m]; gc.k1[m] = -2.3022081985f * a4[m]; gc.q[m] = q4_[m]; gc.mean[m] = m4[m];
    }
    ra = mt == 0 ? a4[0] : mt == 1 ? a4[1] : mt == 2 ? a4[2] : a4[3];
    rb = ra * (mt == 0 ? m4[0] : mt == 1 ? m4[1] : mt == 2 ? m4[2] : m4[3]);
  }
  if (VMASK != 0 && h == 0) { tab[lane] = ra; tab[32 + lane] = rb; }
  const int mtg = mb * 4 + mt;
  K1CY(3);
  auto half_out = [&](const float* a8, int n, int hh, bool isv) {
    const int ct = nb * 3 + ((nb & 1) ? (n == 2 ? 0 : n + 1) : n);
    const int which = ct >> 4, hp = (ct & 15) * 2;
    float y[8];
    const float* c = exc + n * 128;
    if (!isv) {
      const float sc = (which == 0) ? 0.25f * 1.4426950408889634f : 1.f;
      const float a2 = ra * sc, b2 = rb * sc;
#pragma unroll
      for (int q = 0; q < 2; ++q) {
        const float* cc = c + 8 * (2 * hh + q) + 4 * h;
        const f32x4 c4 = (*(const f32x4*)cc + *(const f32x4*)(cc + 32)) + (*(const f32x4*)(cc + 64) + *(const f32x4*)(cc + 96));
#pragma unroll
        for (int e = 0; e < 4; ++e) y[4 * q + e] = a8[4 * q + e] * a2 - b2 * c4[e];
      }
      const u32x4 o = {cvtpk(y[0], y[1]), cvtpk(y[2], y[3]), cvtpk(y[4], y[5]), cvtpk(y[6], y[7])};
      st16q(rws, (unsigned)(which == 0 ? WS_Q : WS_K) + (unsigned)((((hp + hh) * 64 + mtg) * 64 + lane) * 16), o);
    } else {
      const int hd = hp + blk, d = lane & 15;
      const float* cc = c + (lane & 31);
      const float csl = (cc[0] + cc[32]) + (cc[64] + cc[96]);
#pragma unroll
      for (int q = 0; q < 2; ++q) {
        const f32x4 a4 = *(const f32x4*)(tab + 8 * (2 * hh + q) + 4 * h);
        const f32x4 b4 = *(const f32x4*)(tab + 32 + 8 * (2 * hh + q) + 4 * h);
#pragma unroll
        for (int e = 0; e < 4; ++e) y[4 * q + e] = a8[4 * q + e] * a4[e] - b4[e] * csl;
      }
      const u32x4 o = {cvtpk(y[0], y[1]), cvtpk(y[2], y[3]), cvtpk(y[4], y[5]), cvtpk(y[6], y[7])};
      st16q(rws, (unsigned)WS_V + (unsigned)(((((hd * 64 + mtg) * 2 + hh) * 2 + h) * 16 + d) * 16), o);
    }
  };
  {
    float a8[8];
#pragma unroll
    for (int e = 0; e < 8; ++e) a8[e] = aco[e];
    half_out(a8, g, 0, (VMASK & 1) != 0);
#pragma unroll
    for (int e = 0; e < 8; ++e) a8[e] = aco[8 + e];
    half_out(a8, g, 1, (VMASK & 1) != 0);
    half_out(hs, 2, g, (VMASK & 4) != 0);
  }
  K1CY(4);
}
constexpr int L_GA = 114688, L_GB = 65536, L_GC = 16384;
DI void dma16x4s(__amdgpu_buffer_rsrc_t rsrc, unsigned voff, unsigned so, unsigned lds_addr) {
  asm volatile("s_mov_b32 m0, %0\n\ts_nop 0\n\t"
               "buffer_load_dwordx4 %1, %2, %3 offen sc1 lds\n\t"
               "buffer_load_dwordx4 %1, %2, %3 offen offset:1024 sc1 lds\n\t"
               "buffer_load_dwordx4 %1, %2, %3 offen offset:2048 sc1 lds\n\t"
               "buffer_load_dwordx4 %1, %2, %3 offen offset:3072 sc1 lds"
               :: "s"(lds_addr), "v"(voff), "s"(rsrc), "s"(so) : "memory");
}
struct GLd {
  __amdgpu_buffer_rsrc_t rx, rw;
  unsigned vx0, vx1, vg, sx0, sg0, dx, dw;
  int rot;
};
DI GLd gld_init(char* lds_, const u16* __restrict__ x, const WPtrs& P, int mbg, int nbg, int hw, int rot) {
  GLd G;
  const int lane = lane_fresh();
  const int up = hw & 1;
  G.rx = mk_rsrc_x(x, S * D * 2);
  const unsigned long long ag = (unsigned long long)P.wg, au = (unsigned long long)P.wu;
  const unsigned long long aw = up ? au : ag;
  G.rw = mk_rsrc_x((const void*)(((unsigned long long)rfl((unsigned)(aw >> 32)) << 32) | rfl((unsigned)aw)), D * FF * 2);
  G.vx0 = (lane >> 3) * 1024 + (((lane & 7) ^ ((lane >> 4) & 7)) * 16);
  G.vx1 = (lane >> 3) * 1024 + (((lane & 7) ^ ((4 + (lane >> 4)) & 7)) * 16);
  const int drow = lane >> 4, pos = (lane >> 1) & 7, half = lane & 1;
  G.vg = drow * (FF * 2) + (pos ^ (2 * drow + up)) * 32 + half * 16;
  G.sx0 = rfl((unsigned)((mbg * 128 + hw * 32) * 1024));
  G.sg0 = rfl((unsigned)((hw >> 1) * 16 * (FF * 2) + nbg * 256));
  G.dx = rfl((unsigned)(uintptr_t)lds_ + hw * 4096);
  G.dw = rfl((unsigned)(uintptr_t)lds_ + 16384 + (hw >> 1) * 8192 + up * 4096);
  G.rot = rot;
  snop4();
  return G;
}
DI void issue_gx(const GLd& G, int st, int slot) {
  const unsigned sx = G.sx0 + ((G.rot + st) & 7) * 128;
  dma16x4e(G.rx, G.vx0, G.vx1, sx, sx + 8192, sx + 2 * 8192, sx + 3 * 8192, G.dx + slot);
}
DI void issue_gw(const GLd& G, int st, int half_, int slot) {
  const unsigned sg = G.sg0 + (unsigned)((((G.rot + st) & 7) * 4 + 2 * half_) * 16 * (FF * 2));
  dma16x4(G.rw, G.vg, sg, sg + 4 * FF * 2, sg + 8 * FF * 2, sg + 12 * FF * 2, G.dw + slot + half_ * 16384);
}
DI void issue_gw2(const GLd& G, int st, int q, int slot) {
  const unsigned sg = G.sg0 + (unsigned)((((G.rot + st) & 7) * 4 + 2) * 16 * (FF * 2)) + q * 8 * FF * 2;
  dma16x2(G.rw, G.vg, sg, sg + 4 * FF * 2, G.dw + slot + 16384 + q * 2048);
}
DI void issue_g(const GLd& G, int st, int slot) { issue_gx(G, st, slot); issue_gw(G, st, 0, slot); issue_gw(G, st, 1, slot); }
DI void k1_loader(char* lds_, const u16* __restrict__ x, const u16* __restrict__ W, const WPtrs& P, char* __restrict__ ws, int mb, int nb, bool has_g1, int mbg, int nbg, int w, CSV& cs, int wgi) {
  const int lane = lane_fresh();
  const unsigned lds0 = (unsigned)(uintptr_t)lds_;
  const int rot = ROT_OF(K1_ROTK);
  const int id = w * 256 + blockIdx.x;
  const unsigned pto = K1_PT + w * 8192;
  auto post4 = [&]() { const GLd G = gld_init(lds_, x, P, mbg, nbg, w, ROT_OF(G1_ROTK)); issue_g(G, 0, L_GA); };
  if (w < 2) {
    const __amdgpu_buffer_rsrc_t rx = mk_rsrc_x(x, S * D * 2);
    const unsigned vx0 = (lane >> 3) * 1024 + (((lane & 7) ^ ((lane >> 4) & 7)) * 16);
    const unsigned vx1 = (lane >> 3) * 1024 + (((lane & 7) ^ ((4 + (lane >> 4)) & 7)) * 16);
    const int i0 = w * 8;
    unsigned slot = lds0 + i0 * 1024, kc = rot;
    const unsigned slot_end = slot + K1_NS * K1_STG, sb = (unsigned)(mb * 128 * 1024 + i0 * 8192);
    auto issue = [&]() {
      const unsigned so = sb + kc * 128;
      dma16x4e(rx, vx0, vx1, so, so + 8192, so + 2 * 8192, so + 3 * 8192, slot);
      dma16x4e(rx, vx0, vx1, so + 4 * 8192, so + 5 * 8192, so + 6 * 8192, so + 7 * 8192, slot + 4096);
      kc = (kc + 1) & 7; slot += K1_STG;
      if (slot == slot_end) slot -= K1_NS * K1_STG;
    };
    k1_loader_loop<8>(issue, post4, has_g1, P, id, lane, lds0 + pto, ws, cs, wgi);
  } else {
    const __amdgpu_buffer_rsrc_t rw = mk_rsrc_x(W, D * 1536 * 2);
    const unsigned vw = (lane >> 2) * 3072 + (lane & 3) * 16;
    const unsigned vp = (lane >> 3) * 3072 + (((lane & 7) ^ (((lane >> 4) & 1) << 2)) * 16);
    const int u = w - 2;
    unsigned slot = lds0 + 16384 + u * 4096, kc = rot;
    const unsigned slot_end = slot + K1_NS * K1_STG;
    const unsigned pp = (unsigned)(nb * 192 + ((nb & 1) ? 64 : 0) + u * 32 * 3072), ps = (unsigned)(nb * 192 + ((nb & 1) ? 0 : 128) + u * 32 * 3072);
    auto issue = [&]() {
      const unsigned so = kc * (64 * 3072);
      dma16x4(rw, vp, so + pp, so + pp + 8 * 3072, so + pp + 16 * 3072, so + pp + 24 * 3072, slot);
      dma16x2(rw, vw, so + ps, so + ps + 16 * 3072, slot + 8192 - u * 2048);
      kc = (kc + 1) & 7; slot += K1_STG;
      if (slot == slot_end) slot -= K1_NS * K1_STG;
    };
    k1_loader_loop<6>(issue, post4, has_g1, P, id, lane, lds0 + pto, ws, cs, wgi);
  }
  bar_only();
  { const int dg_w = w + 8; (void)dg_w; TS(2); }
}

typedef __attribute__((ext_vector_type(4))) unsigned srd_t;
typedef __attribute__((ext_vector_type(16))) unsigned u32x16;
typedef __attribute__((address_space(1))) unsigned long long gu64;
constexpr float ATT_BIAS = 96.f;
constexpr float ATT_LIM = 1.329228e36f;
constexpr int L_CONST = 131072, L_Q = 139264, L_EX = 147456, L_TOTAL = 163840;
constexpr int L_RT = 65536;
constexpr int L_PART = 0;
constexpr int L_PROG = L_CONST + 128;
#define G1_ASM \
    "s_mov_b32 s72, 0\n\t" \
    "s_mov_b32 s73, 0\n\t" \
    "v_mov_b32 v109, 0\n\t" \
    "v_mov_b32 v110, 0\n\t" \
    "v_mov_b32 v64, 0\n\t" \
    "v_mov_b32 v65, 0\n\t" \
    "v_mov_b32 v66, 0\n\t" \
    "v_mov_b32 v67, 0\n\t" \
    "s_nop 1\n\t" \
    "v_mfma_f32_32x32x16_bf16 v[0:15], v[64:67], v[64:67], 0\n\t" \
    "v_mfma_f32_32x32x16_bf16 v[16:31], v[64:67], v[64:67], 0\n\t" \
    "v_mfma_f32_32x32x16_bf16 v[32:47], v[64:67], v[64:67], 0\n\t" \
    "v_mfma_f32_32x32x16_bf16 v[48:63], v[64:67], v[64:67], 0\n\t" \
    "s_barrier\n\t" \
    "ds_read_b64_tr_b16 v[96:97], v108 offset:0\n\t" \
    "ds_read_b64_tr_b16 v[98:99], v108 offset:1024\n\t" \
    "ds_read_b128 v[64:67], v104 offset:0\n\t" \
    "ds_read_b128 v[68:71], v104 offset:4096\n\t" \
    "ds_read_b128 v[72:75], v104 offset:8192\n\t" \
    "ds_read_b128 v[76:79], v104 offset:12288\n\t" \
    ".p2align 6\n\t" \
    "L_g1_%=:\n\t" \
    "s_barrier\n\t" \
    "s_waitcnt lgkmcnt(3)\n\t" \
    "v_mfma_f32_32x32x16_bf16 v[0:15], v[96:99], v[64:67], v[0:15]\n\t" \
    "ds_read_b128 v[64:67], v105 offset:0\n\t" \
    "s_add_u32 s73, s73, 1\n\t" \
    "s_mov_b32 s74, 0xffff4000\n\t" \
    "s_cmp_eq_u32 s73, 3\n\t" \
    "s_cselect_b32 s74, 0x18000, s74\n\t" \
    "s_cselect_b32 s73, 0, s73\n\t" \
    "v_add_u32 v104, s74, v104\n\t" \
    "s_cmp_eq_u32 s72, 2\n\t" \
    "s_cbranch_scc0 L_nq_%=\n\t" \
    "s_bitcmp1_b32 s54, 0\n\t" \
    "s_cbranch_scc0 L_nq_%=\n\t" \
    "v_mov_b32 v130, 1\n\t" \
    "s_mov_b64 exec, 1\n\t" \
    "buffer_atomic_add v130, off, s[44:47], s53\n\t" \
    "s_mov_b64 exec, -1\n\t" \
    "v_mov_b32 v130, 1\n\t" \
    "s_mov_b64 exec, 1\n\t" \
    "buffer_atomic_add v130, off, s[44:47], s52\n\t" \
    "s_mov_b64 exec, -1\n\t" \
    "L_nq_%=:\n\t" \
    "ds_read_b64_tr_b16 v[100:101], v108 offset:8192\n\t" \
    "ds_read_b64_tr_b16 v[102:103], v108 offset:9216\n\t" \
    "v_lshlrev_b32 v129, 16, v96\n\t" \
    "v_and_b32 v130, 0xffff0000, v96\n\t" \
    "v_add_f32 v109, v109, v129\n\t" \
    "v_add_f32 v110, v110, v130\n\t" \
    "s_waitcnt lgkmcnt(5)\n\t" \
    "v_mfma_f32_32x32x16_bf16 v[16:31], v[96:99], v[68:71], v[16:31]\n\t" \
    "ds_read_b128 v[68:71], v105 offset:4096\n\t" \
    "v_lshlrev_b32 v129, 16, v97\n\t" \
    "v_and_b32 v130, 0xffff0000, v97\n\t" \
    "v_add_f32 v109, v109, v129\n\t" \
    "v_add_f32 v110, v110, v130\n\t" \
    "s_waitcnt lgkmcnt(5)\n\t" \
    "v_mfma_f32_32x32x16_bf16 v[32:47], v[96:99], v[72:75], v[32:47]\n\t" \
    "ds_read_b128 v[72:75], v105 offset:8192\n\t" \
    "v_lshlrev_b32 v129, 16, v98\n\t" \
    "v_and_b32 v130, 0xffff0000, v98\n\t" \
    "v_add_f32 v109, v109, v129\n\t" \
    "v_add_f32 v110, v110, v130\n\t" \
    "s_waitcnt lgkmcnt(5)\n\t" \
    "v_mfma_f32_32x32x16_bf16 v[48:63], v[96:99], v[76:79], v[48:63]\n\t" \
    "ds_read_b128 v[76:79], v105 offset:12288\n\t" \
    "v_lshlrev_b32 v129, 16, v99\n\t" \
    "v_and_b32 v130, 0xffff0000, v99\n\t" \
    "v_add_f32 v109, v109, v129\n\t" \
    "v_add_f32 v110, v110, v130\n\t" \
    "v_add_u32 v105, s74, v105\n\t" \
    "s_waitcnt lgkmcnt(3)\n\t" \
    "v_mfma_f32_32x32x16_bf16 v[0:15], v[100:103], v[64:67], v[0:15]\n\t" \
    "ds_read_b128 v[64:67], v106 offset:0\n\t" \
    "ds_read_b64_tr_b16 v[96:97], v108 offset:16384\n\t" \
    "ds_read_b64_tr_b16 v[98:99], v108 offset:17408\n\t" \
    "v_lshlrev_b32 v129, 16, v100\n\t" \
    "v_and_b32 v130, 0xffff0000, v100\n\t" \
    "v_add_f32 v109, v109, v129\n\t" \
    "v_add_f32 v110, v110, v130\n\t" \
    "s_waitcnt lgkmcnt(5)\n\t" \
    "v_mfma_f32_32x32x16_bf16 v[16:31], v[100:103], v[68:71], v[16:31]\n\t" \
    "ds_read_b128 v[68:71], v106 offset:4096\n\t" \
    "v_lshlrev_b32 v129, 16, v101\n\t" \
    "v_and_b32 v130, 0xffff0000, v101\n\t" \
    "v_add_f32 v109, v109, v129\n\t" \
    "v_add_f32 v110, v110, v130\n\t" \
    "s_waitcnt lgkmcnt(5)\n\t" \
    "v_mfma_f32_32x32x16_bf16 v[32:47], v[100:103], v[72:75], v[32:47]\n\t" \
    "ds_read_b128 v[72:75], v106 offset:8192\n\t" \
    "v_lshlrev_b32 v129, 16, v102\n\t" \
    "v_and_b32 v130, 0xffff0000, v102\n\t" \
    "v_add_f32 v109, v109, v129\n\t" \
    "v_add_f32 v110, v110, v130\n\t" \
    "s_waitcnt lgkmcnt(5)\n\t" \
    "v_mfma_f32_32x32x16_bf16 v[48:63], v[100:103], v[76:79], v[48:63]\n\t" \
    "ds_read_b128 v[76:79], v106 offset:12288\n\t" \
    "v_lshlrev_b32 v129, 16, v103\n\t" \
    "v_and_b32 v130, 0xffff0000, v103\n\t" \
    "v_add_f32 v109, v109, v129\n\t" \
    "v_add_f32 v110, v110, v130\n\t" \
    "v_add_u32 v106, s74, v106\n\t" \
    "s_barrier\n\t" \
    "s_waitcnt lgkmcnt(3)\n\t" \
    "v_mfma_f32_32x32x16_bf16 v[0:15], v[96:99], v[64:67], v[0:15]\n\t" \
    "ds_read_b128 v[64:67], v107 offset:0\n\t" \
    "ds_read_b64_tr_b16 v[100:101], v108 offset:24576\n\t" \
    "ds_read_b64_tr_b16 v[102:103], v108 offset:25600\n\t" \
    "v_lshlrev_b32 v129, 16, v96\n\t" \
    "v_and_b32 v130, 0xffff0000, v96\n\t" \
    "v_add_f32 v109, v109, v129\n\t" \
    "v_add_f32 v110, v110, v130\n\t" \
    "s_waitcnt lgkmcnt(5)\n\t" \
    "v_mfma_f32_32x32x16_bf16 v[16:31], v[96:99], v[68:71], v[16:31]\n\t" \
    "ds_read_b128 v[68:71], v107 offset:4096\n\t" \
    "v_lshlrev_b32 v129, 16, v97\n\t" \
    "v_and_b32 v130, 0xffff0000, v97\n\t" \
    "v_add_f32 v109, v109, v129\n\t" \
    "v_add_f32 v110, v110, v130\n\t" \
    "s_waitcnt lgkmcnt(5)\n\t" \
    "v_mfma_f32_32x32x16_bf16 v[32:47], v[96:99], v[72:75], v[32:47]\n\t" \
    "ds_read_b128 v[72:75], v107 offset:8192\n\t" \
    "v_lshlrev_b32 v129, 16, v98\n\t" \
    "v_and_b32 v130, 0xffff0000, v98\n\t" \
    "v_add_f32 v109, v109, v129\n\t" \
    "v_add_f32 v110, v110, v130\n\t" \
    "s_waitcnt lgkmcnt(5)\n\t" \
    "v_mfma_f32_32x32x16_bf16 v[48:63], v[96:99], v[76:79], v[48:63]\n\t" \
    "ds_read_b128 v[76:79], v107 offset:12288\n\t" \
    "v_lshlrev_b32 v129, 16, v99\n\t" \
    "v_and_b32 v130, 0xffff0000, v99\n\t" \
    "v_add_f32 v109, v109, v129\n\t" \
    "v_add_f32 v110, v110, v130\n\t" \
    "v_add_u32 v107, s74, v107\n\t" \
    "s_waitcnt lgkmcnt(3)\n\t" \
    "v_mfma_f32_32x32x16_bf16 v[0:15], v[100:103], v[64:67], v[0:15]\n\t" \
    "ds_read_b128 v[64:67], v104 offset:0\n\t" \
    "v_add_u32 v108, s74, v108\n\t" \
    "ds_read_b64_tr_b16 v[96:97], v108 offset:0\n\t" \
    "ds_read_b64_tr_b16 v[98:99], v108 offset:1024\n\t" \
    "v_lshlrev_b32 v129, 16, v100\n\t" \
    "v_and_b32 v130, 0xffff0000, v100\n\t" \
    "v_add_f32 v109, v109, v129\n\t" \
    "v_add_f32 v110, v110, v130\n\t" \
    "s_waitcnt lgkmcnt(5)\n\t" \
    "v_mfma_f32_32x32x16_bf16 v[16:31], v[100:103], v[68:71], v[16:31]\n\t" \
    "ds_read_b128 v[68:71], v104 offset:4096\n\t" \
    "v_lshlrev_b32 v129, 16, v101\n\t" \
    "v_and_b32 v130, 0xffff0000, v101\n\t" \
    "v_add_f32 v109, v109, v129\n\t" \
    "v_add_f32 v110, v110, v130\n\t" \
    "s_waitcnt lgkmcnt(5)\n\t" \
    "v_mfma_f32_32x32x16_bf16 v[32:47], v[100:103], v[72:75], v[32:47]\n\t" \
    "ds_read_b128 v[72:75], v104 offset:8192\n\t" \
    "v_lshlrev_b32 v129, 16, v102\n\t" \
    "v_and_b32 v130, 0xffff0000, v102\n\t" \
    "v_add_f32 v109, v109, v129\n\t" \
    "v_add_f32 v110, v110, v130\n\t" \
    "s_waitcnt lgkmcnt(5)\n\t" \
    "v_mfma_f32_32x32x16_bf16 v[48:63], v[100:103], v[76:79], v[48:63]\n\t" \
    "ds_read_b128 v[76:79], v104 offset:12288\n\t" \
    "v_lshlrev_b32 v129, 16, v103\n\t" \
    "v_and_b32 v130, 0xffff0000, v103\n\t" \
    "v_add_f32 v109, v109, v129\n\t" \
    "v_add_f32 v110, v110, v130\n\t" \
    "s_cmp_eq_u32 s72, 1\n\t" \
    "s_cbranch_scc0 L_nw_%=\n\t" \
    "s_waitcnt vmcnt(0)\n\t" \
    "L_nw_%=:\n\t" \
    "s_add_u32 s72, s72, 1\n\t" \
    "s_cmp_lt_u32 s72, 8\n\t" \
    "s_cbranch_scc1 L_g1_%=\n\t" \
    "s_waitcnt lgkmcnt(0)\n\t" \
    "s_barrier\n\t" \
    "v_add_f32 v109, v109, v110\n\t" \
    "v_mov_b32 v110, v109\n\t" \
    "s_nop 1\n\t" \
    "v_permlane32_swap_b32 v109, v110\n\t" \
    "s_nop 1\n\t" \
    "v_add_f32 v109, v109, v110\n\t" \
    "v_cvt_pk_bf16_f32 v96, v109, v109\n\t" \
    "v_lshlrev_b32 v110, 16, v96\n\t" \
    "v_sub_f32 v110, v109, v110\n\t" \
    "v_cvt_pk_bf16_f32 v97, v110, 0\n\t" \
    "v_mov_b32 v98, 0\n\t" \
    "v_mov_b32 v99, 0\n\t" \
    "s_mov_b32 exec_lo, 0\n\t" \
    "v_mov_b32 v96, 0\n\t" \
    "v_mov_b32 v97, 0\n\t" \
    "s_mov_b32 exec_lo, -1\n\t" \
    "v_cvt_pk_bf16_f32 v65, -v92, 0\n\t" \
    "v_lshlrev_b32 v66, 16, v65\n\t" \
    "v_sub_f32 v66, -v92, v66\n\t" \
    "v_cvt_pk_bf16_f32 v64, -v92, v66\n\t" \
    "v_mov_b32 v66, 0\n\t" \
    "v_mov_b32 v67, 0\n\t" \
    "v_cvt_pk_bf16_f32 v69, -v93, 0\n\t" \
    "v_lshlrev_b32 v70, 16, v69\n\t" \
    "v_sub_f32 v70, -v93, v70\n\t" \
    "v_cvt_pk_bf16_f32 v68, -v93, v70\n\t" \
    "v_mov_b32 v70, 0\n\t" \
    "v_mov_b32 v71, 0\n\t" \
    "v_cvt_pk_bf16_f32 v73, -v94, 0\n\t" \
    "v_lshlrev_b32 v74, 16, v73\n\t" \
    "v_sub_f32 v74, -v94, v74\n\t" \
    "v_cvt_pk_bf16_f32 v72, -v94, v74\n\t" \
    "v_mov_b32 v74, 0\n\t" \
    "v_mov_b32 v75, 0\n\t" \
    "v_cvt_pk_bf16_f32 v77, -v95, 0\n\t" \
    "v_lshlrev_b32 v78, 16, v77\n\t" \
    "v_sub_f32 v78, -v95, v78\n\t" \
    "v_cvt_pk_bf16_f32 v76, -v95, v78\n\t" \
    "v_mov_b32 v78, 0\n\t" \
    "v_mov_b32 v79, 0\n\t" \
    "s_nop 1\n\t" \
    "v_mfma_f32_32x32x16_bf16 v[0:15], v[96:99], v[64:67], v[0:15]\n\t" \
    "v_mfma_f32_32x32x16_bf16 v[16:31], v[96:99], v[68:71], v[16:31]\n\t" \
    "v_mfma_f32_32x32x16_bf16 v[32:47], v[96:99], v[72:75], v[32:47]\n\t" \
    "v_mfma_f32_32x32x16_bf16 v[48:63], v[96:99], v[76:79], v[48:63]\n\t" \
    "s_nop 15\n\t" \
    "v_pk_mul_f32 v[112:113], v[0:1], v[0:1]\n\t" \
    "v_pk_mul_f32 v[114:115], v[2:3], v[2:3]\n\t" \
    "v_pk_mul_f32 v[116:117], v[4:5], v[4:5]\n\t" \
    "v_pk_mul_f32 v[118:119], v[6:7], v[6:7]\n\t" \
    "v_pk_fma_f32 v[112:113], v[112:113], v[80:81], v[84:85] op_sel:[0,0,0] op_sel_hi:[1,0,0]\n\t" \
    "v_pk_fma_f32 v[114:115], v[114:115], v[80:81], v[84:85] op_sel:[0,0,0] op_sel_hi:[1,0,0]\n\t" \
    "v_pk_fma_f32 v[116:117], v[116:117], v[80:81], v[84:85] op_sel:[0,0,0] op_sel_hi:[1,0,0]\n\t" \
    "v_pk_fma_f32 v[118:119], v[118:119], v[80:81], v[84:85] op_sel:[0,0,0] op_sel_hi:[1,0,0]\n\t" \
    "v_pk_mul_f32 v[112:113], v[112:113], v[0:1]\n\t" \
    "v_pk_mul_f32 v[114:115], v[114:115], v[2:3]\n\t" \
    "v_pk_mul_f32 v[116:117], v[116:117], v[4:5]\n\t" \
    "v_pk_mul_f32 v[118:119], v[118:119], v[6:7]\n\t" \
    "v_exp_f32 v112, v112\n\t" \
    "v_exp_f32 v113, v113\n\t" \
    "v_exp_f32 v114, v114\n\t" \
    "v_exp_f32 v115, v115\n\t" \
    "v_exp_f32 v116, v116\n\t" \
    "v_exp_f32 v117, v117\n\t" \
    "v_exp_f32 v118, v118\n\t" \
    "v_exp_f32 v119, v119\n\t" \
    "v_pk_mul_f32 v[8:9], v[8:9], v[0:1]\n\t" \
    "v_pk_mul_f32 v[10:11], v[10:11], v[2:3]\n\t" \
    "v_pk_mul_f32 v[12:13], v[12:13], v[4:5]\n\t" \
    "v_pk_mul_f32 v[14:15], v[14:15], v[6:7]\n\t" \
    "v_pk_fma_f32 v[112:113], v[112:113], v[88:89], v[88:89] op_sel:[0,0,0] op_sel_hi:[1,0,0]\n\t" \
    "v_pk_fma_f32 v[114:115], v[114:115], v[88:89], v[88:89] op_sel:[0,0,0] op_sel_hi:[1,0,0]\n\t" \
    "v_pk_fma_f32 v[116:117], v[116:117], v[88:89], v[88:89] op_sel:[0,0,0] op_sel_hi:[1,0,0]\n\t" \
    "v_pk_fma_f32 v[118:119], v[118:119], v[88:89], v[88:89] op_sel:[0,0,0] op_sel_hi:[1,0,0]\n\t" \
    "v_rcp_f32 v112, v112\n\t" \
    "v_rcp_f32 v113, v113\n\t" \
    "v_rcp_f32 v114, v114\n\t" \
    "v_rcp_f32 v115, v115\n\t" \
    "v_rcp_f32 v116, v116\n\t" \
    "v_rcp_f32 v117, v117\n\t" \
    "v_rcp_f32 v118, v118\n\t" \
    "v_rcp_f32 v119, v119\n\t" \
    "s_nop 0\n\t" \
    "v_pk_mul_f32 v[112:113], v[112:113], v[8:9]\n\t" \
    "v_pk_mul_f32 v[114:115], v[114:115], v[10:11]\n\t" \
    "v_pk_mul_f32 v[116:117], v[116:117], v[12:13]\n\t" \
    "v_pk_mul_f32 v[118:119], v[118:119], v[14:15]\n\t" \
    "v_cvt_pk_bf16_f32 v120, v112, v113\n\t" \
    "v_cvt_pk_bf16_f32 v121, v114, v115\n\t" \
    "v_cvt_pk_bf16_f32 v122, v116, v117\n\t" \
    "v_cvt_pk_bf16_f32 v123, v118, v119\n\t" \
    "s_add_u32 s68, s55, 0\n\t" \
    "buffer_store_dwordx4 v[120:123], v128, s[44:47], s68 offen sc1\n\t" \
    "v_pk_mul_f32 v[112:113], v[16:17], v[16:17]\n\t" \
    "v_pk_mul_f32 v[114:115], v[18:19], v[18:19]\n\t" \
    "v_pk_mul_f32 v[116:117], v[20:21], v[20:21]\n\t" \
    "v_pk_mul_f32 v[118:119], v[22:23], v[22:23]\n\t" \
    "v_pk_fma_f32 v[112:113], v[112:113], v[80:81], v[84:85] op_sel:[0,1,1] op_sel_hi:[1,1,1]\n\t" \
    "v_pk_fma_f32 v[114:115], v[114:115], v[80:81], v[84:85] op_sel:[0,1,1] op_sel_hi:[1,1,1]\n\t" \
    "v_pk_fma_f32 v[116:117], v[116:117], v[80:81], v[84:85] op_sel:[0,1,1] op_sel_hi:[1,1,1]\n\t" \
    "v_pk_fma_f32 v[118:119], v[118:119], v[80:81], v[84:85] op_sel:[0,1,1] op_sel_hi:[1,1,1]\n\t" \
    "v_pk_mul_f32 v[112:113], v[112:113], v[16:17]\n\t" \
    "v_pk_mul_f32 v[114:115], v[114:115], v[18:19]\n\t" \
    "v_pk_mul_f32 v[116:117], v[116:117], v[20:21]\n\t" \
    "v_pk_mul_f32 v[118:119], v[118:119], v[22:23]\n\t" \
    "v_exp_f32 v112, v112\n\t" \
    "v_exp_f32 v113, v113\n\t" \
    "v_exp_f32 v114, v114\n\t" \
    "v_exp_f32 v115, v115\n\t" \
    "v_exp_f32 v116, v116\n\t" \
    "v_exp_f32 v117, v117\n\t" \
    "v_exp_f32 v118, v118\n\t" \
    "v_exp_f32 v119, v119\n\t" \
    "v_pk_mul_f32 v[24:25], v[24:25], v[16:17]\n\t" \
    "v_pk_mul_f32 v[26:27], v[26:27], v[18:19]\n\t" \
    "v_pk_mul_f32 v[28:29], v[28:29], v[20:21]\n\t" \
    "v_pk_mul_f32 v[30:31], v[30:31], v[22:23]\n\t" \
    "v_pk_fma_f32 v[112:113], v[112:113], v[88:89], v[88:89] op_sel:[0,1,1] op_sel_hi:[1,1,1]\n\t" \
    "v_pk_fma_f32 v[114:115], v[114:115], v[88:89], v[88:89] op_sel:[0,1,1] op_sel_hi:[1,1,1]\n\t" \
    "v_pk_fma_f32 v[116:117], v[116:117], v[88:89], v[88:89] op_sel:[0,1,1] op_sel_hi:[1,1,1]\n\t" \
    "v_pk_fma_f32 v[118:119], v[118:119], v[88:89], v[88:89] op_sel:[0,1,1] op_sel_hi:[1,1,1]\n\t" \
    "v_rcp_f32 v112, v112\n\t" \
    "v_rcp_f32 v113, v113\n\t" \
    "v_rcp_f32 v114, v114\n\t" \
    "v_rcp_f32 v115, v115\n\t" \
    "v_rcp_f32 v116, v116\n\t" \
    "v_rcp_f32 v117, v117\n\t" \
    "v_rcp_f32 v118, v118\n\t" \
    "v_rcp_f32 v119, v119\n\t" \
    "s_nop 0\n\t" \
    "v_pk_mul_f32 v[112:113], v[112:113], v[24:25]\n\t" \
    "v_pk_mul_f32 v[114:115], v[114:115], v[26:27]\n\t" \
    "v_pk_mul_f32 v[116:117], v[116:117], v[28:29]\n\t" \
    "v_pk_mul_f32 v[118:119], v[118:119], v[30:31]\n\t" \
    "v_cvt_pk_bf16_f32 v124, v112, v113\n\t" \
    "v_cvt_pk_bf16_f32 v125, v114, v115\n\t" \
    "v_cvt_pk_bf16_f32 v126, v116, v117\n\t" \
    "v_cvt_pk_bf16_f32 v127, v118, v119\n\t" \
    "s_add_u32 s68, s55, 114688\n\t" \
    "buffer_store_dwordx4 v[124:127], v128, s[44:47], s68 offen sc1\n\t" \
    "v_pk_mul_f32 v[112:113], v[32:33], v[32:33]\n\t" \
    "v_pk_mul_f32 v[114:115], v[34:35], v[34:35]\n\t" \
    "v_pk_mul_f32 v[116:117], v[36:37], v[36:37]\n\t" \
    "v_pk_mul_f32 v[118:119], v[38:39], v[38:39]\n\t" \
    "v_pk_fma_f32 v[112:113], v[112:113], v[82:83], v[86:87] op_sel:[0,0,0] op_sel_hi:[1,0,0]\n\t" \
    "v_pk_fma_f32 v[114:115], v[114:115], v[82:83], v[86:87] op_sel:[0,0,0] op_sel_hi:[1,0,0]\n\t" \
    "v_pk_fma_f32 v[116:117], v[116:117], v[82:83], v[86:87] op_sel:[0,0,0] op_sel_hi:[1,0,0]\n\t" \
    "v_pk_fma_f32 v[118:119], v[118:119], v[82:83], v[86:87] op_sel:[0,0,0] op_sel_hi:[1,0,0]\n\t" \
    "v_pk_mul_f32 v[112:113], v[112:113], v[32:33]\n\t" \
    "v_pk_mul_f32 v[114:115], v[114:115], v[34:35]\n\t" \
    "v_pk_mul_f32 v[116:117], v[116:117], v[36:37]\n\t" \
    "v_pk_mul_f32 v[118:119], v[118:119], v[38:39]\n\t" \
    "v_exp_f32 v112, v112\n\t" \
    "v_exp_f32 v113, v113\n\t" \
    "v_exp_f32 v114, v114\n\t" \
    "v_exp_f32 v115, v115\n\t" \
    "v_exp_f32 v116, v116\n\t" \
    "v_exp_f32 v117, v117\n\t" \
    "v_exp_f32 v118, v118\n\t" \
    "v_exp_f32 v119, v119\n\t" \
    "v_pk_mul_f32 v[40:41], v[40:41], v[32:33]\n\t" \
    "v_pk_mul_f32 v[42:43], v[42:43], v[34:35]\n\t" \
    "v_pk_mul_f32 v[44:45], v[44:45], v[36:37]\n\t" \
    "v_pk_mul_f32 v[46:47], v[46:47], v[38:39]\n\t" \
    "v_pk_fma_f32 v[112:113], v[112:113], v[90:91], v[90:91] op_sel:[0,0,0] op_sel_hi:[1,0,0]\n\t" \
    "v_pk_fma_f32 v[114:115], v[114:115], v[90:91], v[90:91] op_sel:[0,0,0] op_sel_hi:[1,0,0]\n\t" \
    "v_pk_fma_f32 v[116:117], v[116:117], v[90:91], v[90:91] op_sel:[0,0,0] op_sel_hi:[1,0,0]\n\t" \
    "v_pk_fma_f32 v[118:119], v[118:119], v[90:91], v[90:91] op_sel:[0,0,0] op_sel_hi:[1,0,0]\n\t" \
    "v_rcp_f32 v112, v112\n\t" \
    "v_rcp_f32 v113, v113\n\t" \
    "v_rcp_f32 v114, v114\n\t" \
    "v_rcp_f32 v115, v115\n\t" \
    "v_rcp_f32 v116, v116\n\t" \
    "v_rcp_f32 v117, v117\n\t" \
    "v_rcp_f32 v118, v118\n\t" \
    "v_rcp_f32 v119, v119\n\t" \
    "s_nop 0\n\t" \
    "v_pk_mul_f32 v[112:113], v[112:113], v[40:41]\n\t" \
    "v_pk_mul_f32 v[114:115], v[114:115], v[42:43]\n\t" \
    "v_pk_mul_f32 v[116:117], v[116:117], v[44:45]\n\t" \
    "v_pk_mul_f32 v[118:119], v[118:119], v[46:47]\n\t" \
    "v_cvt_pk_bf16_f32 v120, v112, v113\n\t" \
    "v_cvt_pk_bf16_f32 v121, v114, v115\n\t" \
    "v_cvt_pk_bf16_f32 v122, v116, v117\n\t" \
    "v_cvt_pk_bf16_f32 v123, v118, v119\n\t" \
    "s_add_u32 s68, s55, 229376\n\t" \
    "buffer_store_dwordx4 v[120:123], v128, s[44:47], s68 offen sc1\n\t" \
    "s_nop 7\n\t" \
    "v_pk_mul_f32 v[112:113], v[48:49], v[48:49]\n\t" \
    "v_pk_mul_f32 v[114:115], v[50:51], v[50:51]\n\t" \
    "v_pk_mul_f32 v[116:117], v[52:53], v[52:53]\n\t" \
    "v_pk_mul_f32 v[118:119], v[54:55], v[54:55]\n\t" \
    "v_pk_fma_f32 v[112:113], v[112:113], v[82:83], v[86:87] op_sel:[0,1,1] op_sel_hi:[1,1,1]\n\t" \
    "v_pk_fma_f32 v[114:115], v[114:115], v[82:83], v[86:87] op_sel:[0,1,1] op_sel_hi:[1,1,1]\n\t" \
    "v_pk_fma_f32 v[116:117], v[116:117], v[82:83], v[86:87] op_sel:[0,1,1] op_sel_hi:[1,1,1]\n\t" \
    "v_pk_fma_f32 v[118:119], v[118:119], v[82:83], v[86:87] op_sel:[0,1,1] op_sel_hi:[1,1,1]\n\t" \
    "v_pk_mul_f32 v[112:113], v[112:113], v[48:49]\n\t" \
    "v_pk_mul_f32 v[114:115], v[114:115], v[50:51]\n\t" \
    "v_pk_mul_f32 v[116:117], v[116:117], v[52:53]\n\t" \
    "v_pk_mul_f32 v[118:119], v[118:119], v[54:55]\n\t" \
    "v_exp_f32 v112, v112\n\t" \
    "v_exp_f32 v113, v113\n\t" \
    "v_exp_f32 v114, v114\n\t" \
    "v_exp_f32 v115, v115\n\t" \
    "v_exp_f32 v116, v116\n\t" \
    "v_exp_f32 v117, v117\n\t" \
    "v_exp_f32 v118, v118\n\t" \
    "v_exp_f32 v119, v119\n\t" \
    "v_pk_mul_f32 v[56:57], v[56:57], v[48:49]\n\t" \
    "v_pk_mul_f32 v[58:59], v[58:59], v[50:51]\n\t" \
    "v_pk_mul_f32 v[60:61], v[60:61], v[52:53]\n\t" \
    "v_pk_mul_f32 v[62:63], v[62:63], v[54:55]\n\t" \
    "v_pk_fma_f32 v[112:113], v[112:113], v[90:91], v[90:91] op_sel:[0,1,1] op_sel_hi:[1,1,1]\n\t" \
    "v_pk_fma_f32 v[114:115], v[114:115], v[90:91], v[90:91] op_sel:[0,1,1] op_sel_hi:[1,1,1]\n\t" \
    "v_pk_fma_f32 v[116:117], v[116:117], v[90:91], v[90:91] op_sel:[0,1,1] op_sel_hi:[1,1,1]\n\t" \
    "v_pk_fma_f32 v[118:119], v[118:119], v[90:91], v[90:91] op_sel:[0,1,1] op_sel_hi:[1,1,1]\n\t" \
    "v_rcp_f32 v112, v112\n\t" \
    "v_rcp_f32 v113, v113\n\t" \
    "v_rcp_f32 v114, v114\n\t" \
    "v_rcp_f32 v115, v115\n\t" \
    "v_rcp_f32 v116, v116\n\t" \
    "v_rcp_f32 v117, v117\n\t" \
    "v_rcp_f32 v118, v118\n\t" \
    "v_rcp_f32 v119, v119\n\t" \
    "s_nop 0\n\t" \
    "v_pk_mul_f32 v[112:113], v[112:113], v[56:57]\n\t" \
    "v_pk_mul_f32 v[114:115], v[114:115], v[58:59]\n\t" \
    "v_pk_mul_f32 v[116:117], v[116:117], v[60:61]\n\t" \
    "v_pk_mul_f32 v[118:119], v[118:119], v[62:63]\n\t" \
    "v_cvt_pk_bf16_f32 v124, v112, v113\n\t" \
    "v_cvt_pk_bf16_f32 v125, v114, v115\n\t" \
    "v_cvt_pk_bf16_f32 v126, v116, v117\n\t" \
    "v_cvt_pk_bf16_f32 v127, v118, v119\n\t" \
    "s_add_u32 s68, s55, 344064\n\t" \
    "buffer_store_dwordx4 v[124:127], v128, s[44:47], s68 offen sc1\n\t" \
    ""
#define ATT_ASM \
    "s_waitcnt lgkmcnt(0)\n\t" \
    "ds_read_b128 v[88:91], v112\n\t" \
    "v_mov_b32 v101, v109\n\t" \
    "v_mov_b32 v103, v106\n\t" \
    "ds_read_b128 v[72:75], v109 offset:0\n\t" \
    "ds_read_b128 v[76:79], v109 offset:1024\n\t" \
    "ds_read_b128 v[80:83], v106 offset:0\n\t" \
    "v_lshlrev_b32 v105, 14, v107\n\t" \
    "s_mov_b32 s76, -6\n\t" \
    "s_waitcnt lgkmcnt(2)\n\t" \
    "v_mfma_f32_32x32x16_bf16 v[32:47], v[72:75], v[88:91], 0\n\t" \
    "s_waitcnt lgkmcnt(1)\n\t" \
    "v_mfma_f32_32x32x16_bf16 v[48:63], v[76:79], v[88:91], 0\n\t" \
    "s_nop 9\n\t" \
    "v_max_f32 v110, v32, v33\n\t" \
    "v_max_f32 v111, v34, v35\n\t" \
    "v_max3_f32 v110, v110, v36, v37\n\t" \
    "v_max3_f32 v111, v111, v38, v39\n\t" \
    "v_max3_f32 v110, v110, v40, v41\n\t" \
    "v_max3_f32 v111, v111, v42, v43\n\t" \
    "v_max3_f32 v110, v110, v44, v45\n\t" \
    "v_max3_f32 v111, v111, v46, v47\n\t" \
    "s_nop 3\n\t" \
    "v_max3_f32 v110, v110, v48, v49\n\t" \
    "v_max3_f32 v111, v111, v50, v51\n\t" \
    "v_max3_f32 v110, v110, v52, v53\n\t" \
    "v_max3_f32 v111, v111, v54, v55\n\t" \
    "v_max3_f32 v110, v110, v56, v57\n\t" \
    "v_max3_f32 v111, v111, v58, v59\n\t" \
    "v_max3_f32 v110, v110, v60, v61\n\t" \
    "v_max3_f32 v111, v111, v62, v63\n\t" \
    "v_max_f32 v110, v110, v111\n\t" \
    "v_mov_b32 v111, v110\n\t" \
    "s_nop 1\n\t" \
    "v_permlane32_swap_b32 v110, v111\n\t" \
    "s_nop 1\n\t" \
    "v_max_f32 v110, v110, v111\n\t" \
    "v_sub_f32 v16, s60, v110\n\t" \
    "v_sub_f32 v17, s60, v110\n\t" \
    "v_sub_f32 v18, s60, v110\n\t" \
    "v_sub_f32 v19, s60, v110\n\t" \
    "v_sub_f32 v20, s60, v110\n\t" \
    "v_sub_f32 v21, s60, v110\n\t" \
    "v_sub_f32 v22, s60, v110\n\t" \
    "v_sub_f32 v23, s60, v110\n\t" \
    "v_sub_f32 v24, s60, v110\n\t" \
    "v_sub_f32 v25, s60, v110\n\t" \
    "v_sub_f32 v26, s60, v110\n\t" \
    "v_sub_f32 v27, s60, v110\n\t" \
    "v_sub_f32 v28, s60, v110\n\t" \
    "v_sub_f32 v29, s60, v110\n\t" \
    "v_sub_f32 v30, s60, v110\n\t" \
    "v_sub_f32 v31, s60, v110\n\t" \
    "s_nop 1\n\t" \
    "v_mfma_f32_32x32x16_bf16 v[32:47], v[72:75], v[88:91], v[16:31]\n\t" \
    "s_nop 15\n\t" \
    "s_waitcnt lgkmcnt(0)\n\t" \
    "v_mfma_f32_32x32x16_bf16 v[48:63], v[76:79], v[88:91], v[16:31]\n\t" \
    "ds_read_b128 v[72:75], v101 offset:2048\n\t" \
    "ds_read_b128 v[84:87], v103 offset:512\n\t" \
    "ds_read_b128 v[92:95], v103 offset:1024\n\t" \
    "v_exp_f32 v32, v32\n\t" \
    "v_exp_f32 v33, v33\n\t" \
    "v_exp_f32 v34, v34\n\t" \
    "v_cvt_pk_bf16_f32 v64, v32, v33\n\t" \
    "v_exp_f32 v35, v35\n\t" \
    "v_exp_f32 v36, v36\n\t" \
    "v_cvt_pk_bf16_f32 v65, v34, v35\n\t" \
    "v_exp_f32 v37, v37\n\t" \
    "v_exp_f32 v38, v38\n\t" \
    "v_cvt_pk_bf16_f32 v66, v36, v37\n\t" \
    "s_setprio 3\n\t" \
    "v_exp_f32 v39, v39\n\t" \
    "v_exp_f32 v40, v40\n\t" \
    "v_cvt_pk_bf16_f32 v67, v38, v39\n\t" \
    "v_exp_f32 v41, v41\n\t" \
    "v_exp_f32 v42, v42\n\t" \
    "v_mfma_f32_32x32x16_bf16 v[0:15], v[80:83], v[64:67], 0\n\t" \
    "v_cvt_pk_bf16_f32 v68, v40, v41\n\t" \
    "v_exp_f32 v43, v43\n\t" \
    "v_exp_f32 v44, v44\n\t" \
    "v_cvt_pk_bf16_f32 v69, v42, v43\n\t" \
    "v_exp_f32 v45, v45\n\t" \
    "v_exp_f32 v46, v46\n\t" \
    "v_exp_f32 v47, v47\n\t" \
    "v_cvt_pk_bf16_f32 v70, v44, v45\n\t" \
    "v_cvt_pk_bf16_f32 v71, v46, v47\n\t" \
    "s_waitcnt lgkmcnt(0)\n\t" \
    "v_mfma_f32_32x32x16_bf16 v[32:47], v[72:75], v[88:91], v[16:31]\n\t" \
    "ds_read_b128 v[76:79], v101 offset:3072\n\t" \
    "ds_read_b128 v[96:99], v103 offset:1536\n\t" \
    "ds_read_b128 v[80:83], v103 offset:2048\n\t" \
    "v_exp_f32 v48, v48\n\t" \
    "v_exp_f32 v49, v49\n\t" \
    "v_exp_f32 v50, v50\n\t" \
    "v_cvt_pk_bf16_f32 v64, v48, v49\n\t" \
    "v_exp_f32 v51, v51\n\t" \
    "v_exp_f32 v52, v52\n\t" \
    "v_cvt_pk_bf16_f32 v65, v50, v51\n\t" \
    "v_exp_f32 v53, v53\n\t" \
    "v_mfma_f32_32x32x16_bf16 v[0:15], v[84:87], v[68:71], v[0:15]\n\t" \
    "v_exp_f32 v54, v54\n\t" \
    "v_cvt_pk_bf16_f32 v66, v52, v53\n\t" \
    "v_exp_f32 v55, v55\n\t" \
    "v_exp_f32 v56, v56\n\t" \
    "v_cvt_pk_bf16_f32 v67, v54, v55\n\t" \
    "v_exp_f32 v57, v57\n\t" \
    "v_exp_f32 v58, v58\n\t" \
    "v_mfma_f32_32x32x16_bf16 v[0:15], v[92:95], v[64:67], v[0:15]\n\t" \
    "v_cvt_pk_bf16_f32 v68, v56, v57\n\t" \
    "v_exp_f32 v59, v59\n\t" \
    "v_exp_f32 v60, v60\n\t" \
    "v_cvt_pk_bf16_f32 v69, v58, v59\n\t" \
    "v_exp_f32 v61, v61\n\t" \
    "v_exp_f32 v62, v62\n\t" \
    "v_exp_f32 v63, v63\n\t" \
    "v_cvt_pk_bf16_f32 v70, v60, v61\n\t" \
    "v_cvt_pk_bf16_f32 v71, v62, v63\n\t" \
    "s_waitcnt lgkmcnt(0)\n\t" \
    "v_mfma_f32_32x32x16_bf16 v[48:63], v[76:79], v[88:91], v[16:31]\n\t" \
    "ds_read_b128 v[72:75], v101 offset:4096\n\t" \
    "ds_read_b128 v[84:87], v103 offset:2560\n\t" \
    "ds_read_b128 v[92:95], v103 offset:3072\n\t" \
    "v_exp_f32 v32, v32\n\t" \
    "v_exp_f32 v33, v33\n\t" \
    "v_exp_f32 v34, v34\n\t" \
    "v_cvt_pk_bf16_f32 v64, v32, v33\n\t" \
    "v_exp_f32 v35, v35\n\t" \
    "v_exp_f32 v36, v36\n\t" \
    "v_cvt_pk_bf16_f32 v65, v34, v35\n\t" \
    "v_exp_f32 v37, v37\n\t" \
    "v_mfma_f32_32x32x16_bf16 v[0:15], v[96:99], v[68:71], v[0:15]\n\t" \
    "v_exp_f32 v38, v38\n\t" \
    "v_cvt_pk_bf16_f32 v66, v36, v37\n\t" \
    "v_exp_f32 v39, v39\n\t" \
    "v_exp_f32 v40, v40\n\t" \
    "v_cvt_pk_bf16_f32 v67, v38, v39\n\t" \
    "v_exp_f32 v41, v41\n\t" \
    "v_exp_f32 v42, v42\n\t" \
    "v_mfma_f32_32x32x16_bf16 v[0:15], v[80:83], v[64:67], v[0:15]\n\t" \
    "v_cvt_pk_bf16_f32 v68, v40, v41\n\t" \
    "v_exp_f32 v43, v43\n\t" \
    "v_exp_f32 v44, v44\n\t" \
    "v_cvt_pk_bf16_f32 v69, v42, v43\n\t" \
    "v_exp_f32 v45, v45\n\t" \
    "v_exp_f32 v46, v46\n\t" \
    "v_exp_f32 v47, v47\n\t" \
    "v_cvt_pk_bf16_f32 v70, v44, v45\n\t" \
    "v_cvt_pk_bf16_f32 v71, v46, v47\n\t" \
    "s_waitcnt lgkmcnt(0)\n\t" \
    "v_mfma_f32_32x32x16_bf16 v[32:47], v[72:75], v[88:91], v[16:31]\n\t" \
    "ds_read_b128 v[76:79], v101 offset:5120\n\t" \
    "ds_read_b128 v[96:99], v103 offset:3584\n\t" \
    "ds_read_b128 v[80:83], v103 offset:4096\n\t" \
    "v_exp_f32 v48, v48\n\t" \
    "v_exp_f32 v49, v49\n\t" \
    "v_exp_f32 v50, v50\n\t" \
    "v_cvt_pk_bf16_f32 v64, v48, v49\n\t" \
    "v_exp_f32 v51, v51\n\t" \
    "v_exp_f32 v52, v52\n\t" \
    "v_cvt_pk_bf16_f32 v65, v50, v51\n\t" \
    "v_exp_f32 v53, v53\n\t" \
    "v_mfma_f32_32x32x16_bf16 v[0:15], v[84:87], v[68:71], v[0:15]\n\t" \
    "v_exp_f32 v54, v54\n\t" \
    "v_cvt_pk_bf16_f32 v66, v52, v53\n\t" \
    "v_exp_f32 v55, v55\n\t" \
    "v_exp_f32 v56, v56\n\t" \
    "v_cvt_pk_bf16_f32 v67, v54, v55\n\t" \
    "v_exp_f32 v57, v57\n\t" \
    "v_exp_f32 v58, v58\n\t" \
    "v_mfma_f32_32x32x16_bf16 v[0:15], v[92:95], v[64:67], v[0:15]\n\t" \
    "v_cvt_pk_bf16_f32 v68, v56, v57\n\t" \
    "v_exp_f32 v59, v59\n\t" \
    "v_exp_f32 v60, v60\n\t" \
    "v_cvt_pk_bf16_f32 v69, v58, v59\n\t" \
    "v_exp_f32 v61, v61\n\t" \
    "v_exp_f32 v62, v62\n\t" \
    "v_exp_f32 v63, v63\n\t" \
    "v_cvt_pk_bf16_f32 v70, v60, v61\n\t" \
    "v_cvt_pk_bf16_f32 v71, v62, v63\n\t" \
    "s_waitcnt lgkmcnt(0)\n\t" \
    "v_mfma_f32_32x32x16_bf16 v[48:63], v[76:79], v[88:91], v[16:31]\n\t" \
    "ds_read_b128 v[72:75], v101 offset:6144\n\t" \
    "ds_read_b128 v[84:87], v103 offset:4608\n\t" \
    "ds_read_b128 v[92:95], v103 offset:5120\n\t" \
    "v_exp_f32 v32, v32\n\t" \
    "v_exp_f32 v33, v33\n\t" \
    "v_exp_f32 v34, v34\n\t" \
    "v_cvt_pk_bf16_f32 v64, v32, v33\n\t" \
    "v_exp_f32 v35, v35\n\t" \
    "v_exp_f32 v36, v36\n\t" \
    "v_cvt_pk_bf16_f32 v65, v34, v35\n\t" \
    "v_exp_f32 v37, v37\n\t" \
    "v_mfma_f32_32x32x16_bf16 v[0:15], v[96:99], v[68:71], v[0:15]\n\t" \
    "v_exp_f32 v38, v38\n\t" \
    "v_cvt_pk_bf16_f32 v66, v36, v37\n\t" \
    "v_exp_f32 v39, v39\n\t" \
    "v_exp_f32 v40, v40\n\t" \
    "v_cvt_pk_bf16_f32 v67, v38, v39\n\t" \
    "v_exp_f32 v41, v41\n\t" \
    "v_exp_f32 v42, v42\n\t" \
    "v_mfma_f32_32x32x16_bf16 v[0:15], v[80:83], v[64:67], v[0:15]\n\t" \
    "v_cvt_pk_bf16_f32 v68, v40, v41\n\t" \
    "v_exp_f32 v43, v43\n\t" \
    "v_exp_f32 v44, v44\n\t" \
    "v_cvt_pk_bf16_f32 v69, v42, v43\n\t" \
    "v_exp_f32 v45, v45\n\t" \
    "v_exp_f32 v46, v46\n\t" \
    "v_exp_f32 v47, v47\n\t" \
    "v_cvt_pk_bf16_f32 v70, v44, v45\n\t" \
    "v_cvt_pk_bf16_f32 v71, v46, v47\n\t" \
    "s_waitcnt lgkmcnt(0)\n\t" \
    "v_mfma_f32_32x32x16_bf16 v[32:47], v[72:75], v[88:91], v[16:31]\n\t" \
    "ds_read_b128 v[76:79], v101 offset:7168\n\t" \
    "ds_read_b128 v[96:99], v103 offset:5632\n\t" \
    "ds_read_b128 v[80:83], v103 offset:6144\n\t" \
    "v_exp_f32 v48, v48\n\t" \
    "v_exp_f32 v49, v49\n\t" \
    "v_exp_f32 v50, v50\n\t" \
    "v_cvt_pk_bf16_f32 v64, v48, v49\n\t" \
    "v_exp_f32 v51, v51\n\t" \
    "v_exp_f32 v52, v52\n\t" \
    "v_cvt_pk_bf16_f32 v65, v50, v51\n\t" \
    "v_exp_f32 v53, v53\n\t" \
    "s_bitcmp1_b32 s58, 0\n\t" \
    "s_cbranch_scc0 L_nh_%=\n\t" \
    "s_waitcnt vmcnt(0)\n\t" \
    "v_mov_b32 v110, 1\n\t" \
    "s_mov_b64 exec, 1\n\t" \
    "buffer_atomic_add v110, off, s[44:47], s57\n\t" \
    "s_mov_b64 exec, -1\n\t" \
    "L_nh_%=:\n\t" \
    "v_mfma_f32_32x32x16_bf16 v[0:15], v[84:87], v[68:71], v[0:15]\n\t" \
    "v_exp_f32 v54, v54\n\t" \
    "v_cvt_pk_bf16_f32 v66, v52, v53\n\t" \
    "v_exp_f32 v55, v55\n\t" \
    "v_exp_f32 v56, v56\n\t" \
    "v_cvt_pk_bf16_f32 v67, v54, v55\n\t" \
    "v_exp_f32 v57, v57\n\t" \
    "v_exp_f32 v58, v58\n\t" \
    "v_mfma_f32_32x32x16_bf16 v[0:15], v[92:95], v[64:67], v[0:15]\n\t" \
    "v_cvt_pk_bf16_f32 v68, v56, v57\n\t" \
    "v_exp_f32 v59, v59\n\t" \
    "v_exp_f32 v60, v60\n\t" \
    "v_cvt_pk_bf16_f32 v69, v58, v59\n\t" \
    "v_exp_f32 v61, v61\n\t" \
    "v_exp_f32 v62, v62\n\t" \
    "v_exp_f32 v63, v63\n\t" \
    "v_cvt_pk_bf16_f32 v70, v60, v61\n\t" \
    "v_cvt_pk_bf16_f32 v71, v62, v63\n\t" \
    "s_waitcnt lgkmcnt(0)\n\t" \
    "v_mfma_f32_32x32x16_bf16 v[48:63], v[76:79], v[88:91], v[16:31]\n\t" \
    "ds_read_b128 v[72:75], v101 offset:16384\n\t" \
    "ds_read_b128 v[84:87], v103 offset:6656\n\t" \
    "ds_read_b128 v[92:95], v103 offset:7168\n\t" \
    "v_exp_f32 v32, v32\n\t" \
    "v_exp_f32 v33, v33\n\t" \
    "v_exp_f32 v34, v34\n\t" \
    "v_cvt_pk_bf16_f32 v64, v32, v33\n\t" \
    "v_exp_f32 v35, v35\n\t" \
    "v_exp_f32 v36, v36\n\t" \
    "v_cvt_pk_bf16_f32 v65, v34, v35\n\t" \
    "v_exp_f32 v37, v37\n\t" \
    "v_mfma_f32_32x32x16_bf16 v[0:15], v[96:99], v[68:71], v[0:15]\n\t" \
    "v_exp_f32 v38, v38\n\t" \
    "v_cvt_pk_bf16_f32 v66, v36, v37\n\t" \
    "v_exp_f32 v39, v39\n\t" \
    "v_exp_f32 v40, v40\n\t" \
    "v_cvt_pk_bf16_f32 v67, v38, v39\n\t" \
    "v_exp_f32 v41, v41\n\t" \
    "v_exp_f32 v42, v42\n\t" \
    "v_mfma_f32_32x32x16_bf16 v[0:15], v[80:83], v[64:67], v[0:15]\n\t" \
    "v_cvt_pk_bf16_f32 v68, v40, v41\n\t" \
    "v_exp_f32 v43, v43\n\t" \
    "v_exp_f32 v44, v44\n\t" \
    "v_cvt_pk_bf16_f32 v69, v42, v43\n\t" \
    "v_exp_f32 v45, v45\n\t" \
    "v_exp_f32 v46, v46\n\t" \
    "v_exp_f32 v47, v47\n\t" \
    "v_cvt_pk_bf16_f32 v70, v44, v45\n\t" \
    "v_cvt_pk_bf16_f32 v71, v46, v47\n\t" \
    "s_waitcnt lgkmcnt(0)\n\t" \
    "v_mfma_f32_32x32x16_bf16 v[32:47], v[72:75], v[88:91], v[16:31]\n\t" \
    "ds_read_b128 v[76:79], v101 offset:17408\n\t" \
    "ds_read_b128 v[96:99], v103 offset:7680\n\t" \
    "v_add_u32 v103, v103, v105\n\t" \
    "ds_read_b128 v[80:83], v103 offset:0\n\t" \
    "v_exp_f32 v48, v48\n\t" \
    "v_exp_f32 v49, v49\n\t" \
    "v_add_u32 v101, 0x4000, v101\n\t" \
    "v_exp_f32 v50, v50\n\t" \
    "v_cvt_pk_bf16_f32 v64, v48, v49\n\t" \
    "v_exp_f32 v51, v51\n\t" \
    "v_exp_f32 v52, v52\n\t" \
    "v_cvt_pk_bf16_f32 v65, v50, v51\n\t" \
    "v_exp_f32 v53, v53\n\t" \
    "v_mfma_f32_32x32x16_bf16 v[0:15], v[84:87], v[68:71], v[0:15]\n\t" \
    "v_exp_f32 v54, v54\n\t" \
    "s_setprio 2\n\t" \
    "v_cvt_pk_bf16_f32 v66, v52, v53\n\t" \
    "v_exp_f32 v55, v55\n\t" \
    "v_exp_f32 v56, v56\n\t" \
    "v_cvt_pk_bf16_f32 v67, v54, v55\n\t" \
    "v_exp_f32 v57, v57\n\t" \
    "v_exp_f32 v58, v58\n\t" \
    "v_mfma_f32_32x32x16_bf16 v[0:15], v[92:95], v[64:67], v[0:15]\n\t" \
    "v_cvt_pk_bf16_f32 v68, v56, v57\n\t" \
    "v_exp_f32 v59, v59\n\t" \
    "v_exp_f32 v60, v60\n\t" \
    "v_cvt_pk_bf16_f32 v69, v58, v59\n\t" \
    "v_exp_f32 v61, v61\n\t" \
    "v_exp_f32 v62, v62\n\t" \
    "s_setprio 1\n\t" \
    "v_exp_f32 v63, v63\n\t" \
    "v_cvt_pk_bf16_f32 v70, v60, v61\n\t" \
    "v_cvt_pk_bf16_f32 v71, v62, v63\n\t" \
    "s_setprio 0\n\t" \
    "s_barrier\n\t" \
    ".p2align 6\n\t" \
    "L_att_%=:\n\t" \
    "s_waitcnt lgkmcnt(0)\n\t" \
    "v_mfma_f32_32x32x16_bf16 v[48:63], v[76:79], v[88:91], v[16:31]\n\t" \
    "ds_read_b128 v[72:75], v101 offset:2048\n\t" \
    "ds_read_b128 v[84:87], v103 offset:512\n\t" \
    "ds_read_b128 v[92:95], v103 offset:1024\n\t" \
    "ds_write_b32 v114, v101\n\t" \
    "v_exp_f32 v32, v32\n\t" \
    "v_exp_f32 v33, v33\n\t" \
    "v_exp_f32 v34, v34\n\t" \
    "v_cvt_pk_bf16_f32 v64, v32, v33\n\t" \
    "v_exp_f32 v35, v35\n\t" \
    "v_exp_f32 v36, v36\n\t" \
    "v_cvt_pk_bf16_f32 v65, v34, v35\n\t" \
    "v_exp_f32 v37, v37\n\t" \
    "v_mfma_f32_32x32x16_bf16 v[0:15], v[96:99], v[68:71], v[0:15]\n\t" \
    "v_exp_f32 v38, v38\n\t" \
    "v_cvt_pk_bf16_f32 v66, v36, v37\n\t" \
    "s_setprio 3\n\t" \
    "v_exp_f32 v39, v39\n\t" \
    "v_exp_f32 v40, v40\n\t" \
    "v_cvt_pk_bf16_f32 v67, v38, v39\n\t" \
    "v_exp_f32 v41, v41\n\t" \
    "v_exp_f32 v42, v42\n\t" \
    "v_mfma_f32_32x32x16_bf16 v[0:15], v[80:83], v[64:67], v[0:15]\n\t" \
    "v_cvt_pk_bf16_f32 v68, v40, v41\n\t" \
    "v_exp_f32 v43, v43\n\t" \
    "v_exp_f32 v44, v44\n\t" \
    "v_cvt_pk_bf16_f32 v69, v42, v43\n\t" \
    "v_exp_f32 v45, v45\n\t" \
    "v_exp_f32 v46, v46\n\t" \
    "v_exp_f32 v47, v47\n\t" \
    "v_cvt_pk_bf16_f32 v70, v44, v45\n\t" \
    "v_cvt_pk_bf16_f32 v71, v46, v47\n\t" \
    "s_waitcnt lgkmcnt(0)\n\t" \
    "v_mfma_f32_32x32x16_bf16 v[32:47], v[72:75], v[88:91], v[16:31]\n\t" \
    "ds_read_b128 v[76:79], v101 offset:3072\n\t" \
    "ds_read_b128 v[96:99], v103 offset:1536\n\t" \
    "ds_read_b128 v[80:83], v103 offset:2048\n\t" \
    "v_exp_f32 v48, v48\n\t" \
    "v_exp_f32 v49, v49\n\t" \
    "v_exp_f32 v50, v50\n\t" \
    "v_cvt_pk_bf16_f32 v64, v48, v49\n\t" \
    "v_exp_f32 v51, v51\n\t" \
    "v_exp_f32 v52, v52\n\t" \
    "v_cvt_pk_bf16_f32 v65, v50, v51\n\t" \
    "v_exp_f32 v53, v53\n\t" \
    "v_mfma_f32_32x32x16_bf16 v[0:15], v[84:87], v[68:71], v[0:15]\n\t" \
    "v_exp_f32 v54, v54\n\t" \
    "v_cvt_pk_bf16_f32 v66, v52, v53\n\t" \
    "v_exp_f32 v55, v55\n\t" \
    "v_exp_f32 v56, v56\n\t" \
    "v_cvt_pk_bf16_f32 v67, v54, v55\n\t" \
    "v_exp_f32 v57, v57\n\t" \
    "v_exp_f32 v58, v58\n\t" \
    "v_mfma_f32_32x32x16_bf16 v[0:15], v[92:95], v[64:67], v[0:15]\n\t" \
    "v_cvt_pk_bf16_f32 v68, v56, v57\n\t" \
    "v_exp_f32 v59, v59\n\t" \
    "v_exp_f32 v60, v60\n\t" \
    "v_cvt_pk_bf16_f32 v69, v58, v59\n\t" \
    "v_exp_f32 v61, v61\n\t" \
    "v_exp_f32 v62, v62\n\t" \
    "v_exp_f32 v63, v63\n\t" \
    "v_cvt_pk_bf16_f32 v70, v60, v61\n\t" \
    "v_cvt_pk_bf16_f32 v71, v62, v63\n\t" \
    "s_waitcnt lgkmcnt(0)\n\t" \
    "v_mfma_f32_32x32x16_bf16 v[48:63], v[76:79], v[88:91], v[16:31]\n\t" \
    "ds_read_b128 v[72:75], v101 offset:4096\n\t" \
    "ds_read_b128 v[84:87], v103 offset:2560\n\t" \
    "ds_read_b128 v[92:95], v103 offset:3072\n\t" \
    "v_exp_f32 v32, v32\n\t" \
    "v_exp_f32 v33, v33\n\t" \
    "v_exp_f32 v34, v34\n\t" \
    "v_cvt_pk_bf16_f32 v64, v32, v33\n\t" \
    "v_exp_f32 v35, v35\n\t" \
    "v_exp_f32 v36, v36\n\t" \
    "v_cvt_pk_bf16_f32 v65, v34, v35\n\t" \
    "v_exp_f32 v37, v37\n\t" \
    "v_mfma_f32_32x32x16_bf16 v[0:15], v[96:99], v[68:71], v[0:15]\n\t" \
    "v_exp_f32 v38, v38\n\t" \
    "v_cvt_pk_bf16_f32 v66, v36, v37\n\t" \
    "v_exp_f32 v39, v39\n\t" \
    "v_exp_f32 v40, v40\n\t" \
    "v_cvt_pk_bf16_f32 v67, v38, v39\n\t" \
    "v_exp_f32 v41, v41\n\t" \
    "v_exp_f32 v42, v42\n\t" \
    "v_mfma_f32_32x32x16_bf16 v[0:15], v[80:83], v[64:67], v[0:15]\n\t" \
    "v_cvt_pk_bf16_f32 v68, v40, v41\n\t" \
    "v_exp_f32 v43, v43\n\t" \
    "v_exp_f32 v44, v44\n\t" \
    "v_cvt_pk_bf16_f32 v69, v42, v43\n\t" \
    "v_exp_f32 v45, v45\n\t" \
    "v_exp_f32 v46, v46\n\t" \
    "v_exp_f32 v47, v47\n\t" \
    "v_cvt_pk_bf16_f32 v70, v44, v45\n\t" \
    "v_cvt_pk_bf16_f32 v71, v46, v47\n\t" \
    "s_waitcnt lgkmcnt(0)\n\t" \
    "v_mfma_f32_32x32x16_bf16 v[32:47], v[72:75], v[88:91], v[16:31]\n\t" \
    "ds_read_b128 v[76:79], v101 offset:5120\n\t" \
    "ds_read_b128 v[96:99], v103 offset:3584\n\t" \
    "ds_read_b128 v[80:83], v103 offset:4096\n\t" \
    "v_exp_f32 v48, v48\n\t" \
    "v_exp_f32 v49, v49\n\t" \
    "v_exp_f32 v50, v50\n\t" \
    "v_cvt_pk_bf16_f32 v64, v48, v49\n\t" \
    "v_exp_f32 v51, v51\n\t" \
    "v_exp_f32 v52, v52\n\t" \
    "v_cvt_pk_bf16_f32 v65, v50, v51\n\t" \
    "v_exp_f32 v53, v53\n\t" \
    "v_mfma_f32_32x32x16_bf16 v[0:15], v[84:87], v[68:71], v[0:15]\n\t" \
    "v_exp_f32 v54, v54\n\t" \
    "v_cvt_pk_bf16_f32 v66, v52, v53\n\t" \
    "v_exp_f32 v55, v55\n\t" \
    "v_exp_f32 v56, v56\n\t" \
    "v_cvt_pk_bf16_f32 v67, v54, v55\n\t" \
    "v_exp_f32 v57, v57\n\t" \
    "v_exp_f32 v58, v58\n\t" \
    "v_mfma_f32_32x32x16_bf16 v[0:15], v[92:95], v[64:67], v[0:15]\n\t" \
    "v_cvt_pk_bf16_f32 v68, v56, v57\n\t" \
    "v_exp_f32 v59, v59\n\t" \
    "v_exp_f32 v60, v60\n\t" \
    "v_cvt_pk_bf16_f32 v69, v58, v59\n\t" \
    "v_exp_f32 v61, v61\n\t" \
    "v_exp_f32 v62, v62\n\t" \
    "v_exp_f32 v63, v63\n\t" \
    "v_cvt_pk_bf16_f32 v70, v60, v61\n\t" \
    "v_cvt_pk_bf16_f32 v71, v62, v63\n\t" \
    "s_waitcnt lgkmcnt(0)\n\t" \
    "v_mfma_f32_32x32x16_bf16 v[48:63], v[76:79], v[88:91], v[16:31]\n\t" \
    "ds_read_b128 v[72:75], v101 offset:6144\n\t" \
    "ds_read_b128 v[84:87], v103 offset:4608\n\t" \
    "ds_read_b128 v[92:95], v103 offset:5120\n\t" \
    "v_exp_f32 v32, v32\n\t" \
    "v_exp_f32 v33, v33\n\t" \
    "v_exp_f32 v34, v34\n\t" \
    "v_cvt_pk_bf16_f32 v64, v32, v33\n\t" \
    "v_exp_f32 v35, v35\n\t" \
    "v_exp_f32 v36, v36\n\t" \
    "v_cvt_pk_bf16_f32 v65, v34, v35\n\t" \
    "v_exp_f32 v37, v37\n\t" \
    "v_mfma_f32_32x32x16_bf16 v[0:15], v[96:99], v[68:71], v[0:15]\n\t" \
    "v_exp_f32 v38, v38\n\t" \
    "v_cvt_pk_bf16_f32 v66, v36, v37\n\t" \
    "v_exp_f32 v39, v39\n\t" \
    "v_exp_f32 v40, v40\n\t" \
    "v_cvt_pk_bf16_f32 v67, v38, v39\n\t" \
    "v_exp_f32 v41, v41\n\t" \
    "v_exp_f32 v42, v42\n\t" \
    "v_mfma_f32_32x32x16_bf16 v[0:15], v[80:83], v[64:67], v[0:15]\n\t" \
    "v_cvt_pk_bf16_f32 v68, v40, v41\n\t" \
    "v_exp_f32 v43, v43\n\t" \
    "v_exp_f32 v44, v44\n\t" \
    "v_cvt_pk_bf16_f32 v69, v42, v43\n\t" \
    "v_exp_f32 v45, v45\n\t" \
    "v_exp_f32 v46, v46\n\t" \
    "v_exp_f32 v47, v47\n\t" \
    "v_cvt_pk_bf16_f32 v70, v44, v45\n\t" \
    "v_cvt_pk_bf16_f32 v71, v46, v47\n\t" \
    "s_waitcnt lgkmcnt(0)\n\t" \
    "v_mfma_f32_32x32x16_bf16 v[32:47], v[72:75], v[88:91], v[16:31]\n\t" \
    "ds_read_b128 v[76:79], v101 offset:7168\n\t" \
    "ds_read_b128 v[96:99], v103 offset:5632\n\t" \
    "ds_read_b128 v[80:83], v103 offset:6144\n\t" \
    "v_exp_f32 v48, v48\n\t" \
    "v_exp_f32 v49, v49\n\t" \
    "v_exp_f32 v50, v50\n\t" \
    "v_cvt_pk_bf16_f32 v64, v48, v49\n\t" \
    "v_exp_f32 v51, v51\n\t" \
    "v_exp_f32 v52, v52\n\t" \
    "v_cvt_pk_bf16_f32 v65, v50, v51\n\t" \
    "v_exp_f32 v53, v53\n\t" \
    "v_mfma_f32_32x32x16_bf16 v[0:15], v[84:87], v[68:71], v[0:15]\n\t" \
    "v_exp_f32 v54, v54\n\t" \
    "v_cvt_pk_bf16_f32 v66, v52, v53\n\t" \
    "v_exp_f32 v55, v55\n\t" \
    "v_exp_f32 v56, v56\n\t" \
    "v_cvt_pk_bf16_f32 v67, v54, v55\n\t" \
    "v_exp_f32 v57, v57\n\t" \
    "v_exp_f32 v58, v58\n\t" \
    "v_mfma_f32_32x32x16_bf16 v[0:15], v[92:95], v[64:67], v[0:15]\n\t" \
    "v_cvt_pk_bf16_f32 v68, v56, v57\n\t" \
    "v_exp_f32 v59, v59\n\t" \
    "v_exp_f32 v60, v60\n\t" \
    "v_cvt_pk_bf16_f32 v69, v58, v59\n\t" \
    "v_exp_f32 v61, v61\n\t" \
    "v_exp_f32 v62, v62\n\t" \
    "v_exp_f32 v63, v63\n\t" \
    "v_cvt_pk_bf16_f32 v70, v60, v61\n\t" \
    "v_cvt_pk_bf16_f32 v71, v62, v63\n\t" \
    "s_waitcnt lgkmcnt(0)\n\t" \
    "v_mfma_f32_32x32x16_bf16 v[48:63], v[76:79], v[88:91], v[16:31]\n\t" \
    "ds_read_b128 v[72:75], v101 offset:16384\n\t" \
    "ds_read_b128 v[84:87], v103 offset:6656\n\t" \
    "ds_read_b128 v[92:95], v103 offset:7168\n\t" \
    "v_exp_f32 v32, v32\n\t" \
    "v_exp_f32 v33, v33\n\t" \
    "v_exp_f32 v34, v34\n\t" \
    "v_cvt_pk_bf16_f32 v64, v32, v33\n\t" \
    "v_exp_f32 v35, v35\n\t" \
    "v_exp_f32 v36, v36\n\t" \
    "v_cvt_pk_bf16_f32 v65, v34, v35\n\t" \
    "v_exp_f32 v37, v37\n\t" \
    "v_mfma_f32_32x32x16_bf16 v[0:15], v[96:99], v[68:71], v[0:15]\n\t" \
    "v_exp_f32 v38, v38\n\t" \
    "v_cvt_pk_bf16_f32 v66, v36, v37\n\t" \
    "v_exp_f32 v39, v39\n\t" \
    "v_exp_f32 v40, v40\n\t" \
    "v_cvt_pk_bf16_f32 v67, v38, v39\n\t" \
    "v_exp_f32 v41, v41\n\t" \
    "v_exp_f32 v42, v42\n\t" \
    "v_mfma_f32_32x32x16_bf16 v[0:15], v[80:83], v[64:67], v[0:15]\n\t" \
    "v_cvt_pk_bf16_f32 v68, v40, v41\n\t" \
    "v_exp_f32 v43, v43\n\t" \
    "v_exp_f32 v44, v44\n\t" \
    "v_cvt_pk_bf16_f32 v69, v42, v43\n\t" \
    "v_exp_f32 v45, v45\n\t" \
    "v_exp_f32 v46, v46\n\t" \
    "v_exp_f32 v47, v47\n\t" \
    "v_cvt_pk_bf16_f32 v70, v44, v45\n\t" \
    "v_cvt_pk_bf16_f32 v71, v46, v47\n\t" \
    "s_waitcnt lgkmcnt(0)\n\t" \
    "v_mfma_f32_32x32x16_bf16 v[32:47], v[72:75], v[88:91], v[16:31]\n\t" \
    "ds_read_b128 v[76:79], v101 offset:17408\n\t" \
    "ds_read_b128 v[96:99], v103 offset:7680\n\t" \
    "v_add_u32 v103, v103, v105\n\t" \
    "ds_read_b128 v[80:83], v103 offset:0\n\t" \
    "v_exp_f32 v48, v48\n\t" \
    "v_exp_f32 v49, v49\n\t" \
    "v_add_u32 v101, 0x4000, v101\n\t" \
    "v_exp_f32 v50, v50\n\t" \
    "v_cvt_pk_bf16_f32 v64, v48, v49\n\t" \
    "v_exp_f32 v51, v51\n\t" \
    "v_exp_f32 v52, v52\n\t" \
    "v_cvt_pk_bf16_f32 v65, v50, v51\n\t" \
    "v_exp_f32 v53, v53\n\t" \
    "v_mfma_f32_32x32x16_bf16 v[0:15], v[84:87], v[68:71], v[0:15]\n\t" \
    "v_exp_f32 v54, v54\n\t" \
    "s_setprio 2\n\t" \
    "v_cvt_pk_bf16_f32 v66, v52, v53\n\t" \
    "v_exp_f32 v55, v55\n\t" \
    "v_exp_f32 v56, v56\n\t" \
    "v_cvt_pk_bf16_f32 v67, v54, v55\n\t" \
    "v_exp_f32 v57, v57\n\t" \
    "v_exp_f32 v58, v58\n\t" \
    "v_mfma_f32_32x32x16_bf16 v[0:15], v[92:95], v[64:67], v[0:15]\n\t" \
    "v_cvt_pk_bf16_f32 v68, v56, v57\n\t" \
    "v_exp_f32 v59, v59\n\t" \
    "v_exp_f32 v60, v60\n\t" \
    "v_cvt_pk_bf16_f32 v69, v58, v59\n\t" \
    "v_exp_f32 v61, v61\n\t" \
    "v_exp_f32 v62, v62\n\t" \
    "s_setprio 1\n\t" \
    "v_exp_f32 v63, v63\n\t" \
    "v_cvt_pk_bf16_f32 v70, v60, v61\n\t" \
    "v_cvt_pk_bf16_f32 v71, v62, v63\n\t" \
    "s_setprio 0\n\t" \
    "s_add_u32 s76, s76, 1\n\t" \
    "s_cbranch_scc0 L_att_%=\n\t" \
    "s_waitcnt lgkmcnt(0)\n\t" \
    "v_mfma_f32_32x32x16_bf16 v[48:63], v[76:79], v[88:91], v[16:31]\n\t" \
    "ds_read_b128 v[72:75], v101 offset:2048\n\t" \
    "ds_read_b128 v[84:87], v103 offset:512\n\t" \
    "ds_read_b128 v[92:95], v103 offset:1024\n\t" \
    "ds_write_b32 v114, v101\n\t" \
    "v_exp_f32 v32, v32\n\t" \
    "v_exp_f32 v33, v33\n\t" \
    "v_exp_f32 v34, v34\n\t" \
    "v_cvt_pk_bf16_f32 v64, v32, v33\n\t" \
    "v_exp_f32 v35, v35\n\t" \
    "v_exp_f32 v36, v36\n\t" \
    "v_cvt_pk_bf16_f32 v65, v34, v35\n\t" \
    "v_exp_f32 v37, v37\n\t" \
    "v_mfma_f32_32x32x16_bf16 v[0:15], v[96:99], v[68:71], v[0:15]\n\t" \
    "v_exp_f32 v38, v38\n\t" \
    "v_cvt_pk_bf16_f32 v66, v36, v37\n\t" \
    "s_setprio 3\n\t" \
    "v_exp_f32 v39, v39\n\t" \
    "v_exp_f32 v40, v40\n\t" \
    "v_cvt_pk_bf16_f32 v67, v38, v39\n\t" \
    "v_exp_f32 v41, v41\n\t" \
    "v_exp_f32 v42, v42\n\t" \
    "v_mfma_f32_32x32x16_bf16 v[0:15], v[80:83], v[64:67], v[0:15]\n\t" \
    "v_cvt_pk_bf16_f32 v68, v40, v41\n\t" \
    "v_exp_f32 v43, v43\n\t" \
    "v_exp_f32 v44, v44\n\t" \
    "v_cvt_pk_bf16_f32 v69, v42, v43\n\t" \
    "v_exp_f32 v45, v45\n\t" \
    "v_exp_f32 v46, v46\n\t" \
    "v_exp_f32 v47, v47\n\t" \
    "v_cvt_pk_bf16_f32 v70, v44, v45\n\t" \
    "v_cvt_pk_bf16_f32 v71, v46, v47\n\t" \
    "s_waitcnt lgkmcnt(0)\n\t" \
    "v_mfma_f32_32x32x16_bf16 v[32:47], v[72:75], v[88:91], v[16:31]\n\t" \
    "ds_read_b128 v[76:79], v101 offset:3072\n\t" \
    "ds_read_b128 v[96:99], v103 offset:1536\n\t" \
    "ds_read_b128 v[80:83], v103 offset:2048\n\t" \
    "v_exp_f32 v48, v48\n\t" \
    "v_exp_f32 v49, v49\n\t" \
    "v_exp_f32 v50, v50\n\t" \
    "v_cvt_pk_bf16_f32 v64, v48, v49\n\t" \
    "s_add_u32 s70, s59, 0\n\t" \
    "buffer_load_dwordx4 v[120:123], v108, s[44:47], s70 offen offset:0 sc1\n\t" \
    "buffer_load_dwordx4 v[124:127], v108, s[44:47], s70 offen offset:1024 sc1\n\t" \
    "buffer_load_dwordx4 v[128:131], v108, s[44:47], s70 offen offset:2048 sc1\n\t" \
    "buffer_load_dwordx4 v[132:135], v108, s[44:47], s70 offen offset:3072 sc1\n\t" \
    "v_exp_f32 v51, v51\n\t" \
    "v_exp_f32 v52, v52\n\t" \
    "v_cvt_pk_bf16_f32 v65, v50, v51\n\t" \
    "v_exp_f32 v53, v53\n\t" \
    "v_mfma_f32_32x32x16_bf16 v[0:15], v[84:87], v[68:71], v[0:15]\n\t" \
    "v_exp_f32 v54, v54\n\t" \
    "v_cvt_pk_bf16_f32 v66, v52, v53\n\t" \
    "v_exp_f32 v55, v55\n\t" \
    "v_exp_f32 v56, v56\n\t" \
    "v_cvt_pk_bf16_f32 v67, v54, v55\n\t" \
    "v_exp_f32 v57, v57\n\t" \
    "v_exp_f32 v58, v58\n\t" \
    "v_mfma_f32_32x32x16_bf16 v[0:15], v[92:95], v[64:67], v[0:15]\n\t" \
    "v_cvt_pk_bf16_f32 v68, v56, v57\n\t" \
    "v_exp_f32 v59, v59\n\t" \
    "v_exp_f32 v60, v60\n\t" \
    "v_cvt_pk_bf16_f32 v69, v58, v59\n\t" \
    "v_exp_f32 v61, v61\n\t" \
    "v_exp_f32 v62, v62\n\t" \
    "v_exp_f32 v63, v63\n\t" \
    "v_cvt_pk_bf16_f32 v70, v60, v61\n\t" \
    "v_cvt_pk_bf16_f32 v71, v62, v63\n\t" \
    "s_waitcnt lgkmcnt(0)\n\t" \
    "v_mfma_f32_32x32x16_bf16 v[48:63], v[76:79], v[88:91], v[16:31]\n\t" \
    "ds_read_b128 v[72:75], v101 offset:4096\n\t" \
    "ds_read_b128 v[84:87], v103 offset:2560\n\t" \
    "ds_read_b128 v[92:95], v103 offset:3072\n\t" \
    "v_exp_f32 v32, v32\n\t" \
    "v_exp_f32 v33, v33\n\t" \
    "v_exp_f32 v34, v34\n\t" \
    "v_cvt_pk_bf16_f32 v64, v32, v33\n\t" \
    "s_add_u32 s70, s59, 4096\n\t" \
    "buffer_load_dwordx4 v[136:139], v108, s[44:47], s70 offen offset:0 sc1\n\t" \
    "buffer_load_dwordx4 v[140:143], v108, s[44:47], s70 offen offset:1024 sc1\n\t" \
    "buffer_load_dwordx4 v[144:147], v108, s[44:47], s70 offen offset:2048 sc1\n\t" \
    "buffer_load_dwordx4 v[148:151], v108, s[44:47], s70 offen offset:3072 sc1\n\t" \
    "v_exp_f32 v35, v35\n\t" \
    "v_exp_f32 v36, v36\n\t" \
    "v_cvt_pk_bf16_f32 v65, v34, v35\n\t" \
    "v_exp_f32 v37, v37\n\t" \
    "v_mfma_f32_32x32x16_bf16 v[0:15], v[96:99], v[68:71], v[0:15]\n\t" \
    "v_exp_f32 v38, v38\n\t" \
    "v_cvt_pk_bf16_f32 v66, v36, v37\n\t" \
    "v_exp_f32 v39, v39\n\t" \
    "v_exp_f32 v40, v40\n\t" \
    "v_cvt_pk_bf16_f32 v67, v38, v39\n\t" \
    "v_exp_f32 v41, v41\n\t" \
    "v_exp_f32 v42, v42\n\t" \
    "v_mfma_f32_32x32x16_bf16 v[0:15], v[80:83], v[64:67], v[0:15]\n\t" \
    "v_cvt_pk_bf16_f32 v68, v40, v41\n\t" \
    "v_exp_f32 v43, v43\n\t" \
    "v_exp_f32 v44, v44\n\t" \
    "v_cvt_pk_bf16_f32 v69, v42, v43\n\t" \
    "v_exp_f32 v45, v45\n\t" \
    "v_exp_f32 v46, v46\n\t" \
    "v_exp_f32 v47, v47\n\t" \
    "v_cvt_pk_bf16_f32 v70, v44, v45\n\t" \
    "v_cvt_pk_bf16_f32 v71, v46, v47\n\t" \
    "s_waitcnt lgkmcnt(0)\n\t" \
    "v_mfma_f32_32x32x16_bf16 v[32:47], v[72:75], v[88:91], v[16:31]\n\t" \
    "ds_read_b128 v[76:79], v101 offset:5120\n\t" \
    "ds_read_b128 v[96:99], v103 offset:3584\n\t" \
    "ds_read_b128 v[80:83], v103 offset:4096\n\t" \
    "v_exp_f32 v48, v48\n\t" \
    "v_exp_f32 v49, v49\n\t" \
    "v_exp_f32 v50, v50\n\t" \
    "v_cvt_pk_bf16_f32 v64, v48, v49\n\t" \
    "s_add_u32 s70, s59, 8192\n\t" \
    "buffer_load_dwordx4 v[152:155], v108, s[44:47], s70 offen offset:0 sc1\n\t" \
    "buffer_load_dwordx4 v[156:159], v108, s[44:47], s70 offen offset:1024 sc1\n\t" \
    "buffer_load_dwordx4 v[160:163], v108, s[44:47], s70 offen offset:2048 sc1\n\t" \
    "buffer_load_dwordx4 v[164:167], v108, s[44:47], s70 offen offset:3072 sc1\n\t" \
    "v_exp_f32 v51, v51\n\t" \
    "v_exp_f32 v52, v52\n\t" \
    "v_cvt_pk_bf16_f32 v65, v50, v51\n\t" \
    "v_exp_f32 v53, v53\n\t" \
    "v_mfma_f32_32x32x16_bf16 v[0:15], v[84:87], v[68:71], v[0:15]\n\t" \
    "v_exp_f32 v54, v54\n\t" \
    "v_cvt_pk_bf16_f32 v66, v52, v53\n\t" \
    "v_exp_f32 v55, v55\n\t" \
    "v_exp_f32 v56, v56\n\t" \
    "v_cvt_pk_bf16_f32 v67, v54, v55\n\t" \
    "v_exp_f32 v57, v57\n\t" \
    "v_exp_f32 v58, v58\n\t" \
    "v_mfma_f32_32x32x16_bf16 v[0:15], v[92:95], v[64:67], v[0:15]\n\t" \
    "v_cvt_pk_bf16_f32 v68, v56, v57\n\t" \
    "v_exp_f32 v59, v59\n\t" \
    "v_exp_f32 v60, v60\n\t" \
    "v_cvt_pk_bf16_f32 v69, v58, v59\n\t" \
    "v_exp_f32 v61, v61\n\t" \
    "v_exp_f32 v62, v62\n\t" \
    "v_exp_f32 v63, v63\n\t" \
    "v_cvt_pk_bf16_f32 v70, v60, v61\n\t" \
    "v_cvt_pk_bf16_f32 v71, v62, v63\n\t" \
    "s_waitcnt lgkmcnt(0)\n\t" \
    "v_mfma_f32_32x32x16_bf16 v[48:63], v[76:79], v[88:91], v[16:31]\n\t" \
    "ds_read_b128 v[72:75], v101 offset:6144\n\t" \
    "ds_read_b128 v[84:87], v103 offset:4608\n\t" \
    "ds_read_b128 v[92:95], v103 offset:5120\n\t" \
    "v_exp_f32 v32, v32\n\t" \
    "v_exp_f32 v33, v33\n\t" \
    "v_exp_f32 v34, v34\n\t" \
    "v_cvt_pk_bf16_f32 v64, v32, v33\n\t" \
    "v_exp_f32 v35, v35\n\t" \
    "v_exp_f32 v36, v36\n\t" \
    "v_cvt_pk_bf16_f32 v65, v34, v35\n\t" \
    "v_exp_f32 v37, v37\n\t" \
    "v_mfma_f32_32x32x16_bf16 v[0:15], v[96:99], v[68:71], v[0:15]\n\t" \
    "v_exp_f32 v38, v38\n\t" \
    "v_cvt_pk_bf16_f32 v66, v36, v37\n\t" \
    "v_exp_f32 v39, v39\n\t" \
    "v_exp_f32 v40, v40\n\t" \
    "v_cvt_pk_bf16_f32 v67, v38, v39\n\t" \
    "v_exp_f32 v41, v41\n\t" \
    "v_exp_f32 v42, v42\n\t" \
    "v_mfma_f32_32x32x16_bf16 v[0:15], v[80:83], v[64:67], v[0:15]\n\t" \
    "v_cvt_pk_bf16_f32 v68, v40, v41\n\t" \
    "v_exp_f32 v43, v43\n\t" \
    "v_exp_f32 v44, v44\n\t" \
    "v_cvt_pk_bf16_f32 v69, v42, v43\n\t" \
    "v_exp_f32 v45, v45\n\t" \
    "v_exp_f32 v46, v46\n\t" \
    "v_exp_f32 v47, v47\n\t" \
    "v_cvt_pk_bf16_f32 v70, v44, v45\n\t" \
    "v_cvt_pk_bf16_f32 v71, v46, v47\n\t" \
    "s_waitcnt lgkmcnt(0)\n\t" \
    "v_mfma_f32_32x32x16_bf16 v[32:47], v[72:75], v[88:91], v[16:31]\n\t" \
    "ds_read_b128 v[76:79], v101 offset:7168\n\t" \
    "ds_read_b128 v[96:99], v103 offset:5632\n\t" \
    "ds_read_b128 v[80:83], v103 offset:6144\n\t" \
    "v_exp_f32 v48, v48\n\t" \
    "v_exp_f32 v49, v49\n\t" \
    "v_exp_f32 v50, v50\n\t" \
    "v_cvt_pk_bf16_f32 v64, v48, v49\n\t" \
    "v_exp_f32 v51, v51\n\t" \
    "v_exp_f32 v52, v52\n\t" \
    "v_cvt_pk_bf16_f32 v65, v50, v51\n\t" \
    "v_exp_f32 v53, v53\n\t" \
    "v_mfma_f32_32x32x16_bf16 v[0:15], v[84:87], v[68:71], v[0:15]\n\t" \
    "v_exp_f32 v54, v54\n\t" \
    "v_cvt_pk_bf16_f32 v66, v52, v53\n\t" \
    "v_exp_f32 v55, v55\n\t" \
    "v_exp_f32 v56, v56\n\t" \
    "v_cvt_pk_bf16_f32 v67, v54, v55\n\t" \
    "v_exp_f32 v57, v57\n\t" \
    "v_exp_f32 v58, v58\n\t" \
    "v_mfma_f32_32x32x16_bf16 v[0:15], v[92:95], v[64:67], v[0:15]\n\t" \
    "v_cvt_pk_bf16_f32 v68, v56, v57\n\t" \
    "v_exp_f32 v59, v59\n\t" \
    "v_exp_f32 v60, v60\n\t" \
    "v_cvt_pk_bf16_f32 v69, v58, v59\n\t" \
    "v_exp_f32 v61, v61\n\t" \
    "v_exp_f32 v62, v62\n\t" \
    "v_exp_f32 v63, v63\n\t" \
    "v_cvt_pk_bf16_f32 v70, v60, v61\n\t" \
    "v_cvt_pk_bf16_f32 v71, v62, v63\n\t" \
    "s_waitcnt lgkmcnt(0)\n\t" \
    "v_mfma_f32_32x32x16_bf16 v[48:63], v[76:79], v[88:91], v[16:31]\n\t" \
    "ds_read_b128 v[84:87], v103 offset:6656\n\t" \
    "ds_read_b128 v[92:95], v103 offset:7168\n\t" \
    "v_exp_f32 v32, v32\n\t" \
    "v_exp_f32 v33, v33\n\t" \
    "v_exp_f32 v34, v34\n\t" \
    "v_cvt_pk_bf16_f32 v64, v32, v33\n\t" \
    "v_and_b32 v115, 0xf0, v108\n\t" \
    "v_lshlrev_b32 v115, 6, v115\n\t" \
    "buffer_load_dword v115, v115, s[44:47], s71 offen sc1\n\t" \
    "v_exp_f32 v35, v35\n\t" \
    "v_exp_f32 v36, v36\n\t" \
    "v_cvt_pk_bf16_f32 v65, v34, v35\n\t" \
    "v_exp_f32 v37, v37\n\t" \
    "v_mfma_f32_32x32x16_bf16 v[0:15], v[96:99], v[68:71], v[0:15]\n\t" \
    "v_exp_f32 v38, v38\n\t" \
    "v_cvt_pk_bf16_f32 v66, v36, v37\n\t" \
    "v_exp_f32 v39, v39\n\t" \
    "v_exp_f32 v40, v40\n\t" \
    "v_cvt_pk_bf16_f32 v67, v38, v39\n\t" \
    "v_exp_f32 v41, v41\n\t" \
    "v_exp_f32 v42, v42\n\t" \
    "v_mfma_f32_32x32x16_bf16 v[0:15], v[80:83], v[64:67], v[0:15]\n\t" \
    "v_cvt_pk_bf16_f32 v68, v40, v41\n\t" \
    "v_exp_f32 v43, v43\n\t" \
    "v_exp_f32 v44, v44\n\t" \
    "v_cvt_pk_bf16_f32 v69, v42, v43\n\t" \
    "v_exp_f32 v45, v45\n\t" \
    "v_exp_f32 v46, v46\n\t" \
    "v_exp_f32 v47, v47\n\t" \
    "v_cvt_pk_bf16_f32 v70, v44, v45\n\t" \
    "v_cvt_pk_bf16_f32 v71, v46, v47\n\t" \
    "s_waitcnt lgkmcnt(0)\n\t" \
    "ds_read_b128 v[96:99], v103 offset:7680\n\t" \
    "v_exp_f32 v48, v48\n\t" \
    "v_exp_f32 v49, v49\n\t" \
    "v_exp_f32 v50, v50\n\t" \
    "v_cvt_pk_bf16_f32 v64, v48, v49\n\t" \
    "v_exp_f32 v51, v51\n\t" \
    "v_exp_f32 v52, v52\n\t" \
    "v_cvt_pk_bf16_f32 v65, v50, v51\n\t" \
    "v_exp_f32 v53, v53\n\t" \
    "v_mfma_f32_32x32x16_bf16 v[0:15], v[84:87], v[68:71], v[0:15]\n\t" \
    "v_exp_f32 v54, v54\n\t" \
    "s_setprio 2\n\t" \
    "v_cvt_pk_bf16_f32 v66, v52, v53\n\t" \
    "v_exp_f32 v55, v55\n\t" \
    "v_exp_f32 v56, v56\n\t" \
    "v_cvt_pk_bf16_f32 v67, v54, v55\n\t" \
    "v_exp_f32 v57, v57\n\t" \
    "v_exp_f32 v58, v58\n\t" \
    "v_mfma_f32_32x32x16_bf16 v[0:15], v[92:95], v[64:67], v[0:15]\n\t" \
    "v_cvt_pk_bf16_f32 v68, v56, v57\n\t" \
    "v_exp_f32 v59, v59\n\t" \
    "v_exp_f32 v60, v60\n\t" \
    "v_cvt_pk_bf16_f32 v69, v58, v59\n\t" \
    "v_exp_f32 v61, v61\n\t" \
    "v_exp_f32 v62, v62\n\t" \
    "s_setprio 1\n\t" \
    "v_exp_f32 v63, v63\n\t" \
    "v_cvt_pk_bf16_f32 v70, v60, v61\n\t" \
    "v_cvt_pk_bf16_f32 v71, v62, v63\n\t" \
    "s_setprio 0\n\t" \
    "s_waitcnt lgkmcnt(0)\n\t" \
    "v_mfma_f32_32x32x16_bf16 v[0:15], v[96:99], v[68:71], v[0:15]\n\t" \
    "s_nop 15\n\t" \
    "s_nop 3\n\t" \
    "v_pk_fma_f32 v[110:111], v[0:1], 0, v[8:9]\n\t" \
    "v_pk_fma_f32 v[110:111], v[2:3], 0, v[110:111]\n\t" \
    "v_pk_fma_f32 v[110:111], v[4:5], 0, v[110:111]\n\t" \
    "v_pk_fma_f32 v[110:111], v[6:7], 0, v[110:111]\n\t" \
    "v_add_f32 v110, v110, v111\n\t" \
    "v_cmp_ngt_f32 vcc, s56, v110\n\t" \
    "s_cmp_lg_u64 vcc, 0\n\t" \
    "s_cselect_b32 s76, 1, 0\n\t" \
    ""
#define TAIL_ASM \
    "v_rcp_f32 v8, v8\n\t" \
    "s_nop 0\n\t" \
    "v_mul_f32 v0, v0, v8\n\t" \
    "v_mul_f32 v1, v1, v8\n\t" \
    "v_mul_f32 v2, v2, v8\n\t" \
    "v_mul_f32 v3, v3, v8\n\t" \
    "v_mul_f32 v4, v4, v8\n\t" \
    "v_mul_f32 v5, v5, v8\n\t" \
    "v_mul_f32 v6, v6, v8\n\t" \
    "v_mul_f32 v7, v7, v8\n\t" \
    "v_cvt_pk_bf16_f32 v16, v0, v1\n\t" \
    "v_cvt_pk_bf16_f32 v17, v2, v3\n\t" \
    "v_cvt_pk_bf16_f32 v18, v4, v5\n\t" \
    "v_cvt_pk_bf16_f32 v19, v6, v7\n\t" \
    "v_min_u32 v16, 0xfffffffe, v16\n\t" \
    "v_min_u32 v17, 0xfffffffe, v17\n\t" \
    "v_min_u32 v18, 0xfffffffe, v18\n\t" \
    "v_min_u32 v19, 0xfffffffe, v19\n\t" \
    "buffer_store_dwordx4 v[16:19], v108, s[44:47], s80 offen sc1\n\t" \
    "s_add_u32 s70, s82, 12288\n\t" \
    "s_nop 2\n\t" \
    "buffer_load_dwordx4 v[16:19], v108, s[44:47], s70 offen offset:0 sc1\n\t" \
    "buffer_load_dwordx4 v[20:23], v108, s[44:47], s70 offen offset:1024 sc1\n\t" \
    "buffer_load_dwordx4 v[24:27], v108, s[44:47], s70 offen offset:2048 sc1\n\t" \
    "buffer_load_dwordx4 v[28:31], v108, s[44:47], s70 offen offset:3072 sc1\n\t" \
    "buffer_load_dwordx2 v[104:105], v109, s[88:91], 0 offen\n\t" \
    "buffer_load_dwordx2 v[106:107], v109, s[88:91], 0 offen offset:16\n\t" \
    "v_and_b32 v113, 0xf0, v108\n\t" \
    "v_lshlrev_b32 v113, 6, v113\n\t" \
    "s_waitcnt vmcnt(7)\n\t" \
    "v_cmp_ne_u32 vcc, -1, v115\n\t" \
    "s_and_b32 s74, vcc_lo, 0xffff\n\t" \
    "s_mov_b32 s72, 0\n\t" \
    "s_bitcmp1_b32 s72, 0\n\t" \
    "s_cbranch_scc1 L_sa0_%=\n\t" \
    "s_bfe_u32 s68, s74, 0x40000\n\t" \
    "s_cmp_eq_u32 s68, 15\n\t" \
    "s_cbranch_scc0 L_sa0_%=\n\t" \
    "s_add_u32 s70, s81, 0\n\t" \
    "buffer_load_dwordx4 v[32:35], v108, s[44:47], s70 offen offset:0 sc1\n\t" \
    "buffer_load_dwordx4 v[36:39], v108, s[44:47], s70 offen offset:1024 sc1\n\t" \
    "buffer_load_dwordx4 v[40:43], v108, s[44:47], s70 offen offset:2048 sc1\n\t" \
    "buffer_load_dwordx4 v[44:47], v108, s[44:47], s70 offen offset:3072 sc1\n\t" \
    "s_bitset1_b32 s72, 0\n\t" \
    "L_sa0_%=:\n\t" \
    "s_bitcmp1_b32 s72, 1\n\t" \
    "s_cbranch_scc1 L_sa1_%=\n\t" \
    "s_bfe_u32 s68, s74, 0x40004\n\t" \
    "s_cmp_eq_u32 s68, 15\n\t" \
    "s_cbranch_scc0 L_sa1_%=\n\t" \
    "s_add_u32 s70, s81, 4096\n\t" \
    "buffer_load_dwordx4 v[48:51], v108, s[44:47], s70 offen offset:0 sc1\n\t" \
    "buffer_load_dwordx4 v[52:55], v108, s[44:47], s70 offen offset:1024 sc1\n\t" \
    "buffer_load_dwordx4 v[56:59], v108, s[44:47], s70 offen offset:2048 sc1\n\t" \
    "buffer_load_dwordx4 v[60:63], v108, s[44:47], s70 offen offset:3072 sc1\n\t" \
    "s_bitset1_b32 s72, 1\n\t" \
    "L_sa1_%=:\n\t" \
    "s_bitcmp1_b32 s72, 2\n\t" \
    "s_cbranch_scc1 L_sa2_%=\n\t" \
    "s_bfe_u32 s68, s74, 0x40008\n\t" \
    "s_cmp_eq_u32 s68, 15\n\t" \
    "s_cbranch_scc0 L_sa2_%=\n\t" \
    "s_add_u32 s70, s81, 8192\n\t" \
    "buffer_load_dwordx4 v[64:67], v108, s[44:47], s70 offen offset:0 sc1\n\t" \
    "buffer_load_dwordx4 v[68:71], v108, s[44:47], s70 offen offset:1024 sc1\n\t" \
    "buffer_load_dwordx4 v[72:75], v108, s[44:47], s70 offen offset:2048 sc1\n\t" \
    "buffer_load_dwordx4 v[76:79], v108, s[44:47], s70 offen offset:3072 sc1\n\t" \
    "s_bitset1_b32 s72, 2\n\t" \
    "L_sa2_%=:\n\t" \
    "s_bitcmp1_b32 s72, 3\n\t" \
    "s_cbranch_scc1 L_sa3_%=\n\t" \
    "s_bfe_u32 s68, s74, 0x4000c\n\t" \
    "s_cmp_eq_u32 s68, 15\n\t" \
    "s_cbranch_scc0 L_sa3_%=\n\t" \
    "s_add_u32 s70, s81, 12288\n\t" \
    "buffer_load_dwordx4 v[80:83], v108, s[44:47], s70 offen offset:0 sc1\n\t" \
    "buffer_load_dwordx4 v[84:87], v108, s[44:47], s70 offen offset:1024 sc1\n\t" \
    "buffer_load_dwordx4 v[88:91], v108, s[44:47], s70 offen offset:2048 sc1\n\t" \
    "buffer_load_dwordx4 v[92:95], v108, s[44:47], s70 offen offset:3072 sc1\n\t" \
    "s_bitset1_b32 s72, 3\n\t" \
    "L_sa3_%=:\n\t" \
    "s_cmp_eq_u32 s72, 15\n\t" \
    "s_cselect_b32 s73, 1, 0\n\t" \
    "s_barrier\n\t" \
    "v_add_u32 v110, s84, v108\n\t" \
    "ds_read_b128 v[0:3], v110 offset:0\n\t" \
    "ds_read_b128 v[4:7], v110 offset:1024\n\t" \
    "ds_read_b128 v[8:11], v110 offset:16384\n\t" \
    "ds_read_b128 v[12:15], v110 offset:17408\n\t" \
    "ds_read_b128 v[96:99], v110 offset:32768\n\t" \
    "ds_read_b128 v[100:103], v110 offset:33792\n\t" \
    "s_waitcnt lgkmcnt(2)\n\t" \
    "v_add_f32 v0, v0, v8\n\t" \
    "v_add_f32 v1, v1, v9\n\t" \
    "v_add_f32 v2, v2, v10\n\t" \
    "v_add_f32 v3, v3, v11\n\t" \
    "v_add_f32 v4, v4, v12\n\t" \
    "v_add_f32 v5, v5, v13\n\t" \
    "v_add_f32 v6, v6, v14\n\t" \
    "v_add_f32 v7, v7, v15\n\t" \
    "ds_read_b128 v[8:11], v110 offset:49152\n\t" \
    "ds_read_b128 v[12:15], v110 offset:50176\n\t" \
    "s_waitcnt lgkmcnt(2)\n\t" \
    "v_add_f32 v0, v0, v96\n\t" \
    "v_add_f32 v1, v1, v97\n\t" \
    "v_add_f32 v2, v2, v98\n\t" \
    "v_add_f32 v3, v3, v99\n\t" \
    "v_add_f32 v4, v4, v100\n\t" \
    "v_add_f32 v5, v5, v101\n\t" \
    "v_add_f32 v6, v6, v102\n\t" \
    "v_add_f32 v7, v7, v103\n\t" \
    "s_waitcnt lgkmcnt(0)\n\t" \
    "v_add_f32 v0, v0, v8\n\t" \
    "v_add_f32 v1, v1, v9\n\t" \
    "v_add_f32 v2, v2, v10\n\t" \
    "v_add_f32 v3, v3, v11\n\t" \
    "v_add_f32 v4, v4, v12\n\t" \
    "v_add_f32 v5, v5, v13\n\t" \
    "v_add_f32 v6, v6, v14\n\t" \
    "v_add_f32 v7, v7, v15\n\t" \
    "v_mul_f32 v96, 0xbfb8aa3b, v0\n\t" \
    "v_mul_f32 v97, 0xbfb8aa3b, v1\n\t" \
    "v_mul_f32 v98, 0xbfb8aa3b, v2\n\t" \
    "v_mul_f32 v99, 0xbfb8aa3b, v3\n\t" \
    "v_mul_f32 v100, 0xbfb8aa3b, v4\n\t" \
    "v_mul_f32 v101, 0xbfb8aa3b, v5\n\t" \
    "v_mul_f32 v102, 0xbfb8aa3b, v6\n\t" \
    "v_mul_f32 v103, 0xbfb8aa3b, v7\n\t" \
    "v_exp_f32 v96, v96\n\t" \
    "v_exp_f32 v97, v97\n\t" \
    "v_exp_f32 v98, v98\n\t" \
    "v_exp_f32 v99, v99\n\t" \
    "v_exp_f32 v100, v100\n\t" \
    "v_exp_f32 v101, v101\n\t" \
    "v_exp_f32 v102, v102\n\t" \
    "v_exp_f32 v103, v103\n\t" \
    "v_add_f32 v96, 1.0, v96\n\t" \
    "v_add_f32 v97, 1.0, v97\n\t" \
    "v_add_f32 v98, 1.0, v98\n\t" \
    "v_add_f32 v99, 1.0, v99\n\t" \
    "v_add_f32 v100, 1.0, v100\n\t" \
    "v_add_f32 v101, 1.0, v101\n\t" \
    "v_add_f32 v102, 1.0, v102\n\t" \
    "v_add_f32 v103, 1.0, v103\n\t" \
    "v_rcp_f32 v96, v96\n\t" \
    "v_rcp_f32 v97, v97\n\t" \
    "v_rcp_f32 v98, v98\n\t" \
    "v_rcp_f32 v99, v99\n\t" \
    "v_rcp_f32 v100, v100\n\t" \
    "v_rcp_f32 v101, v101\n\t" \
    "v_rcp_f32 v102, v102\n\t" \
    "v_rcp_f32 v103, v103\n\t" \
    "v_mul_f32 v96, v96, v0\n\t" \
    "v_mul_f32 v97, v97, v1\n\t" \
    "v_mul_f32 v98, v98, v2\n\t" \
    "v_mul_f32 v99, v99, v3\n\t" \
    "v_mul_f32 v100, v100, v4\n\t" \
    "v_mul_f32 v101, v101, v5\n\t" \
    "v_mul_f32 v102, v102, v6\n\t" \
    "v_mul_f32 v103, v103, v7\n\t" \
    "s_mov_b32 s71, 0\n\t" \
    "L_w0_%=:\n\t" \
    "s_bitcmp1_b32 s72, 0\n\t" \
    "s_cbranch_scc1 L_h0_%=\n\t" \
    "s_mov_b32 s73, 0\n\t" \
    "s_add_u32 s71, s71, 1\n\t" \
    "s_cmp_ge_u32 s71, 0x200000\n\t" \
    "s_cbranch_scc1 L_ok0_%=\n\t" \
    "buffer_load_dword v115, v113, s[44:47], s81 offen sc1\n\t" \
    "s_waitcnt vmcnt(0)\n\t" \
    "v_cmp_ne_u32 vcc, -1, v115\n\t" \
    "s_and_b32 s74, vcc_lo, 0xffff\n\t" \
    "s_bitcmp1_b32 s72, 0\n\t" \
    "s_cbranch_scc1 L_sp00_%=\n\t" \
    "s_bfe_u32 s68, s74, 0x40000\n\t" \
    "s_cmp_eq_u32 s68, 15\n\t" \
    "s_cbranch_scc0 L_sp00_%=\n\t" \
    "s_add_u32 s70, s81, 0\n\t" \
    "buffer_load_dwordx4 v[32:35], v108, s[44:47], s70 offen offset:0 sc1\n\t" \
    "buffer_load_dwordx4 v[36:39], v108, s[44:47], s70 offen offset:1024 sc1\n\t" \
    "buffer_load_dwordx4 v[40:43], v108, s[44:47], s70 offen offset:2048 sc1\n\t" \
    "buffer_load_dwordx4 v[44:47], v108, s[44:47], s70 offen offset:3072 sc1\n\t" \
    "s_bitset1_b32 s72, 0\n\t" \
    "L_sp00_%=:\n\t" \
    "s_bitcmp1_b32 s72, 1\n\t" \
    "s_cbranch_scc1 L_sp01_%=\n\t" \
    "s_bfe_u32 s68, s74, 0x40004\n\t" \
    "s_cmp_eq_u32 s68, 15\n\t" \
    "s_cbranch_scc0 L_sp01_%=\n\t" \
    "s_add_u32 s70, s81, 4096\n\t" \
    "buffer_load_dwordx4 v[48:51], v108, s[44:47], s70 offen offset:0 sc1\n\t" \
    "buffer_load_dwordx4 v[52:55], v108, s[44:47], s70 offen offset:1024 sc1\n\t" \
    "buffer_load_dwordx4 v[56:59], v108, s[44:47], s70 offen offset:2048 sc1\n\t" \
    "buffer_load_dwordx4 v[60:63], v108, s[44:47], s70 offen offset:3072 sc1\n\t" \
    "s_bitset1_b32 s72, 1\n\t" \
    "L_sp01_%=:\n\t" \
    "s_bitcmp1_b32 s72, 2\n\t" \
    "s_cbranch_scc1 L_sp02_%=\n\t" \
    "s_bfe_u32 s68, s74, 0x40008\n\t" \
    "s_cmp_eq_u32 s68, 15\n\t" \
    "s_cbranch_scc0 L_sp02_%=\n\t" \
    "s_add_u32 s70, s81, 8192\n\t" \
    "buffer_load_dwordx4 v[64:67], v108, s[44:47], s70 offen offset:0 sc1\n\t" \
    "buffer_load_dwordx4 v[68:71], v108, s[44:47], s70 offen offset:1024 sc1\n\t" \
    "buffer_load_dwordx4 v[72:75], v108, s[44:47], s70 offen offset:2048 sc1\n\t" \
    "buffer_load_dwordx4 v[76:79], v108, s[44:47], s70 offen offset:3072 sc1\n\t" \
    "s_bitset1_b32 s72, 2\n\t" \
    "L_sp02_%=:\n\t" \
    "s_bitcmp1_b32 s72, 3\n\t" \
    "s_cbranch_scc1 L_sp03_%=\n\t" \
    "s_bfe_u32 s68, s74, 0x4000c\n\t" \
    "s_cmp_eq_u32 s68, 15\n\t" \
    "s_cbranch_scc0 L_sp03_%=\n\t" \
    "s_add_u32 s70, s81, 12288\n\t" \
    "buffer_load_dwordx4 v[80:83], v108, s[44:47], s70 offen offset:0 sc1\n\t" \
    "buffer_load_dwordx4 v[84:87], v108, s[44:47], s70 offen offset:1024 sc1\n\t" \
    "buffer_load_dwordx4 v[88:91], v108, s[44:47], s70 offen offset:2048 sc1\n\t" \
    "buffer_load_dwordx4 v[92:95], v108, s[44:47], s70 offen offset:3072 sc1\n\t" \
    "s_bitset1_b32 s72, 3\n\t" \
    "L_sp03_%=:\n\t" \
    "s_bitcmp1_b32 s72, 0\n\t" \
    "s_cbranch_scc1 L_h0_%=\n\t" \
    "s_sleep 4\n\t" \
    "s_branch L_w0_%=\n\t" \
    "L_h0_%=:\n\t" \
    "s_cmp_eq_u32 s73, 1\n\t" \
    "s_cbranch_scc1 L_f0_%=\n\t" \
    "s_waitcnt vmcnt(0)\n\t" \
    "L_f0_%=:\n\t" \
    "s_waitcnt vmcnt(12)\n\t" \
    "v_max3_u32 v112, v32, v33, v34\n\t" \
    "v_max3_u32 v112, v112, v35, v36\n\t" \
    "v_max3_u32 v112, v112, v37, v38\n\t" \
    "v_max3_u32 v112, v112, v39, v40\n\t" \
    "v_max3_u32 v112, v112, v41, v42\n\t" \
    "v_max3_u32 v112, v112, v43, v44\n\t" \
    "v_max3_u32 v112, v112, v45, v46\n\t" \
    "v_max_u32 v112, v112, v47\n\t" \
    "v_cmp_eq_u32 vcc, -1, v112\n\t" \
    "s_cbranch_vccz L_ok0_%=\n\t" \
    "s_bitset0_b32 s72, 0\n\t" \
    "s_branch L_w0_%=\n\t" \
    "L_ok0_%=:\n\t" \
    "v_mfma_f32_32x32x16_bf16 v[0:15], v[120:123], v[32:35], 0\n\t" \
    "v_mfma_f32_32x32x16_bf16 v[0:15], v[124:127], v[36:39], v[0:15]\n\t" \
    "v_mfma_f32_32x32x16_bf16 v[0:15], v[128:131], v[40:43], v[0:15]\n\t" \
    "v_mfma_f32_32x32x16_bf16 v[0:15], v[132:135], v[44:47], v[0:15]\n\t" \
    "L_w1_%=:\n\t" \
    "s_bitcmp1_b32 s72, 1\n\t" \
    "s_cbranch_scc1 L_h1_%=\n\t" \
    "s_mov_b32 s73, 0\n\t" \
    "s_add_u32 s71, s71, 1\n\t" \
    "s_cmp_ge_u32 s71, 0x200000\n\t" \
    "s_cbranch_scc1 L_ok1_%=\n\t" \
    "buffer_load_dword v115, v113, s[44:47], s81 offen sc1\n\t" \
    "s_waitcnt vmcnt(0)\n\t" \
    "v_cmp_ne_u32 vcc, -1, v115\n\t" \
    "s_and_b32 s74, vcc_lo, 0xffff\n\t" \
    "s_bitcmp1_b32 s72, 1\n\t" \
    "s_cbranch_scc1 L_sp11_%=\n\t" \
    "s_bfe_u32 s68, s74, 0x40004\n\t" \
    "s_cmp_eq_u32 s68, 15\n\t" \
    "s_cbranch_scc0 L_sp11_%=\n\t" \
    "s_add_u32 s70, s81, 4096\n\t" \
    "buffer_load_dwordx4 v[48:51], v108, s[44:47], s70 offen offset:0 sc1\n\t" \
    "buffer_load_dwordx4 v[52:55], v108, s[44:47], s70 offen offset:1024 sc1\n\t" \
    "buffer_load_dwordx4 v[56:59], v108, s[44:47], s70 offen offset:2048 sc1\n\t" \
    "buffer_load_dwordx4 v[60:63], v108, s[44:47], s70 offen offset:3072 sc1\n\t" \
    "s_bitset1_b32 s72, 1\n\t" \
    "L_sp11_%=:\n\t" \
    "s_bitcmp1_b32 s72, 2\n\t" \
    "s_cbranch_scc1 L_sp12_%=\n\t" \
    "s_bfe_u32 s68, s74, 0x40008\n\t" \
    "s_cmp_eq_u32 s68, 15\n\t" \
    "s_cbranch_scc0 L_sp12_%=\n\t" \
    "s_add_u32 s70, s81, 8192\n\t" \
    "buffer_load_dwordx4 v[64:67], v108, s[44:47], s70 offen offset:0 sc1\n\t" \
    "buffer_load_dwordx4 v[68:71], v108, s[44:47], s70 offen offset:1024 sc1\n\t" \
    "buffer_load_dwordx4 v[72:75], v108, s[44:47], s70 offen offset:2048 sc1\n\t" \
    "buffer_load_dwordx4 v[76:79], v108, s[44:47], s70 offen offset:3072 sc1\n\t" \
    "s_bitset1_b32 s72, 2\n\t" \
    "L_sp12_%=:\n\t" \
    "s_bitcmp1_b32 s72, 3\n\t" \
    "s_cbranch_scc1 L_sp13_%=\n\t" \
    "s_bfe_u32 s68, s74, 0x4000c\n\t" \
    "s_cmp_eq_u32 s68, 15\n\t" \
    "s_cbranch_scc0 L_sp13_%=\n\t" \
    "s_add_u32 s70, s81, 12288\n\t" \
    "buffer_load_dwordx4 v[80:83], v108, s[44:47], s70 offen offset:0 sc1\n\t" \
    "buffer_load_dwordx4 v[84:87], v108, s[44:47], s70 offen offset:1024 sc1\n\t" \
    "buffer_load_dwordx4 v[88:91], v108, s[44:47], s70 offen offset:2048 sc1\n\t" \
    "buffer_load_dwordx4 v[92:95], v108, s[44:47], s70 offen offset:3072 sc1\n\t" \
    "s_bitset1_b32 s72, 3\n\t" \
    "L_sp13_%=:\n\t" \
    "s_bitcmp1_b32 s72, 1\n\t" \
    "s_cbranch_scc1 L_h1_%=\n\t" \
    "s_sleep 4\n\t" \
    "s_branch L_w1_%=\n\t" \
    "L_h1_%=:\n\t" \
    "s_cmp_eq_u32 s73, 1\n\t" \
    "s_cbranch_scc1 L_f1_%=\n\t" \
    "s_waitcnt vmcnt(0)\n\t" \
    "L_f1_%=:\n\t" \
    "s_waitcnt vmcnt(8)\n\t" \
    "v_max3_u32 v112, v48, v49, v50\n\t" \
    "v_max3_u32 v112, v112, v51, v52\n\t" \
    "v_max3_u32 v112, v112, v53, v54\n\t" \
    "v_max3_u32 v112, v112, v55, v56\n\t" \
    "v_max3_u32 v112, v112, v57, v58\n\t" \
    "v_max3_u32 v112, v112, v59, v60\n\t" \
    "v_max3_u32 v112, v112, v61, v62\n\t" \
    "v_max_u32 v112, v112, v63\n\t" \
    "v_cmp_eq_u32 vcc, -1, v112\n\t" \
    "s_cbranch_vccz L_ok1_%=\n\t" \
    "s_bitset0_b32 s72, 1\n\t" \
    "s_branch L_w1_%=\n\t" \
    "L_ok1_%=:\n\t" \
    "v_mfma_f32_32x32x16_bf16 v[0:15], v[136:139], v[48:51], v[0:15]\n\t" \
    "v_mfma_f32_32x32x16_bf16 v[0:15], v[140:143], v[52:55], v[0:15]\n\t" \
    "v_mfma_f32_32x32x16_bf16 v[0:15], v[144:147], v[56:59], v[0:15]\n\t" \
    "v_mfma_f32_32x32x16_bf16 v[0:15], v[148:151], v[60:63], v[0:15]\n\t" \
    "L_w2_%=:\n\t" \
    "s_bitcmp1_b32 s72, 2\n\t" \
    "s_cbranch_scc1 L_h2_%=\n\t" \
    "s_mov_b32 s73, 0\n\t" \
    "s_add_u32 s71, s71, 1\n\t" \
    "s_cmp_ge_u32 s71, 0x200000\n\t" \
    "s_cbranch_scc1 L_ok2_%=\n\t" \
    "buffer_load_dword v115, v113, s[44:47], s81 offen sc1\n\t" \
    "s_waitcnt vmcnt(0)\n\t" \
    "v_cmp_ne_u32 vcc, -1, v115\n\t" \
    "s_and_b32 s74, vcc_lo, 0xffff\n\t" \
    "s_bitcmp1_b32 s72, 2\n\t" \
    "s_cbranch_scc1 L_sp22_%=\n\t" \
    "s_bfe_u32 s68, s74, 0x40008\n\t" \
    "s_cmp_eq_u32 s68, 15\n\t" \
    "s_cbranch_scc0 L_sp22_%=\n\t" \
    "s_add_u32 s70, s81, 8192\n\t" \
    "buffer_load_dwordx4 v[64:67], v108, s[44:47], s70 offen offset:0 sc1\n\t" \
    "buffer_load_dwordx4 v[68:71], v108, s[44:47], s70 offen offset:1024 sc1\n\t" \
    "buffer_load_dwordx4 v[72:75], v108, s[44:47], s70 offen offset:2048 sc1\n\t" \
    "buffer_load_dwordx4 v[76:79], v108, s[44:47], s70 offen offset:3072 sc1\n\t" \
    "s_bitset1_b32 s72, 2\n\t" \
    "L_sp22_%=:\n\t" \
    "s_bitcmp1_b32 s72, 3\n\t" \
    "s_cbranch_scc1 L_sp23_%=\n\t" \
    "s_bfe_u32 s68, s74, 0x4000c\n\t" \
    "s_cmp_eq_u32 s68, 15\n\t" \
    "s_cbranch_scc0 L_sp23_%=\n\t" \
    "s_add_u32 s70, s81, 12288\n\t" \
    "buffer_load_dwordx4 v[80:83], v108, s[44:47], s70 offen offset:0 sc1\n\t" \
    "buffer_load_dwordx4 v[84:87], v108, s[44:47], s70 offen offset:1024 sc1\n\t" \
    "buffer_load_dwordx4 v[88:91], v108, s[44:47], s70 offen offset:2048 sc1\n\t" \
    "buffer_load_dwordx4 v[92:95], v108, s[44:47], s70 offen offset:3072 sc1\n\t" \
    "s_bitset1_b32 s72, 3\n\t" \
    "L_sp23_%=:\n\t" \
    "s_bitcmp1_b32 s72, 2\n\t" \
    "s_cbranch_scc1 L_h2_%=\n\t" \
    "s_sleep 4\n\t" \
    "s_branch L_w2_%=\n\t" \
    "L_h2_%=:\n\t" \
    "s_cmp_eq_u32 s73, 1\n\t" \
    "s_cbranch_scc1 L_f2_%=\n\t" \
    "s_waitcnt vmcnt(0)\n\t" \
    "L_f2_%=:\n\t" \
    "s_waitcnt vmcnt(4)\n\t" \
    "v_max3_u32 v112, v64, v65, v66\n\t" \
    "v_max3_u32 v112, v112, v67, v68\n\t" \
    "v_max3_u32 v112, v112, v69, v70\n\t" \
    "v_max3_u32 v112, v112, v71, v72\n\t" \
    "v_max3_u32 v112, v112, v73, v74\n\t" \
    "v_max3_u32 v112, v112, v75, v76\n\t" \
    "v_max3_u32 v112, v112, v77, v78\n\t" \
    "v_max_u32 v112, v112, v79\n\t" \
    "v_cmp_eq_u32 vcc, -1, v112\n\t" \
    "s_cbranch_vccz L_ok2_%=\n\t" \
    "s_bitset0_b32 s72, 2\n\t" \
    "s_branch L_w2_%=\n\t" \
    "L_ok2_%=:\n\t" \
    "v_mfma_f32_32x32x16_bf16 v[0:15], v[152:155], v[64:67], v[0:15]\n\t" \
    "v_mfma_f32_32x32x16_bf16 v[0:15], v[156:159], v[68:71], v[0:15]\n\t" \
    "v_mfma_f32_32x32x16_bf16 v[0:15], v[160:163], v[72:75], v[0:15]\n\t" \
    "v_mfma_f32_32x32x16_bf16 v[0:15], v[164:167], v[76:79], v[0:15]\n\t" \
    "L_w3_%=:\n\t" \
    "s_bitcmp1_b32 s72, 3\n\t" \
    "s_cbranch_scc1 L_h3_%=\n\t" \
    "s_mov_b32 s73, 0\n\t" \
    "s_add_u32 s71, s71, 1\n\t" \
    "s_cmp_ge_u32 s71, 0x200000\n\t" \
    "s_cbranch_scc1 L_ok3_%=\n\t" \
    "buffer_load_dword v115, v113, s[44:47], s81 offen sc1\n\t" \
    "s_waitcnt vmcnt(0)\n\t" \
    "v_cmp_ne_u32 vcc, -1, v115\n\t" \
    "s_and_b32 s74, vcc_lo, 0xffff\n\t" \
    "s_bitcmp1_b32 s72, 3\n\t" \
    "s_cbranch_scc1 L_sp33_%=\n\t" \
    "s_bfe_u32 s68, s74, 0x4000c\n\t" \
    "s_cmp_eq_u32 s68, 15\n\t" \
    "s_cbranch_scc0 L_sp33_%=\n\t" \
    "s_add_u32 s70, s81, 12288\n\t" \
    "buffer_load_dwordx4 v[80:83], v108, s[44:47], s70 offen offset:0 sc1\n\t" \
    "buffer_load_dwordx4 v[84:87], v108, s[44:47], s70 offen offset:1024 sc1\n\t" \
    "buffer_load_dwordx4 v[88:91], v108, s[44:47], s70 offen offset:2048 sc1\n\t" \
    "buffer_load_dwordx4 v[92:95], v108, s[44:47], s70 offen offset:3072 sc1\n\t" \
    "s_bitset1_b32 s72, 3\n\t" \
    "L_sp33_%=:\n\t" \
    "s_bitcmp1_b32 s72, 3\n\t" \
    "s_cbranch_scc1 L_h3_%=\n\t" \
    "s_sleep 4\n\t" \
    "s_branch L_w3_%=\n\t" \
    "L_h3_%=:\n\t" \
    "s_cmp_eq_u32 s73, 1\n\t" \
    "s_cbranch_scc1 L_f3_%=\n\t" \
    "s_waitcnt vmcnt(0)\n\t" \
    "L_f3_%=:\n\t" \
    "s_waitcnt vmcnt(0)\n\t" \
    "v_max3_u32 v112, v80, v81, v82\n\t" \
    "v_max3_u32 v112, v112, v83, v84\n\t" \
    "v_max3_u32 v112, v112, v85, v86\n\t" \
    "v_max3_u32 v112, v112, v87, v88\n\t" \
    "v_max3_u32 v112, v112, v89, v90\n\t" \
    "v_max3_u32 v112, v112, v91, v92\n\t" \
    "v_max3_u32 v112, v112, v93, v94\n\t" \
    "v_max_u32 v112, v112, v95\n\t" \
    "v_cmp_eq_u32 vcc, -1, v112\n\t" \
    "s_cbranch_vccz L_ok3_%=\n\t" \
    "s_bitset0_b32 s72, 3\n\t" \
    "s_branch L_w3_%=\n\t" \
    "L_ok3_%=:\n\t" \
    "v_mfma_f32_32x32x16_bf16 v[0:15], v[16:19], v[80:83], v[0:15]\n\t" \
    "v_mfma_f32_32x32x16_bf16 v[0:15], v[20:23], v[84:87], v[0:15]\n\t" \
    "v_mfma_f32_32x32x16_bf16 v[0:15], v[24:27], v[88:91], v[0:15]\n\t" \
    "v_mfma_f32_32x32x16_bf16 v[0:15], v[28:31], v[92:95], v[0:15]\n\t" \
    "v_add_u32 v110, s86, v108\n\t" \
    "v_add_u32 v111, s87, v108\n\t" \
    "v_lshlrev_b32 v32, 16, v104\n\t" \
    "v_and_b32 v33, 0xffff0000, v104\n\t" \
    "v_lshlrev_b32 v34, 16, v105\n\t" \
    "v_and_b32 v35, 0xffff0000, v105\n\t" \
    "v_lshlrev_b32 v36, 16, v106\n\t" \
    "v_and_b32 v37, 0xffff0000, v106\n\t" \
    "v_lshlrev_b32 v38, 16, v107\n\t" \
    "v_and_b32 v39, 0xffff0000, v107\n\t" \
    "s_nop 3\n\t" \
    "s_cmp_eq_u32 s85, 0\n\t" \
    "s_cbranch_scc1 L_kh0_%=\n\t" \
    "ds_write_b128 v110, v[0:3]\n\t" \
    "ds_write_b128 v110, v[4:7] offset:1024\n\t" \
    "s_branch L_khd_%=\n\t" \
    "L_kh0_%=:\n\t" \
    "ds_write_b128 v110, v[8:11]\n\t" \
    "ds_write_b128 v110, v[12:15] offset:1024\n\t" \
    "L_khd_%=:\n\t" \
    "s_waitcnt lgkmcnt(0)\n\t" \
    "s_barrier\n\t" \
    "ds_read_b128 v[40:43], v111\n\t" \
    "ds_read_b128 v[44:47], v111 offset:1024\n\t" \
    "s_cmp_eq_u32 s85, 0\n\t" \
    "s_cbranch_scc1 L_kh1_%=\n\t" \
    "v_mov_b32 v0, v8\n\t" \
    "v_mov_b32 v1, v9\n\t" \
    "v_mov_b32 v2, v10\n\t" \
    "v_mov_b32 v3, v11\n\t" \
    "v_mov_b32 v4, v12\n\t" \
    "v_mov_b32 v5, v13\n\t" \
    "v_mov_b32 v6, v14\n\t" \
    "v_mov_b32 v7, v15\n\t" \
    "L_kh1_%=:\n\t" \
    "s_waitcnt lgkmcnt(0)\n\t" \
    "v_add_f32 v0, v0, v40\n\t" \
    "v_add_f32 v1, v1, v41\n\t" \
    "v_add_f32 v2, v2, v42\n\t" \
    "v_add_f32 v3, v3, v43\n\t" \
    "v_add_f32 v4, v4, v44\n\t" \
    "v_add_f32 v5, v5, v45\n\t" \
    "v_add_f32 v6, v6, v46\n\t" \
    "v_add_f32 v7, v7, v47\n\t" \
    "v_fma_f32 v0, v0, v96, v32\n\t" \
    "v_fma_f32 v1, v1, v97, v33\n\t" \
    "v_fma_f32 v2, v2, v98, v34\n\t" \
    "v_fma_f32 v3, v3, v99, v35\n\t" \
    "v_fma_f32 v4, v4, v100, v36\n\t" \
    "v_fma_f32 v5, v5, v101, v37\n\t" \
    "v_fma_f32 v6, v6, v102, v38\n\t" \
    "v_fma_f32 v7, v7, v103, v39\n\t" \
    "v_cvt_pk_bf16_f32 v8, v0, v1\n\t" \
    "v_cvt_pk_bf16_f32 v9, v2, v3\n\t" \
    "v_cvt_pk_bf16_f32 v10, v4, v5\n\t" \
    "v_cvt_pk_bf16_f32 v11, v6, v7\n\t" \
    "buffer_store_dwordx2 v[8:9], v109, s[92:95], 0 offen\n\t" \
    "buffer_store_dwordx2 v[10:11], v109, s[92:95], 0 offen offset:16\n\t" \
    ""
#define G2_ASM \
    "s_setprio 3\n\t" \
    "s_mov_b32 s68, s48\n\t" \
    "s_add_u32 s69, s48, 114688\n\t" \
    "s_mov_b32 s70, s49\n\t" \
    "s_add_u32 s71, s49, 114688\n\t" \
    "buffer_load_dwordx4 v[64:67], v160, s[44:47], s68 offen offset:0 sc1\n\t" \
    "buffer_load_dwordx4 v[68:71], v160, s[44:47], s69 offen offset:0 sc1\n\t" \
    "buffer_load_dwordx4 v[72:75], v160, s[44:47], s70 offen offset:0 sc1\n\t" \
    "buffer_load_dwordx4 v[76:79], v160, s[44:47], s71 offen offset:0 sc1\n\t" \
    "buffer_load_dwordx4 v[80:83], v160, s[44:47], s68 offen offset:1024 sc1\n\t" \
    "buffer_load_dwordx4 v[84:87], v160, s[44:47], s69 offen offset:1024 sc1\n\t" \
    "buffer_load_dwordx4 v[88:91], v160, s[44:47], s70 offen offset:1024 sc1\n\t" \
    "buffer_load_dwordx4 v[92:95], v160, s[44:47], s71 offen offset:1024 sc1\n\t" \
    "buffer_load_dwordx4 v[96:99], v160, s[44:47], s68 offen offset:2048 sc1\n\t" \
    "buffer_load_dwordx4 v[100:103], v160, s[44:47], s69 offen offset:2048 sc1\n\t" \
    "buffer_load_dwordx4 v[104:107], v160, s[44:47], s70 offen offset:2048 sc1\n\t" \
    "buffer_load_dwordx4 v[108:111], v160, s[44:47], s71 offen offset:2048 sc1\n\t" \
    "buffer_load_dwordx4 v[112:115], v160, s[44:47], s68 offen offset:3072 sc1\n\t" \
    "buffer_load_dwordx4 v[116:119], v160, s[44:47], s69 offen offset:3072 sc1\n\t" \
    "buffer_load_dwordx4 v[120:123], v160, s[44:47], s70 offen offset:3072 sc1\n\t" \
    "buffer_load_dwordx4 v[124:127], v160, s[44:47], s71 offen offset:3072 sc1\n\t" \
    "s_add_u32 s68, s68, 4096\n\t" \
    "s_add_u32 s69, s69, 4096\n\t" \
    "s_add_u32 s70, s70, 4096\n\t" \
    "s_add_u32 s71, s71, 4096\n\t" \
    "buffer_load_dwordx4 v[128:131], v160, s[44:47], s68 offen offset:0 sc1\n\t" \
    "buffer_load_dwordx4 v[132:135], v160, s[44:47], s69 offen offset:0 sc1\n\t" \
    "buffer_load_dwordx4 v[136:139], v160, s[44:47], s70 offen offset:0 sc1\n\t" \
    "buffer_load_dwordx4 v[140:143], v160, s[44:47], s71 offen offset:0 sc1\n\t" \
    "buffer_load_dwordx4 v[144:147], v160, s[44:47], s68 offen offset:1024 sc1\n\t" \
    "buffer_load_dwordx4 v[148:151], v160, s[44:47], s69 offen offset:1024 sc1\n\t" \
    "buffer_load_dwordx4 v[152:155], v160, s[44:47], s70 offen offset:1024 sc1\n\t" \
    "buffer_load_dwordx4 v[156:159], v160, s[44:47], s71 offen offset:1024 sc1\n\t" \
    "s_waitcnt vmcnt(21)\n\t" \
    "v_mfma_f32_32x32x16_bf16 v[0:15], v[72:75], v[64:67], 0\n\t" \
    "s_sleep 2\n\t" \
    "s_waitcnt vmcnt(20)\n\t" \
    "v_mfma_f32_32x32x16_bf16 v[16:31], v[76:79], v[64:67], 0\n\t" \
    "s_sleep 2\n\t" \
    "v_mfma_f32_32x32x16_bf16 v[32:47], v[72:75], v[68:71], 0\n\t" \
    "s_sleep 2\n\t" \
    "v_mfma_f32_32x32x16_bf16 v[48:63], v[76:79], v[68:71], 0\n\t" \
    "buffer_load_dwordx4 v[64:67], v160, s[44:47], s68 offen offset:2048 sc1\n\t" \
    "buffer_load_dwordx4 v[68:71], v160, s[44:47], s69 offen offset:2048 sc1\n\t" \
    "buffer_load_dwordx4 v[72:75], v160, s[44:47], s70 offen offset:2048 sc1\n\t" \
    "buffer_load_dwordx4 v[76:79], v160, s[44:47], s71 offen offset:2048 sc1\n\t" \
    "s_sleep 2\n\t" \
    "s_waitcnt vmcnt(21)\n\t" \
    "v_mfma_f32_32x32x16_bf16 v[0:15], v[88:91], v[80:83], v[0:15]\n\t" \
    "s_sleep 2\n\t" \
    "s_waitcnt vmcnt(20)\n\t" \
    "v_mfma_f32_32x32x16_bf16 v[16:31], v[92:95], v[80:83], v[16:31]\n\t" \
    "s_sleep 2\n\t" \
    "v_mfma_f32_32x32x16_bf16 v[32:47], v[88:91], v[84:87], v[32:47]\n\t" \
    "s_sleep 2\n\t" \
    "v_mfma_f32_32x32x16_bf16 v[48:63], v[92:95], v[84:87], v[48:63]\n\t" \
    "buffer_load_dwordx4 v[80:83], v160, s[44:47], s68 offen offset:3072 sc1\n\t" \
    "buffer_load_dwordx4 v[84:87], v160, s[44:47], s69 offen offset:3072 sc1\n\t" \
    "buffer_load_dwordx4 v[88:91], v160, s[44:47], s70 offen offset:3072 sc1\n\t" \
    "buffer_load_dwordx4 v[92:95], v160, s[44:47], s71 offen offset:3072 sc1\n\t" \
    "s_sleep 2\n\t" \
    "s_waitcnt vmcnt(21)\n\t" \
    "v_mfma_f32_32x32x16_bf16 v[0:15], v[104:107], v[96:99], v[0:15]\n\t" \
    "s_sleep 2\n\t" \
    "s_waitcnt vmcnt(20)\n\t" \
    "v_mfma_f32_32x32x16_bf16 v[16:31], v[108:111], v[96:99], v[16:31]\n\t" \
    "s_sleep 2\n\t" \
    "v_mfma_f32_32x32x16_bf16 v[32:47], v[104:107], v[100:103], v[32:47]\n\t" \
    "s_sleep 2\n\t" \
    "v_mfma_f32_32x32x16_bf16 v[48:63], v[108:111], v[100:103], v[48:63]\n\t" \
    "s_add_u32 s68, s68, 4096\n\t" \
    "s_add_u32 s69, s69, 4096\n\t" \
    "s_add_u32 s70, s70, 4096\n\t" \
    "s_add_u32 s71, s71, 4096\n\t" \
    "buffer_load_dwordx4 v[96:99], v160, s[44:47], s68 offen offset:0 sc1\n\t" \
    "buffer_load_dwordx4 v[100:103], v160, s[44:47], s69 offen offset:0 sc1\n\t" \
    "buffer_load_dwordx4 v[104:107], v160, s[44:47], s70 offen offset:0 sc1\n\t" \
    "buffer_load_dwordx4 v[108:111], v160, s[44:47], s71 offen offset:0 sc1\n\t" \
    "s_sleep 2\n\t" \
    "s_waitcnt vmcnt(21)\n\t" \
    "v_mfma_f32_32x32x16_bf16 v[0:15], v[120:123], v[112:115], v[0:15]\n\t" \
    "s_sleep 2\n\t" \
    "s_waitcnt vmcnt(20)\n\t" \
    "v_mfma_f32_32x32x16_bf16 v[16:31], v[124:127], v[112:115], v[16:31]\n\t" \
    "s_sleep 2\n\t" \
    "v_mfma_f32_32x32x16_bf16 v[32:47], v[120:123], v[116:119], v[32:47]\n\t" \
    "s_sleep 2\n\t" \
    "v_mfma_f32_32x32x16_bf16 v[48:63], v[124:127], v[116:119], v[48:63]\n\t" \
    "buffer_load_dwordx4 v[112:115], v160, s[44:47], s68 offen offset:1024 sc1\n\t" \
    "buffer_load_dwordx4 v[116:119], v160, s[44:47], s69 offen offset:1024 sc1\n\t" \
    "buffer_load_dwordx4 v[120:123], v160, s[44:47], s70 offen offset:1024 sc1\n\t" \
    "buffer_load_dwordx4 v[124:127], v160, s[44:47], s71 offen offset:1024 sc1\n\t" \
    "s_sleep 2\n\t" \
    "s_waitcnt vmcnt(21)\n\t" \
    "v_mfma_f32_32x32x16_bf16 v[0:15], v[136:139], v[128:131], v[0:15]\n\t" \
    "s_sleep 2\n\t" \
    "s_waitcnt vmcnt(20)\n\t" \
    "v_mfma_f32_32x32x16_bf16 v[16:31], v[140:143], v[128:131], v[16:31]\n\t" \
    "s_sleep 2\n\t" \
    "v_mfma_f32_32x32x16_bf16 v[32:47], v[136:139], v[132:135], v[32:47]\n\t" \
    "s_sleep 2\n\t" \
    "v_mfma_f32_32x32x16_bf16 v[48:63], v[140:143], v[132:135], v[48:63]\n\t" \
    "buffer_load_dwordx4 v[128:131], v160, s[44:47], s68 offen offset:2048 sc1\n\t" \
    "buffer_load_dwordx4 v[132:135], v160, s[44:47], s69 offen offset:2048 sc1\n\t" \
    "buffer_load_dwordx4 v[136:139], v160, s[44:47], s70 offen offset:2048 sc1\n\t" \
    "buffer_load_dwordx4 v[140:143], v160, s[44:47], s71 offen offset:2048 sc1\n\t" \
    "s_sleep 2\n\t" \
    "s_waitcnt vmcnt(21)\n\t" \
    "v_mfma_f32_32x32x16_bf16 v[0:15], v[152:155], v[144:147], v[0:15]\n\t" \
    "s_sleep 2\n\t" \
    "s_waitcnt vmcnt(20)\n\t" \
    "v_mfma_f32_32x32x16_bf16 v[16:31], v[156:159], v[144:147], v[16:31]\n\t" \
    "s_sleep 2\n\t" \
    "v_mfma_f32_32x32x16_bf16 v[32:47], v[152:155], v[148:151], v[32:47]\n\t" \
    "s_sleep 2\n\t" \
    "v_mfma_f32_32x32x16_bf16 v[48:63], v[156:159], v[148:151], v[48:63]\n\t" \
    "buffer_load_dwordx4 v[144:147], v160, s[44:47], s68 offen offset:3072 sc1\n\t" \
    "buffer_load_dwordx4 v[148:151], v160, s[44:47], s69 offen offset:3072 sc1\n\t" \
    "buffer_load_dwordx4 v[152:155], v160, s[44:47], s70 offen offset:3072 sc1\n\t" \
    "buffer_load_dwordx4 v[156:159], v160, s[44:47], s71 offen offset:3072 sc1\n\t" \
    "s_sleep 2\n\t" \
    "s_waitcnt vmcnt(21)\n\t" \
    "v_mfma_f32_32x32x16_bf16 v[0:15], v[72:75], v[64:67], v[0:15]\n\t" \
    "s_sleep 2\n\t" \
    "s_waitcnt vmcnt(20)\n\t" \
    "v_mfma_f32_32x32x16_bf16 v[16:31], v[76:79], v[64:67], v[16:31]\n\t" \
    "s_sleep 2\n\t" \
    "v_mfma_f32_32x32x16_bf16 v[32:47], v[72:75], v[68:71], v[32:47]\n\t" \
    "s_sleep 2\n\t" \
    "v_mfma_f32_32x32x16_bf16 v[48:63], v[76:79], v[68:71], v[48:63]\n\t" \
    "s_add_u32 s68, s68, 4096\n\t" \
    "s_add_u32 s69, s69, 4096\n\t" \
    "s_add_u32 s70, s70, 4096\n\t" \
    "s_add_u32 s71, s71, 4096\n\t" \
    "buffer_load_dwordx4 v[64:67], v160, s[44:47], s68 offen offset:0 sc1\n\t" \
    "buffer_load_dwordx4 v[68:71], v160, s[44:47], s69 offen offset:0 sc1\n\t" \
    "buffer_load_dwordx4 v[72:75], v160, s[44:47], s70 offen offset:0 sc1\n\t" \
    "buffer_load_dwordx4 v[76:79], v160, s[44:47], s71 offen offset:0 sc1\n\t" \
    "s_sleep 2\n\t" \
    "s_waitcnt vmcnt(21)\n\t" \
    "v_mfma_f32_32x32x16_bf16 v[0:15], v[88:91], v[80:83], v[0:15]\n\t" \
    "s_sleep 2\n\t" \
    "s_waitcnt vmcnt(20)\n\t" \
    "v_mfma_f32_32x32x16_bf16 v[16:31], v[92:95], v[80:83], v[16:31]\n\t" \
    "s_sleep 2\n\t" \
    "v_mfma_f32_32x32x16_bf16 v[32:47], v[88:91], v[84:87], v[32:47]\n\t" \
    "s_sleep 2\n\t" \
    "v_mfma_f32_32x32x16_bf16 v[48:63], v[92:95], v[84:87], v[48:63]\n\t" \
    "buffer_load_dwordx4 v[80:83], v160, s[44:47], s68 offen offset:1024 sc1\n\t" \
    "buffer_load_dwordx4 v[84:87], v160, s[44:47], s69 offen offset:1024 sc1\n\t" \
    "buffer_load_dwordx4 v[88:91], v160, s[44:47], s70 offen offset:1024 sc1\n\t" \
    "buffer_load_dwordx4 v[92:95], v160, s[44:47], s71 offen offset:1024 sc1\n\t" \
    "s_sleep 2\n\t" \
    "s_waitcnt vmcnt(21)\n\t" \
    "v_mfma_f32_32x32x16_bf16 v[0:15], v[104:107], v[96:99], v[0:15]\n\t" \
    "s_sleep 2\n\t" \
    "s_waitcnt vmcnt(20)\n\t" \
    "v_mfma_f32_32x32x16_bf16 v[16:31], v[108:111], v[96:99], v[16:31]\n\t" \
    "s_sleep 2\n\t" \
    "v_mfma_f32_32x32x16_bf16 v[32:47], v[104:107], v[100:103], v[32:47]\n\t" \
    "s_sleep 2\n\t" \
    "v_mfma_f32_32x32x16_bf16 v[48:63], v[108:111], v[100:103], v[48:63]\n\t" \
    "buffer_load_dwordx4 v[96:99], v160, s[44:47], s68 offen offset:2048 sc1\n\t" \
    "buffer_load_dwordx4 v[100:103], v160, s[44:47], s69 offen offset:2048 sc1\n\t" \
    "buffer_load_dwordx4 v[104:107], v160, s[44:47], s70 offen offset:2048 sc1\n\t" \
    "buffer_load_dwordx4 v[108:111], v160, s[44:47], s71 offen offset:2048 sc1\n\t" \
    "s_sleep 2\n\t" \
    "s_waitcnt vmcnt(21)\n\t" \
    "v_mfma_f32_32x32x16_bf16 v[0:15], v[120:123], v[112:115], v[0:15]\n\t" \
    "s_sleep 2\n\t" \
    "s_waitcnt vmcnt(20)\n\t" \
    "v_mfma_f32_32x32x16_bf16 v[16:31], v[124:127], v[112:115], v[16:31]\n\t" \
    "s_sleep 2\n\t" \
    "v_mfma_f32_32x32x16_bf16 v[32:47], v[120:123], v[116:119], v[32:47]\n\t" \
    "s_sleep 2\n\t" \
    "v_mfma_f32_32x32x16_bf16 v[48:63], v[124:127], v[116:119], v[48:63]\n\t" \
    "buffer_load_dwordx4 v[112:115], v160, s[44:47], s68 offen offset:3072 sc1\n\t" \
    "buffer_load_dwordx4 v[116:119], v160, s[44:47], s69 offen offset:3072 sc1\n\t" \
    "buffer_load_dwordx4 v[120:123], v160, s[44:47], s70 offen offset:3072 sc1\n\t" \
    "buffer_load_dwordx4 v[124:127], v160, s[44:47], s71 offen offset:3072 sc1\n\t" \
    "s_sleep 2\n\t" \
    "s_waitcnt vmcnt(21)\n\t" \
    "v_mfma_f32_32x32x16_bf16 v[0:15], v[136:139], v[128:131], v[0:15]\n\t" \
    "s_sleep 2\n\t" \
    "s_waitcnt vmcnt(20)\n\t" \
    "v_mfma_f32_32x32x16_bf16 v[16:31], v[140:143], v[128:131], v[16:31]\n\t" \
    "s_sleep 2\n\t" \
    "v_mfma_f32_32x32x16_bf16 v[32:47], v[136:139], v[132:135], v[32:47]\n\t" \
    "s_sleep 2\n\t" \
    "v_mfma_f32_32x32x16_bf16 v[48:63], v[140:143], v[132:135], v[48:63]\n\t" \
    "s_add_u32 s68, s68, 4096\n\t" \
    "s_add_u32 s69, s69, 4096\n\t" \
    "s_add_u32 s70, s70, 4096\n\t" \
    "s_add_u32 s71, s71, 4096\n\t" \
    "buffer_load_dwordx4 v[128:131], v160, s[44:47], s68 offen offset:0 sc1\n\t" \
    "buffer_load_dwordx4 v[132:135], v160, s[44:47], s69 offen offset:0 sc1\n\t" \
    "buffer_load_dwordx4 v[136:139], v160, s[44:47], s70 offen offset:0 sc1\n\t" \
    "buffer_load_dwordx4 v[140:143], v160, s[44:47], s71 offen offset:0 sc1\n\t" \
    "s_sleep 2\n\t" \
    "s_waitcnt vmcnt(21)\n\t" \
    "v_mfma_f32_32x32x16_bf16 v[0:15], v[152:155], v[144:147], v[0:15]\n\t" \
    "s_sleep 2\n\t" \
    "s_waitcnt vmcnt(20)\n\t" \
    "v_mfma_f32_32x32x16_bf16 v[16:31], v[156:159], v[144:147], v[16:31]\n\t" \
    "s_sleep 2\n\t" \
    "v_mfma_f32_32x32x16_bf16 v[32:47], v[152:155], v[148:151], v[32:47]\n\t" \
    "s_sleep 2\n\t" \
    "v_mfma_f32_32x32x16_bf16 v[48:63], v[156:159], v[148:151], v[48:63]\n\t" \
    "buffer_load_dwordx4 v[144:147], v160, s[44:47], s68 offen offset:1024 sc1\n\t" \
    "buffer_load_dwordx4 v[148:151], v160, s[44:47], s69 offen offset:1024 sc1\n\t" \
    "buffer_load_dwordx4 v[152:155], v160, s[44:47], s70 offen offset:1024 sc1\n\t" \
    "buffer_load_dwordx4 v[156:159], v160, s[44:47], s71 offen offset:1024 sc1\n\t" \
    "s_sleep 2\n\t" \
    "s_waitcnt vmcnt(21)\n\t" \
    "v_mfma_f32_32x32x16_bf16 v[0:15], v[72:75], v[64:67], v[0:15]\n\t" \
    "s_sleep 2\n\t" \
    "s_waitcnt vmcnt(20)\n\t" \
    "v_mfma_f32_32x32x16_bf16 v[16:31], v[76:79], v[64:67], v[16:31]\n\t" \
    "s_sleep 2\n\t" \
    "v_mfma_f32_32x32x16_bf16 v[32:47], v[72:75], v[68:71], v[32:47]\n\t" \
    "s_sleep 2\n\t" \
    "v_mfma_f32_32x32x16_bf16 v[48:63], v[76:79], v[68:71], v[48:63]\n\t" \
    "buffer_load_dwordx4 v[64:67], v160, s[44:47], s68 offen offset:2048 sc1\n\t" \
    "buffer_load_dwordx4 v[68:71], v160, s[44:47], s69 offen offset:2048 sc1\n\t" \
    "buffer_load_dwordx4 v[72:75], v160, s[44:47], s70 offen offset:2048 sc1\n\t" \
    "buffer_load_dwordx4 v[76:79], v160, s[44:47], s71 offen offset:2048 sc1\n\t" \
    "s_sleep 2\n\t" \
    "s_waitcnt vmcnt(21)\n\t" \
    "v_mfma_f32_32x32x16_bf16 v[0:15], v[88:91], v[80:83], v[0:15]\n\t" \
    "s_sleep 2\n\t" \
    "s_waitcnt vmcnt(20)\n\t" \
    "v_mfma_f32_32x32x16_bf16 v[16:31], v[92:95], v[80:83], v[16:31]\n\t" \
    "s_sleep 2\n\t" \
    "v_mfma_f32_32x32x16_bf16 v[32:47], v[88:91], v[84:87], v[32:47]\n\t" \
    "s_sleep 2\n\t" \
    "v_mfma_f32_32x32x16_bf16 v[48:63], v[92:95], v[84:87], v[48:63]\n\t" \
    "buffer_load_dwordx4 v[80:83], v160, s[44:47], s68 offen offset:3072 sc1\n\t" \
    "buffer_load_dwordx4 v[84:87], v160, s[44:47], s69 offen offset:3072 sc1\n\t" \
    "buffer_load_dwordx4 v[88:91], v160, s[44:47], s70 offen offset:3072 sc1\n\t" \
    "buffer_load_dwordx4 v[92:95], v160, s[44:47], s71 offen offset:3072 sc1\n\t" \
    "s_sleep 2\n\t" \
    "s_waitcnt vmcnt(21)\n\t" \
    "v_mfma_f32_32x32x16_bf16 v[0:15], v[104:107], v[96:99], v[0:15]\n\t" \
    "s_sleep 2\n\t" \
    "s_waitcnt vmcnt(20)\n\t" \
    "v_mfma_f32_32x32x16_bf16 v[16:31], v[108:111], v[96:99], v[16:31]\n\t" \
    "s_sleep 2\n\t" \
    "v_mfma_f32_32x32x16_bf16 v[32:47], v[104:107], v[100:103], v[32:47]\n\t" \
    "s_sleep 2\n\t" \
    "v_mfma_f32_32x32x16_bf16 v[48:63], v[108:111], v[100:103], v[48:63]\n\t" \
    "s_add_u32 s68, s68, 4096\n\t" \
    "s_add_u32 s69, s69, 4096\n\t" \
    "s_add_u32 s70, s70, 4096\n\t" \
    "s_add_u32 s71, s71, 4096\n\t" \
    "buffer_load_dwordx4 v[96:99], v160, s[44:47], s68 offen offset:0 sc1\n\t" \
    "buffer_load_dwordx4 v[100:103], v160, s[44:47], s69 offen offset:0 sc1\n\t" \
    "buffer_load_dwordx4 v[104:107], v160, s[44:47], s70 offen offset:0 sc1\n\t" \
    "buffer_load_dwordx4 v[108:111], v160, s[44:47], s71 offen offset:0 sc1\n\t" \
    "s_sleep 2\n\t" \
    "s_waitcnt vmcnt(21)\n\t" \
    "v_mfma_f32_32x32x16_bf16 v[0:15], v[120:123], v[112:115], v[0:15]\n\t" \
    "s_sleep 2\n\t" \
    "s_waitcnt vmcnt(20)\n\t" \
    "v_mfma_f32_32x32x16_bf16 v[16:31], v[124:127], v[112:115], v[16:31]\n\t" \
    "s_sleep 2\n\t" \
    "v_mfma_f32_32x32x16_bf16 v[32:47], v[120:123], v[116:119], v[32:47]\n\t" \
    "s_sleep 2\n\t" \
    "v_mfma_f32_32x32x16_bf16 v[48:63], v[124:127], v[116:119], v[48:63]\n\t" \
    "buffer_load_dwordx4 v[112:115], v160, s[44:47], s68 offen offset:1024 sc1\n\t" \
    "buffer_load_dwordx4 v[116:119], v160, s[44:47], s69 offen offset:1024 sc1\n\t" \
    "buffer_load_dwordx4 v[120:123], v160, s[44:47], s70 offen offset:1024 sc1\n\t" \
    "buffer_load_dwordx4 v[124:127], v160, s[44:47], s71 offen offset:1024 sc1\n\t" \
    "s_sleep 2\n\t" \
    "s_waitcnt vmcnt(21)\n\t" \
    "v_mfma_f32_32x32x16_bf16 v[0:15], v[136:139], v[128:131], v[0:15]\n\t" \
    "s_sleep 2\n\t" \
    "s_waitcnt vmcnt(20)\n\t" \
    "v_mfma_f32_32x32x16_bf16 v[16:31], v[140:143], v[128:131], v[16:31]\n\t" \
    "s_sleep 2\n\t" \
    "v_mfma_f32_32x32x16_bf16 v[32:47], v[136:139], v[132:135], v[32:47]\n\t" \
    "s_sleep 2\n\t" \
    "v_mfma_f32_32x32x16_bf16 v[48:63], v[140:143], v[132:135], v[48:63]\n\t" \
    "buffer_load_dwordx4 v[128:131], v160, s[44:47], s68 offen offset:2048 sc1\n\t" \
    "buffer_load_dwordx4 v[132:135], v160, s[44:47], s69 offen offset:2048 sc1\n\t" \
    "buffer_load_dwordx4 v[136:139], v160, s[44:47], s70 offen offset:2048 sc1\n\t" \
    "buffer_load_dwordx4 v[140:143], v160, s[44:47], s71 offen offset:2048 sc1\n\t" \
    "s_sleep 2\n\t" \
    "s_waitcnt vmcnt(21)\n\t" \
    "v_mfma_f32_32x32x16_bf16 v[0:15], v[152:155], v[144:147], v[0:15]\n\t" \
    "s_sleep 2\n\t" \
    "s_waitcnt vmcnt(20)\n\t" \
    "v_mfma_f32_32x32x16_bf16 v[16:31], v[156:159], v[144:147], v[16:31]\n\t" \
    "s_sleep 2\n\t" \
    "v_mfma_f32_32x32x16_bf16 v[32:47], v[152:155], v[148:151], v[32:47]\n\t" \
    "s_sleep 2\n\t" \
    "v_mfma_f32_32x32x16_bf16 v[48:63], v[156:159], v[148:151], v[48:63]\n\t" \
    "buffer_load_dwordx4 v[144:147], v160, s[44:47], s68 offen offset:3072 sc1\n\t" \
    "buffer_load_dwordx4 v[148:151], v160, s[44:47], s69 offen offset:3072 sc1\n\t" \
    "buffer_load_dwordx4 v[152:155], v160, s[44:47], s70 offen offset:3072 sc1\n\t" \
    "buffer_load_dwordx4 v[156:159], v160, s[44:47], s71 offen offset:3072 sc1\n\t" \
    "s_sleep 2\n\t" \
    "s_waitcnt vmcnt(21)\n\t" \
    "v_mfma_f32_32x32x16_bf16 v[0:15], v[72:75], v[64:67], v[0:15]\n\t" \
    "s_sleep 2\n\t" \
    "s_waitcnt vmcnt(20)\n\t" \
    "v_mfma_f32_32x32x16_bf16 v[16:31], v[76:79], v[64:67], v[16:31]\n\t" \
    "s_sleep 2\n\t" \
    "v_mfma_f32_32x32x16_bf16 v[32:47], v[72:75], v[68:71], v[32:47]\n\t" \
    "s_sleep 2\n\t" \
    "v_mfma_f32_32x32x16_bf16 v[48:63], v[76:79], v[68:71], v[48:63]\n\t" \
    "s_add_u32 s68, s68, 4096\n\t" \
    "s_add_u32 s69, s69, 4096\n\t" \
    "s_add_u32 s70, s70, 4096\n\t" \
    "s_add_u32 s71, s71, 4096\n\t" \
    "buffer_load_dwordx4 v[64:67], v160, s[44:47], s68 offen offset:0 sc1\n\t" \
    "buffer_load_dwordx4 v[68:71], v160, s[44:47], s69 offen offset:0 sc1\n\t" \
    "buffer_load_dwordx4 v[72:75], v160, s[44:47], s70 offen offset:0 sc1\n\t" \
    "buffer_load_dwordx4 v[76:79], v160, s[44:47], s71 offen offset:0 sc1\n\t" \
    "s_sleep 2\n\t" \
    "s_waitcnt vmcnt(21)\n\t" \
    "v_mfma_f32_32x32x16_bf16 v[0:15], v[88:91], v[80:83], v[0:15]\n\t" \
    "s_sleep 2\n\t" \
    "s_waitcnt vmcnt(20)\n\t" \
    "v_mfma_f32_32x32x16_bf16 v[16:31], v[92:95], v[80:83], v[16:31]\n\t" \
    "s_sleep 2\n\t" \
    "v_mfma_f32_32x32x16_bf16 v[32:47], v[88:91], v[84:87], v[32:47]\n\t" \
    "s_sleep 2\n\t" \
    "v_mfma_f32_32x32x16_bf16 v[48:63], v[92:95], v[84:87], v[48:63]\n\t" \
    "buffer_load_dwordx4 v[80:83], v160, s[44:47], s68 offen offset:1024 sc1\n\t" \
    "buffer_load_dwordx4 v[84:87], v160, s[44:47], s69 offen offset:1024 sc1\n\t" \
    "buffer_load_dwordx4 v[88:91], v160, s[44:47], s70 offen offset:1024 sc1\n\t" \
    "buffer_load_dwordx4 v[92:95], v160, s[44:47], s71 offen offset:1024 sc1\n\t" \
    "s_sleep 2\n\t" \
    "s_waitcnt vmcnt(21)\n\t" \
    "v_mfma_f32_32x32x16_bf16 v[0:15], v[104:107], v[96:99], v[0:15]\n\t" \
    "s_sleep 2\n\t" \
    "s_waitcnt vmcnt(20)\n\t" \
    "v_mfma_f32_32x32x16_bf16 v[16:31], v[108:111], v[96:99], v[16:31]\n\t" \
    "s_sleep 2\n\t" \
    "v_mfma_f32_32x32x16_bf16 v[32:47], v[104:107], v[100:103], v[32:47]\n\t" \
    "s_sleep 2\n\t" \
    "v_mfma_f32_32x32x16_bf16 v[48:63], v[108:111], v[100:103], v[48:63]\n\t" \
    "buffer_load_dwordx4 v[96:99], v160, s[44:47], s68 offen offset:2048 sc1\n\t" \
    "buffer_load_dwordx4 v[100:103], v160, s[44:47], s69 offen offset:2048 sc1\n\t" \
    "buffer_load_dwordx4 v[104:107], v160, s[44:47], s70 offen offset:2048 sc1\n\t" \
    "buffer_load_dwordx4 v[108:111], v160, s[44:47], s71 offen offset:2048 sc1\n\t" \
    "s_sleep 2\n\t" \
    "s_waitcnt vmcnt(21)\n\t" \
    "v_mfma_f32_32x32x16_bf16 v[0:15], v[120:123], v[112:115], v[0:15]\n\t" \
    "s_sleep 2\n\t" \
    "s_waitcnt vmcnt(20)\n\t" \
    "v_mfma_f32_32x32x16_bf16 v[16:31], v[124:127], v[112:115], v[16:31]\n\t" \
    "s_sleep 2\n\t" \
    "v_mfma_f32_32x32x16_bf16 v[32:47], v[120:123], v[116:119], v[32:47]\n\t" \
    "s_sleep 2\n\t" \
    "v_mfma_f32_32x32x16_bf16 v[48:63], v[124:127], v[116:119], v[48:63]\n\t" \
    "buffer_load_dwordx4 v[112:115], v160, s[44:47], s68 offen offset:3072 sc1\n\t" \
    "buffer_load_dwordx4 v[116:119], v160, s[44:47], s69 offen offset:3072 sc1\n\t" \
    "buffer_load_dwordx4 v[120:123], v160, s[44:47], s70 offen offset:3072 sc1\n\t" \
    "buffer_load_dwordx4 v[124:127], v160, s[44:47], s71 offen offset:3072 sc1\n\t" \
    "s_sleep 2\n\t" \
    "s_waitcnt vmcnt(21)\n\t" \
    "v_mfma_f32_32x32x16_bf16 v[0:15], v[136:139], v[128:131], v[0:15]\n\t" \
    "s_sleep 2\n\t" \
    "s_waitcnt vmcnt(20)\n\t" \
    "v_mfma_f32_32x32x16_bf16 v[16:31], v[140:143], v[128:131], v[16:31]\n\t" \
    "s_sleep 2\n\t" \
    "v_mfma_f32_32x32x16_bf16 v[32:47], v[136:139], v[132:135], v[32:47]\n\t" \
    "s_sleep 2\n\t" \
    "v_mfma_f32_32x32x16_bf16 v[48:63], v[140:143], v[132:135], v[48:63]\n\t" \
    "s_sleep 2\n\t" \
    "s_waitcnt vmcnt(17)\n\t" \
    "v_mfma_f32_32x32x16_bf16 v[0:15], v[152:155], v[144:147], v[0:15]\n\t" \
    "s_sleep 2\n\t" \
    "s_waitcnt vmcnt(16)\n\t" \
    "v_mfma_f32_32x32x16_bf16 v[16:31], v[156:159], v[144:147], v[16:31]\n\t" \
    "s_sleep 2\n\t" \
    "v_mfma_f32_32x32x16_bf16 v[32:47], v[152:155], v[148:151], v[32:47]\n\t" \
    "s_sleep 2\n\t" \
    "v_mfma_f32_32x32x16_bf16 v[48:63], v[156:159], v[148:151], v[48:63]\n\t" \
    "s_sleep 2\n\t" \
    "s_waitcnt vmcnt(13)\n\t" \
    "v_mfma_f32_32x32x16_bf16 v[0:15], v[72:75], v[64:67], v[0:15]\n\t" \
    "s_sleep 2\n\t" \
    "s_waitcnt vmcnt(12)\n\t" \
    "v_mfma_f32_32x32x16_bf16 v[16:31], v[76:79], v[64:67], v[16:31]\n\t" \
    "s_sleep 2\n\t" \
    "v_mfma_f32_32x32x16_bf16 v[32:47], v[72:75], v[68:71], v[32:47]\n\t" \
    "s_sleep 2\n\t" \
    "v_mfma_f32_32x32x16_bf16 v[48:63], v[76:79], v[68:71], v[48:63]\n\t" \
    "s_sleep 2\n\t" \
    "s_waitcnt vmcnt(9)\n\t" \
    "v_mfma_f32_32x32x16_bf16 v[0:15], v[88:91], v[80:83], v[0:15]\n\t" \
    "s_sleep 2\n\t" \
    "s_waitcnt vmcnt(8)\n\t" \
    "v_mfma_f32_32x32x16_bf16 v[16:31], v[92:95], v[80:83], v[16:31]\n\t" \
    "s_sleep 2\n\t" \
    "v_mfma_f32_32x32x16_bf16 v[32:47], v[88:91], v[84:87], v[32:47]\n\t" \
    "s_sleep 2\n\t" \
    "v_mfma_f32_32x32x16_bf16 v[48:63], v[92:95], v[84:87], v[48:63]\n\t" \
    "s_sleep 2\n\t" \
    "s_waitcnt vmcnt(5)\n\t" \
    "v_mfma_f32_32x32x16_bf16 v[0:15], v[104:107], v[96:99], v[0:15]\n\t" \
    "s_sleep 2\n\t" \
    "s_waitcnt vmcnt(4)\n\t" \
    "v_mfma_f32_32x32x16_bf16 v[16:31], v[108:111], v[96:99], v[16:31]\n\t" \
    "s_sleep 2\n\t" \
    "v_mfma_f32_32x32x16_bf16 v[32:47], v[104:107], v[100:103], v[32:47]\n\t" \
    "s_sleep 2\n\t" \
    "v_mfma_f32_32x32x16_bf16 v[48:63], v[108:111], v[100:103], v[48:63]\n\t" \
    "s_sleep 2\n\t" \
    "s_waitcnt vmcnt(1)\n\t" \
    "v_mfma_f32_32x32x16_bf16 v[0:15], v[120:123], v[112:115], v[0:15]\n\t" \
    "s_sleep 2\n\t" \
    "s_waitcnt vmcnt(0)\n\t" \
    "v_mfma_f32_32x32x16_bf16 v[16:31], v[124:127], v[112:115], v[16:31]\n\t" \
    "s_sleep 2\n\t" \
    "v_mfma_f32_32x32x16_bf16 v[32:47], v[120:123], v[116:119], v[32:47]\n\t" \
    "s_sleep 2\n\t" \
    "v_mfma_f32_32x32x16_bf16 v[48:63], v[124:127], v[116:119], v[48:63]\n\t" \
    "s_sleep 2\n\t" \
    "s_setprio 0\n\t" \
    "s_nop 15\n\t" \
    ""
DI srd_t mk_srd(const void* p, unsigned bytes) {
  const unsigned long long a = (unsigned long long)p;
  return srd_t{rfl((unsigned)a), rfl((unsigned)(a >> 32) & 0xffffu), rfl(bytes), 0x00020000u};
}
DI void wait_cnt(char* ws, int idx, unsigned target) {
  gu32* cnt = (gu32*)(ws + WS_CNT + idx * 256);
  unsigned spins = 0;
  while (__hip_atomic_load(cnt, __ATOMIC_RELAXED, __HIP_MEMORY_SCOPE_AGENT) < target) {
    __builtin_amdgcn_s_sleep(WC_SLP);
    if (++spins > (1u << 22)) break;
  }
  asm volatile("" ::: "memory");
}
DI void add_cnt(char* ws, int idx) {
  __hip_atomic_fetch_add((gu32*)(ws + WS_CNT + idx * 256), 1u, __ATOMIC_RELAXED, __HIP_MEMORY_SCOPE_AGENT);
}
DI u32x4 ld16s(__amdgpu_buffer_rsrc_t r, unsigned off) { return __builtin_amdgcn_raw_buffer_load_b128(r, off, 0, 16); }
DI void retile_job(char* ws, const WPtrs& P, int batch, int lane, char* lds_, unsigned lds_off) {
  const int id = 448 + batch;
  retile_issue(P, id, lane, (unsigned)(uintptr_t)lds_ + lds_off);
  asm volatile("s_waitcnt vmcnt(0)" ::: "memory");
  retile_store(ws, id, lane, (lds_cptr)lds_ + lds_off);
  asm volatile("s_waitcnt vmcnt(0)" ::: "memory");
  if (lane == 0) add_cnt(ws, id < 672 ? CN_TD : CN_TO);
}
DI void dma16x2s(__amdgpu_buffer_rsrc_t rsrc, unsigned voff, unsigned so, unsigned lds_addr) {
  asm volatile("s_mov_b32 m0, %0\n\ts_nop 0\n\t"
               "buffer_load_dwordx4 %1, %2, %3 offen sc1 lds\n\t"
               "buffer_load_dwordx4 %1, %2, %3 offen offset:1024 sc1 lds"
               :: "s"(lds_addr), "v"(voff), "s"(rsrc), "s"(so) : "memory");
}
#define G2_DEPTH 6
#define V64_159 "v64", "v65", "v66", "v67", "v68", "v69", "v70", "v71", "v72", "v73", "v74", "v75", "v76", "v77", "v78", "v79", "v80", "v81", "v82", "v83", "v84", "v85", "v86", "v87", "v88", "v89", "v90", "v91", "v92", "v93", "v94", "v95", "v96", "v97", "v98", "v99", "v100", "v101", "v102", "v103", "v104", "v105", "v106", "v107", "v108", "v109", "v110", "v111", "v112", "v113", "v114", "v115", "v116", "v117", "v118", "v119", "v120", "v121", "v122", "v123", "v124", "v125", "v126", "v127", "v128", "v129", "v130", "v131", "v132", "v133", "v134", "v135", "v136", "v137", "v138", "v139", "v140", "v141", "v142", "v143", "v144", "v145", "v146", "v147", "v148", "v149", "v150", "v151", "v152", "v153", "v154", "v155", "v156", "v157", "v158", "v159"
DI unsigned poll_issue(__amdgpu_buffer_rsrc_t r, int idx) {
  unsigned v;
  asm volatile("buffer_load_dword %0, off, %1, %2 sc1" : "=v"(v) : "s"(r), "s"(rfl((unsigned)(WS_CNT + idx * 256))) : "memory");
  return v;
}
DI void helper_role(char* lds_, const u16* __restrict__ x, char* __restrict__ ws, const WPtrs& P, int hw, bool has_g1, int wgi, int mb, int nb, int head, int qb, int mb2, int nb2, CSV& cs) {
  int lane = lane_fresh();
  const int dg_w = hw + 8; (void)dg_w;
  const unsigned lds0 = (unsigned)(uintptr_t)lds_;
  const unsigned vl = lane * 16;
  const __amdgpu_buffer_rsrc_t rws = mk_rsrc(ws, 32u * MB);
  const unsigned kvsrc = (unsigned)((hw < 2 ? WS_K : WS_V) + head * 65536 + (hw & 1) * 4096);
  const unsigned kvdst = lds0 + (hw >> 1) * 8192 + (hw & 1) * 4096;
  auto issue_kv = [&](int c) { dma16x4s(rws, vl, kvsrc + c * 8192, kvdst + c * 16384); };
  auto issue_q = [&]() { dma16x2s(rws, vl, (unsigned)(WS_Q + ((head * 64 + qb * 8 + hw * 2) * 64) * 16), lds0 + L_Q + hw * 2048); };
  auto consts = [&]() {
#pragma unroll
    for (int i = 0; i < 2; ++i) {
      const int t = (hw * 2 + i) * 64 + lane;
      const unsigned v = ((t & 31) == 0 || (t & 31) == 4) ? 0x3f803f80u : 0u;
      *(u32x4*)(lds_ + L_CONST + t * 16) = u32x4{v, v, v, v};
    }
  };
  const int cq = CN_QKV + head / 6, ck = CN_QKV + (16 + (head >> 1)) / 3, cv = CN_QKV + (32 + (head >> 1)) / 3;
  auto wait_qkv = [&]() { wait_cnt(ws, cq, 16); wait_cnt(ws, ck, 16); wait_cnt(ws, cv, 16); };
  if (has_g1) {
    const int rot = ROT_OF(G1_ROTK);
    const GLd G = gld_init(lds_, x, P, mb, nb, hw, rot);
    auto gx = [&](int st, int slot) { issue_gx(G, st, slot); };
    auto gw = [&](int st, int slot) { issue_gw(G, st, 0, slot); };
    auto gv = [&](int st, int q, int slot) { issue_gw2(G, st, q, slot); };
    __builtin_amdgcn_s_setprio(HP_PRIO);
#define HW(N) wait_bar<N>()
    issue_g(G, 1, L_GB);
    HW(16); issue_g(G, 2, L_GC);
    HW(24);
    HW(16); gw(3, L_GA);
    HW(16); gx(3, L_GA); gv(3, 0, L_GA);
    DBG(hw + 8, 2);
    HW(14); gw(4, L_GB); gv(3, 1, L_GA);
    HW(16); gx(4, L_GB); gv(4, 0, L_GB);
    HW(14); gw(5, L_GC); gv(4, 1, L_GB);
    DBG(hw + 8, 3);
    HW(12); gx(5, L_GC); gv(5, 0, L_GC);
    HW(14); gw(6, L_GA); gv(5, 1, L_GC);
    HW(12); gx(6, L_GA); gv(6, 0, L_GA);
    HW(14); gw(7, L_GB); gv(6, 1, L_GA);
    DBG(hw + 8, 4);
    HW(12); gx(7, L_GB); gv(7, 0, L_GB);
    HW(14); gv(7, 1, L_GB);
    DBG(hw + 8, 5);
    unsigned p0 = poll_issue(rws, cq), p1 = poll_issue(rws, ck), p2 = poll_issue(rws, cv);
    HW(11);
    TS(8);
    bool ready = false;
    int nkv = 0;
    auto check = [&]() {
      asm volatile("s_waitcnt vmcnt(0)" : "+v"(p0), "+v"(p1), "+v"(p2) :: "memory");
      ready = rfl(p0) >= 16u && rfl(p1) >= 16u && rfl(p2) >= 16u;
      snop4();
      if (ready) { issue_kv(0); issue_kv(1); issue_kv(2); issue_kv(3); }
      else { p0 = poll_issue(rws, cq); p1 = poll_issue(rws, ck); p2 = poll_issue(rws, cv); }
    };
    check();
    HB(63);
    const bool r2 = ready;
    HB(63);
    consts();
    if (!ready) check();
    if (ready) { snop4(); issue_q(); issue_kv(7); }
    HB(63);
    __builtin_amdgcn_s_setprio(0);
    bar_only();
    TS(9);
    (void)r2; (void)nkv;
    if (!ready) {
      asm volatile("s_waitcnt vmcnt(0)" : "+v"(p0), "+v"(p1), "+v"(p2) :: "memory");
      wait_qkv();
      snop4();
      issue_kv(0); issue_kv(1); issue_kv(2); issue_kv(3); issue_q(); issue_kv(7);
    }
    snop4();
    issue_kv(4); issue_kv(5); issue_kv(6);
  } else {
    bar_plain();
    consts();
    if (hw == 0) retile_job(ws, P, wgi * 9 + 8, lane, lds_, L_EX);
    wait_qkv();
    snop4();
    issue_kv(0); issue_kv(1); issue_kv(2); issue_kv(3); issue_q(); issue_kv(7);
    bar_only();
    snop4();
    issue_kv(4); issue_kv(5); issue_kv(6);
  }
  DBG(hw + 8, 6);
  wait_bar<16>();
  DBG(hw + 8, 7);
  TS(10);
  {
    unsigned spins = 0;
    while (true) {
      unsigned p0 = poll_issue(rws, CN_TD), p1 = poll_issue(rws, CN_TO), p2 = poll_issue(rws, CN_PZ);
      asm volatile("s_waitcnt vmcnt(0)" : "+v"(p0), "+v"(p1), "+v"(p2) :: "memory");
      if ((rfl(p0) >= 224u && rfl(p1) >= 64u && rfl(p2) >= 256u) || ++spins > (1u << 22)) break;
      __builtin_amdgcn_s_sleep(2);
    }
  }
  DBG(hw + 8, 8);
  wait_bar<0>();
  DBG(hw + 8, 9);
  TS(11);
  if (hw == 0) {
    asm volatile("s_mov_b64 exec, 1\n\tv_mov_b32 v0, 1\n\tbuffer_atomic_add v0, off, %0, %1 sc0\n\ts_waitcnt vmcnt(0)\n\t"
                 "v_cmp_eq_u32 vcc, 0xff, v0\n\ts_cbranch_vccz L_nofin_%=\n\t"
                 "v_mov_b32 v0, 0\n\tv_mov_b32 v1, 0\n\tbuffer_store_dwordx2 v[0:1], off, %0, %2 sc1\n\t"
                 "L_nofin_%=:\n\ts_mov_b64 exec, -1"
                 :: "s"(mk_srd(ws, 32u * MB)), "s"((unsigned)(WS_CNT + CN_FIN * 256)), "s"((unsigned)(WS_CNT + CN_FLAG * 256)) : "v0", "v1", "vcc", "memory");
  }
  const unsigned hsrc = (unsigned)(WS_H + ((mb2 * 2) * KS_F + hw * 28) * 1024), wsrc = (unsigned)(WS_TD + ((nb2 * 2) * KS_F + hw * 28) * 1024);
  wait_cnt(ws, CN_H + (mb2 >> 1) * 4 + hw, 28);
  TS(12);
  f32x16 acc[4];
  asm volatile(G2_ASM
               : "=&{v[0:15]}"(acc[0]), "=&{v[16:31]}"(acc[1]), "=&{v[32:47]}"(acc[2]), "=&{v[48:63]}"(acc[3])
               : "{v160}"((unsigned)lane_fresh() * 16u), "{s[44:47]}"(mk_srd(ws, 32u * MB)), "{s48}"(rfl(hsrc)), "{s49}"(rfl(wsrc))
               : V64_159, "s68", "s69", "s70", "s71", "scc", "memory");
  lane = lane_fresh();
  TS(13);
  {
    const LDSP(unsigned)* pw = (const LDSP(unsigned)*)(lds_ + L_PROG + (lane & 7) * 512);
    unsigned spins = 0;
    while (true) {
      const unsigned v = *(volatile const LDSP(unsigned)*)pw;
      if (__builtin_amdgcn_ballot_w64(v < lds0 + 4u * 16384u) == 0ull || ++spins > (1u << 22)) break;
      __builtin_amdgcn_s_sleep(2);
    }
    f32x4* dst = (f32x4*)(lds_ + L_PART + hw * 16384);
#pragma unroll
    for (int q = 0; q < 4; ++q)
#pragma unroll
      for (int g = 0; g < 4; ++g) dst[(q * 4 + g) * 64 + lane] = f32x4{acc[q][4 * g], acc[q][4 * g + 1], acc[q][4 * g + 2], acc[q][4 * g + 3]};
  }
  bar_only();
}

DI void compute_role(char* lds_, char* __restrict__ ws, const u16* __restrict__ x, u16* __restrict__ out, const WPtrs& P,
                     int w, bool has_g1, int bi, int xc, int mb, int nbq, int nb, int head, int qb, int mb2, int nb2, G1C& gc) {
  const unsigned lds0 = (unsigned)(uintptr_t)lds_;
  int lane = lane_fresh();
  unsigned vl = lane * 16;
  const int b = blockIdx.x;
  const srd_t rws = mk_srd(ws, 32u * MB);
  if (has_g1) {
    const srd_t PA = {(unsigned)(WS_CNT + CN_PZ * 256), rfl((unsigned)(WS_CNT + (CN_QKV + nbq) * 256)), rfl(w == 0 ? 1u : 0u),
                      rfl((unsigned)(WS_H + ((mb * 4) * KS_F + nb * 8 + w) * 1024))};
    f32x16 a0, a1, a2, a3;
    const int h = lane >> 5, i16 = lane & 15, q4 = i16 >> 2, p4 = i16 & 3, blk = (lane >> 4) & 1, sw = (lane >> 1) & 7;
    u32x4 xa;
#pragma unroll
    for (int ks = 0; ks < 4; ++ks) xa[ks] = lds0 + L_GA + (lane & 31) * 128 + (((2 * ks + h) ^ sw) * 16);
    unsigned wa = lds0 + L_GA + 16384 + 4096 * blk + (8 * h + q4) * 256 + p4 * 8 + ((w ^ (2 * q4 + blk)) & 7) * 32;
    asm volatile(G1_ASM
                 : "=&{v[0:15]}"(a0), "=&{v[16:31]}"(a1), "=&{v[32:47]}"(a2), "=&{v[48:63]}"(a3), "+{v[104:107]}"(xa), "+{v108}"(wa)
                 : "{v128}"(vl), "{s[44:47]}"(rws), "{s[52:55]}"(PA), "{s56}"(rfl(lds0 + L_EX + w * 512)),
                   "{v[80:83]}"(gc.k3), "{v[84:87]}"(gc.k1), "{v[88:91]}"(gc.q), "{v[92:95]}"(gc.mean)
                 : "v64", "v65", "v66", "v67", "v68", "v69", "v70", "v71", "v72", "v73", "v74", "v75", "v76", "v77", "v78", "v79",
                   "v96", "v97", "v98", "v99", "v100", "v101", "v102", "v103", "v109", "v110", "v111",
                   "v112", "v113", "v114", "v115", "v116", "v117", "v118", "v119", "v120", "v121", "v122", "v123", "v124", "v125", "v126", "v127",
                   "v129", "v130", "v131", "v132", "v133", "v134", "v135", "v139", "v140", "v141", "v142", "v143", "v144", "v145", "v146", "v147", "v148", "v149", "v150", "v151", "v152", "v153", "v154", "v155", "v156", "v157", "v158", "v159", "s68", "s69", "s70", "s71", "s72", "s73", "s74", "s78", "s79", "vcc", "scc", "memory");
  } else {
    asm volatile("s_waitcnt vmcnt(0)\n\ts_barrier" ::: "memory");
    if (w == 0 && lane == 0) { add_cnt(ws, CN_QKV + nbq); add_cnt(ws, CN_PZ); }
    retile_job(ws, P, ((bi - 28) * 8 + xc) * 9 + w, lane, lds_, L_RT + w * 8192);
    bar_only();
  }
  DBG(w, 2);
  lane = lane_fresh(); vl = lane * 16;
  constexpr int SLOT = 16384;
  const int r32 = lane & 31;
  const int h = lane >> 5;
  const unsigned isdata = r32 < 16 ? 1u : 0u;
  const unsigned coff = (r32 == 16) ? 0u : (r32 == 20) ? 64u : (r32 > 20 && r32 < 28) ? 80u : 16u;
  const unsigned vbase = lds0 + (isdata ? (unsigned)(8192 + h * 256 + r32 * 16) : (unsigned)L_CONST + coff);
  const unsigned kb = lds0 + vl;
  lds_cptr L = (lds_cptr)lds_;
  f32x16 O;
  bf16x8 qf;
  { const int dg_w = w; (void)dg_w; TS(11); }
  bar_plain();
  DBG(w, 3);
  { const int dg_w = w; (void)dg_w; TS(12); }
  const srd_t PB = {__float_as_uint(ATT_LIM), rfl((unsigned)(WS_CNT + (CN_H + mb * 4 + (nb * 8 + w) / 28) * 256)), rfl(has_g1 ? 1u : 0u),
                    rfl((unsigned)(WS_TO + (((nb2 * 2 + ((w >> 1) & 1)) * 32 + (w & 1) * 16) * 64) * 16))};
  u32x16 wf0, wf1, wf2;
  unsigned oprb;
  const unsigned oro = rfl((unsigned)(WS_O + (((mb2 * 2 + (w >> 2)) * 32 + (w & 1) * 16) * 64) * 16));
  {
    unsigned fail;
    asm volatile(ATT_ASM
                 : "=&{v[0:15]}"(O), "=&{s76}"(fail), "=&{v[88:91]}"(qf),
                   "=&{v[120:135]}"(wf0), "=&{v[136:151]}"(wf1), "=&{v[152:167]}"(wf2), "=&{v115}"(oprb)
                 : "{v106}"(vbase), "{v107}"(isdata), "{v108}"(vl), "{v109}"(kb), "{v114}"(lds0 + L_PROG + w * 512 + lane * 4), "{v112}"(lds0 + L_Q + w * 1024 + vl),
                   "{s[44:47]}"(rws), "{s[56:59]}"(PB), "{s71}"(oro), "{s60}"(__float_as_uint(-ATT_BIAS))
                 : "v16", "v17", "v18", "v19", "v20", "v21", "v22", "v23", "v24", "v25", "v26", "v27", "v28", "v29", "v30", "v31",
                   "v32", "v33", "v34", "v35", "v36", "v37", "v38", "v39", "v40", "v41", "v42", "v43", "v44", "v45", "v46", "v47",
                   "v48", "v49", "v50", "v51", "v52", "v53", "v54", "v55", "v56", "v57", "v58", "v59", "v60", "v61", "v62", "v63",
                   "v64", "v65", "v66", "v67", "v68", "v69", "v70", "v71", "v72", "v73", "v74", "v75", "v76", "v77", "v78", "v79",
                   "v80", "v81", "v82", "v83", "v84", "v85", "v86", "v87", "v92", "v93", "v94", "v95", "v96", "v97", "v98", "v99",
                   "v101", "v103", "v105", "v110", "v111",
                   "s68", "s69", "s70", "s78", "s79", "vcc", "scc", "memory");
    if (__builtin_expect(fail != 0, 0)) {
      asm volatile("s_waitcnt vmcnt(0)" : "+{v[120:135]}"(wf0), "+{v[136:151]}"(wf1), "+{v[152:167]}"(wf2), "+{v115}"(oprb) :: "memory");
      const __amdgpu_buffer_rsrc_t rb = mk_rsrc(ws, 32u * MB);
      const unsigned kp = (unsigned)(WS_K + head * 65536) + vl;
      const unsigned vp = (unsigned)(WS_V + head * 65536) + (unsigned)(h * 256 + (r32 & 15) * 16);
      const unsigned one = (r32 == 16 || r32 == 20) ? 0x3f803f80u : 0u;
      float mx = -INFINITY;
#pragma unroll
      for (int r = 0; r < 16; ++r) O[r] = 0.f;
#pragma unroll 1
      for (int t = 0; t < 64; ++t) {
        const f32x16 s = MFMA(__builtin_bit_cast(bf16x8, ld16s(rb, kp + t * 1024)), qf, O);
#pragma unroll
        for (int r = 0; r < 16; ++r) mx = fmaxf(mx, s[r]);
      }
      mx = fmaxf(mx, __shfl_xor(mx, 32));
      f32x16 C2;
#pragma unroll
      for (int r = 0; r < 16; ++r) C2[r] = -(mx + ATT_BIAS);
#pragma unroll 1
      for (int t = 0; t < 64; ++t) {
        const f32x16 s = MFMA(__builtin_bit_cast(bf16x8, ld16s(rb, kp + t * 1024)), qf, C2);
        u32x4 p0, p1;
#pragma unroll
        for (int j = 0; j < 4; ++j) {
          p0[j] = cvtpk(__builtin_amdgcn_exp2f(s[2 * j]), __builtin_amdgcn_exp2f(s[2 * j + 1]));
          p1[j] = cvtpk(__builtin_amdgcn_exp2f(s[8 + 2 * j]), __builtin_amdgcn_exp2f(s[8 + 2 * j + 1]));
        }
        u32x4 v0 = ld16s(rb, vp + t * 1024), v1 = ld16s(rb, vp + t * 1024 + 512);
#pragma unroll
        for (int j = 0; j < 4; ++j) { v0[j] = isdata ? v0[j] : one; v1[j] = isdata ? v1[j] : one; }
        O = MFMA(__builtin_bit_cast(bf16x8, v0), __builtin_bit_cast(bf16x8, p0), O);
        O = MFMA(__builtin_bit_cast(bf16x8, v1), __builtin_bit_cast(bf16x8, p1), O);
      }
    }
  }
  DBG(w, 4);
  {
    lane = lane_fresh(); vl = lane * 16;
    const int r32 = lane & 31, h = lane >> 5;
    const int tile = w >> 1, mt = tile >> 1, nt = tile & 1, kh = w & 1;
    const int token = mb2 * 64 + mt * 32 + r32, col0 = nb2 * 64 + nt * 32 + 16 * kh + 4 * h;
    const srd_t PD = {rfl((unsigned)(WS_O + (((qb * 8 + w) * 32 + head) * 64) * 16)),
                      rfl((unsigned)(WS_O + (((mb2 * 2 + mt) * 32 + kh * 16) * 64) * 16)),
                      rfl((unsigned)(WS_TO + (((nb2 * 2 + nt) * 32 + kh * 16) * 64) * 16)),
                      0u};
    const srd_t PE = {rfl(lds0 + L_PART + w * 2048), rfl((unsigned)kh), rfl(lds0 + L_EX + w * 2048), rfl(lds0 + L_EX + (w ^ 1) * 2048)};
    const srd_t rxx = mk_srd(x, S * D * 2), rout = mk_srd(out, S * D * 2);
    asm volatile(TAIL_ASM
                 : "+{v[0:15]}"(O), "+{v[120:135]}"(wf0), "+{v[136:151]}"(wf1), "+{v[152:167]}"(wf2), "+{v115}"(oprb)
                 : "{v108}"(vl), "{v109}"((unsigned)((token * D + col0) * 2)), "{s[44:47]}"(rws),
                   "{s[80:83]}"(PD), "{s[84:87]}"(PE), "{s[88:91]}"(rxx), "{s[92:95]}"(rout)
                 : "v16", "v17", "v18", "v19", "v20", "v21", "v22", "v23", "v24", "v25", "v26", "v27", "v28", "v29", "v30", "v31", "v32", "v33", "v34", "v35", "v36", "v37", "v38", "v39", "v40", "v41", "v42", "v43", "v44", "v45", "v46", "v47", "v48", "v49", "v50", "v51", "v52", "v53", "v54", "v55", "v56", "v57", "v58", "v59", "v60", "v61", "v62", "v63", "v64", "v65", "v66", "v67", "v68", "v69", "v70", "v71", "v72", "v73", "v74", "v75", "v76", "v77", "v78", "v79", "v80", "v81", "v82", "v83", "v84", "v85", "v86", "v87", "v88", "v89", "v90", "v91", "v92", "v93", "v94", "v95", "v96", "v97", "v98", "v99", "v100", "v101", "v102", "v103", "v104", "v105", "v106", "v107", "v110", "v111", "v112", "v113", "v114", "v116", "v117", "v118", "v119", "s68", "s69", "s70", "s71", "s72", "s73", "s74", "s78", "s79", "vcc", "scc", "memory");
  }
}

__global__ __launch_bounds__(768) void k_all(const u16* __restrict__ x, const u16* __restrict__ W,
                                             const u16* __restrict__ wg, const u16* __restrict__ wu,
                                             const u16* __restrict__ wd, const u16* __restrict__ wo,
                                             char* __restrict__ ws, u16* __restrict__ out) {
  __shared__ __attribute__((aligned(16))) char lds_[L_TOTAL];
  const WPtrs P = {wg, wu, wd, wo};
  asm volatile("kall_code_begin:");
  unsigned cpf = 0;
  {
    const unsigned li = (threadIdx.x - 448u) * 32u + (blockIdx.x >> 3);
    asm volatile("v_cmp_gt_u32 vcc, 64, %1\n\t"
                 "s_and_saveexec_b64 s[76:77], vcc\n\ts_cbranch_execz L_npf_%=\n\t"
                 "s_getpc_b64 s[72:73]\n\ts_add_u32 s72, s72, kall_code_begin@rel32@lo+4\n\ts_addc_u32 s73, s73, kall_code_begin@rel32@hi+12\n\t"
                 "s_getpc_b64 s[74:75]\n\ts_add_u32 s74, s74, kall_code_end@rel32@lo+4\n\t"
                 "s_and_b32 s72, s72, 0xffffff80\n\ts_sub_u32 s74, s74, s72\n\ts_mov_b32 s75, 0x00020000\n\ts_and_b32 s73, s73, 0xffff\n\t"
                 "v_lshlrev_b32 %0, 7, %2\n\ts_mov_b32 m0, %3\n\ts_nop 0\n\t"
                 "buffer_load_dword %0, s[72:75], 0 offen lds\n\t"
                 "L_npf_%=:\n\ts_mov_b64 exec, s[76:77]"
                 : "=&v"(cpf) : "v"(threadIdx.x - 448u), "v"(li), "s"((unsigned)(uintptr_t)lds_ + 114688u) : "s72", "s73", "s74", "s75", "s76", "s77", "vcc", "scc", "memory");
  }
  DG_BEGIN();
  DBG(threadIdx.x >> 6, 0);
  const int b = blockIdx.x, xc = b & 7, bi = b >> 3;
  const int w = rfl(threadIdx.x >> 6);
  const int dg_w = w; (void)dg_w;
  const int mbq = (xc & 3) * 4 + (bi & 3), nbq =
    (xc >> 2) * 8 + (bi >> 2);
  const bool has_g1 = bi < 28;
  const int mb = mbq, nb = (xc >> 2) * 7 + (bi >> 2);
  const int head = xc * 4 + (bi & 3), qb = bi >> 2;
  const int mb2 = xc * 4 + (bi & 3), nb2 = bi >> 2;
  if (w >= 8) {
    CSV cs;
    k1_loader(lds_, x, W, P, ws, mbq, nbq, has_g1, mb, nb, w - 8, cs, (bi - 28) * 8 + xc);
    DG_MARK(1);
    DBG(w, 1);
    helper_role(lds_, x, ws, P, w - 8, has_g1, (bi - 28) * 8 + xc, mb, nb, head, qb, mb2, nb2, cs);
  } else {
    G1C gc;
    const unsigned oslot = (unsigned)(WS_O + (((qb * 8 + w) * 32 + head) * 64) * 16);
    if (nbq < 10) k1_body<0>(lds_, x, W, ws, mbq, nbq, gc, w, oslot);
    else if (nbq == 10) k1_body<4>(lds_, x, W, ws, mbq, nbq, gc, w, oslot);
    else k1_body<7>(lds_, x, W, ws, mbq, nbq, gc, w, oslot);
    DG_MARK(1);
    DBG(w, 1);
    compute_role(lds_, ws, x, out, P, w, has_g1, bi, xc, mb, nbq, nb, head, qb, mb2, nb2, gc);
  }
  DG_END(0);
  DBG(w, 5);
  asm volatile("kall_code_end:");
}

extern "C" void kernel_launch(void* const* d_in, const int* in_sizes, int n_in, void* d_out, int out_size,
                              void* d_ws, size_t ws_size, hipStream_t stream) {
  k_all<<<256, 768, 0, stream>>>((const u16*)d_in[0], (const u16*)d_in[1], (const u16*)d_in[4], (const u16*)d_in[5],
                                 (const u16*)d_in[6], (const u16*)d_in[2], (char*)d_ws, (u16*)d_out);
}
```
